```python
import torch

def _softmax(x, axis=-1):
    return torch.softmax(x, dim=axis)
def _gelu(x):
    return torch.nn.functional.gelu(x, approximate="tanh")
def _silu(x):
    return torch.nn.functional.silu(x)
def _sigmoid(x):
    return torch.sigmoid(x)
def _layernorm(x, axis=-1, eps=1e-5):
    m = x.mean(dim=axis, keepdim=True)
    v = x.var(dim=axis, keepdim=True, unbiased=False)
    return (x - m) / torch.sqrt(v + eps)
def _scatter_add(tgt, idx, src, axis=0):
    return tgt.clone().index_add_(axis, idx, src)
def _segment_sum(src, seg, n):
    out = torch.zeros((n,) + src.shape[1:], dtype=src.dtype)
    return out.index_add_(0, seg, src)
def _pad(x, pw):
    flat = tuple(v for pair in reversed(pw) for v in pair)
    return torch.nn.functional.pad(x, flat)

kernel_name = 'recurrent_scan__gqa_block_cached__dense_activation_squared_relu__decode'

def setup_inputs():

    _w = (lambda a: (lambda t: t.to(torch.bfloat16) if t.is_floating_point() else t)(torch.as_tensor(a)))
    def _A_setup():
        B, Sq, D = 4, 1, 512
        Hq, Hkv, Sk, Dh = 32, 8, 16384, 128
        assert Hq % Hkv == 0, (Hq, Hkv)
        return {
            "x": _w((lambda *s: torch.randn(*s, dtype=torch.bfloat16) * 0.05)(B, Sq, D)),
            "k_cache": _w((lambda *s: torch.randn(*s, dtype=torch.bfloat16) * 1.6)(B, Hkv, Sk, Dh)),
            "v_cache": _w((lambda *s: torch.randn(*s, dtype=torch.bfloat16) * 0.2)(B, Hkv, Sk, Dh)),
            "wq": _w((lambda *s: torch.randn(*s, dtype=torch.bfloat16) * 1.6)(D, Hq * Dh) * (D ** -0.5)),
            "wo": _w((lambda *s: torch.randn(*s, dtype=torch.bfloat16) * 0.2)(Hq * Dh, D) * ((Hq * Dh) ** -0.5)),
        }
    _A_in = _A_setup()

    def _B_setup():
        B, S, D, F = 4, 1, 512, 1792
        return {
            "x": _w((lambda *s: torch.randn(*s, dtype=torch.bfloat16) * 0.05)(B, S, D)),
            "w1": _w((lambda *s: torch.randn(*s, dtype=torch.bfloat16) * 0.1)(D, F)),
            "b1": _w((lambda *s: torch.randn(*s, dtype=torch.bfloat16) * 0.1)(F)),
            "w2": _w((lambda *s: torch.randn(*s, dtype=torch.bfloat16) * 0.1)(F, D)),
            "b2": _w((lambda *s: torch.randn(*s, dtype=torch.bfloat16) * 0.1)(D)),
        }
    _B_in = _B_setup()

    return {
        "A_x": _A_in["x"],
        "A_k_cache": _A_in["k_cache"],
        "A_v_cache": _A_in["v_cache"],
        "A_wq": _A_in["wq"],
        "A_wo": _A_in["wo"],
        "B_x": _B_in["x"],
        "B_w1": _B_in["w1"],
        "B_b1": _B_in["b1"],
        "B_w2": _B_in["w2"],
        "B_b2": _B_in["b2"],
    }

def reference(A_x, A_k_cache, A_v_cache, A_wq, A_wo, B_x, B_w1, B_b1, B_w2, B_b2):
    def _A_ref(x, k_cache, v_cache, wq, wo):
        B, Sq, D = x.shape
        Hq, Dh = 32, 128
        Hkv = k_cache.shape[1]
        rep = Hq // Hkv
        q = (lambda x, *d: x.permute(*d))((x @ wq).reshape(B, Sq, Hq, Dh), 0, 2, 1, 3)
        k = (lambda x, n, axis: torch.repeat_interleave(x, n, dim=axis))(k_cache, rep, axis=1)
        v = (lambda x, n, axis: torch.repeat_interleave(x, n, dim=axis))(v_cache, rep, axis=1)
        scores = torch.einsum("bhqd,bhkd->bhqk", q, k) * (Dh ** -0.5)
        p = _softmax(scores, axis=-1)
        o = torch.einsum("bhqk,bhkd->bhqd", p, v)
        return (lambda x, *d: x.permute(*d))(o, 0, 2, 1, 3).reshape(B, Sq, Hq * Dh) @ wo

    def _B_ref(x, w1, b1, w2, b2):
        h = torch.einsum("bsd,df->bsf", x, w1) + b1
        zero = torch.zeros_like(h)
        r = torch.where(h > zero, h, zero)
        h = r * r
        out = torch.einsum("bsf,fd->bsd", h, w2) + b2
        return out

    x = A_x
    xn = _layernorm(x, axis=-1)
    s = _A_ref(xn, A_k_cache, A_v_cache, A_wq, A_wo)
    g = _silu(_B_ref(xn, B_w1, B_b1, B_w2, B_b2))
    return x + s * g
```

```cpp
#include <hip/hip_runtime.h>
#include <hip/hip_bf16.h>
#include <stdint.h>

typedef unsigned int u32;
typedef u32 u32x4 __attribute__((ext_vector_type(4)));
typedef u32 u32x2 __attribute__((ext_vector_type(2)));
typedef float f32x4 __attribute__((ext_vector_type(4)));
typedef float f32x2 __attribute__((ext_vector_type(2)));
typedef __bf16 bf16x8 __attribute__((ext_vector_type(8)));
typedef __bf16 bf16x2 __attribute__((ext_vector_type(2)));
#define GAS __attribute__((address_space(1)))
typedef GAS const u32x4 gcu32x4;
typedef GAS const char gcchar;

constexpr int NB = 4, DM = 512, HKV = 8, SK = 16384, DH = 128, FF = 1792;
constexpr int NMLP = 28;
constexpr u32 WS_EPOCH = 0;
constexpr u32 WS_Q  = 0x1000;
constexpr u32 WS_PL = 0x20000;
constexpr u32 WS_MP = 0x30000;
constexpr u32 WS_PO = 0x100000;
constexpr u32 WS_SP = 0x200000;
constexpr u32 WS_END = 0x300000;
constexpr u32 SPIN_CAP = 1u << 17;
constexpr int L_Q   = 0;
constexpr int L_T   = 1024;
constexpr int L_XN  = 2048;
constexpr int L_HP  = 6144;
constexpr int L_H   = 14336;
constexpr int L_ML  = 14848;
constexpr int L_O   = 15104;
constexpr int L_MO  = 16384;
constexpr int L_END = 16384 + 4 * 4 * 144 * 4;
constexpr int LDS_BYTES = 84 * 1024;

struct Params {
    const void* x; const void* k; const void* v; const void* wq; const void* wo;
    const void* w1; const void* b1; const void* w2; const void* b2;
    void* out; void* ws;
};

__device__ __forceinline__ u32x4 ldnt(gcchar* p) { return __builtin_nontemporal_load((gcu32x4*)p); }
__device__ __forceinline__ float bflo(u32 w) { return __uint_as_float(w << 16); }
__device__ __forceinline__ float bfhi(u32 w) { return __uint_as_float(w & 0xffff0000u); }
__device__ __forceinline__ u32 pack2(float lo, float hi) {
    f32x2 v = {lo, hi};
    return __builtin_bit_cast(u32, __builtin_convertvector(v, bf16x2));
}
__device__ __forceinline__ float bfr(float x) { return bflo(pack2(x, 0.f)); }
__device__ __forceinline__ f32x4 mfma(u32x4 a, u32x4 b, f32x4 c) {
    return __builtin_amdgcn_mfma_f32_16x16x32_bf16(__builtin_bit_cast(bf16x8, a), __builtin_bit_cast(bf16x8, b), c, 0, 0, 0);
}
__device__ __forceinline__ float dot2(u32 a, u32 b, float c) {
    return __builtin_amdgcn_fdot2_f32_bf16(__builtin_bit_cast(bf16x2, a), __builtin_bit_cast(bf16x2, b), c, false);
}
template <int CTRL> __device__ __forceinline__ u32 dppu(u32 x) { return (u32)__builtin_amdgcn_mov_dpp((int)x, CTRL, 0xf, 0xf, true); }
template <int CTRL> __device__ __forceinline__ float dppf(float x) { return __uint_as_float(dppu<CTRL>(__float_as_uint(x))); }
constexpr int XOR1 = 0xB1, XOR2 = 0x4E, HMIR = 0x141, ROR4 = 0x124, ROR8 = 0x128, EVEN = 0xA0, ODD = 0xF5;
__device__ __forceinline__ float xrow_max(float x) {
    auto s = __builtin_amdgcn_permlane16_swap(__float_as_uint(x), __float_as_uint(x), false, false);
    x = fmaxf(__uint_as_float(s[0]), __uint_as_float(s[1]));
    auto t = __builtin_amdgcn_permlane32_swap(__float_as_uint(x), __float_as_uint(x), false, false);
    return fmaxf(__uint_as_float(t[0]), __uint_as_float(t[1]));
}
__device__ __forceinline__ float xrow_sum(float x) {
    auto s = __builtin_amdgcn_permlane16_swap(__float_as_uint(x), __float_as_uint(x), false, false);
    x = __uint_as_float(s[0]) + __uint_as_float(s[1]);
    auto t = __builtin_amdgcn_permlane32_swap(__float_as_uint(x), __float_as_uint(x), false, false);
    return __uint_as_float(t[0]) + __uint_as_float(t[1]);
}
__device__ __forceinline__ float x32_max(float x) {
    auto t = __builtin_amdgcn_permlane32_swap(__float_as_uint(x), __float_as_uint(x), false, false);
    return fmaxf(__uint_as_float(t[0]), __uint_as_float(t[1]));
}
__device__ __forceinline__ float x32_sum(float x) {
    auto t = __builtin_amdgcn_permlane32_swap(__float_as_uint(x), __float_as_uint(x), false, false);
    return __uint_as_float(t[0]) + __uint_as_float(t[1]);
}
__device__ __forceinline__ float wave_sum(float x) {
    x += dppf<XOR1>(x); x += dppf<XOR2>(x); x += dppf<ROR4>(x); x += dppf<ROR8>(x);
    return xrow_sum(x);
}
__device__ __forceinline__ void wg_barrier() { asm volatile("s_waitcnt lgkmcnt(0)\n\ts_barrier" ::: "memory"); }

__device__ __forceinline__ u32x4 mk_unit(u32 v0, u32 v1, u32 T) { u32x4 u = {v0, T ^ v1, v1, T ^ v0}; return u; }
__device__ __forceinline__ bool unit_ok(u32x4 u, u32 T) { return ((u.y ^ u.z) == T) & ((u.w ^ u.x) == T); }
__device__ __forceinline__ void st_unit(__amdgpu_buffer_rsrc_t rs, u32 off, u32x4 u) { __builtin_amdgcn_raw_buffer_store_b128(u, rs, off, 0, 16); }
__device__ __forceinline__ u32x4 ld_unit(__amdgpu_buffer_rsrc_t rs, u32 off) { return __builtin_amdgcn_raw_buffer_load_b128(rs, off, 0, 16); }

template <int DD> __device__ __forceinline__ u32x4 tfrag(const u32x4* v) {
    constexpr u32 sel = (DD & 1) ? 0x07060302u : 0x05040100u;
    u32x4 a;
    a.x = __builtin_amdgcn_perm(v[1][DD >> 1], v[0][DD >> 1], sel);
    a.y = __builtin_amdgcn_perm(v[3][DD >> 1], v[2][DD >> 1], sel);
    a.z = __builtin_amdgcn_perm(v[5][DD >> 1], v[4][DD >> 1], sel);
    a.w = __builtin_amdgcn_perm(v[7][DD >> 1], v[6][DD >> 1], sel);
    return a;
}
__device__ __forceinline__ void pv_tile(const u32x4* v, u32x4 p, f32x4 (&acc)[8]) {
    acc[0] = mfma(tfrag<0>(v), p, acc[0]); acc[1] = mfma(tfrag<1>(v), p, acc[1]);
    acc[2] = mfma(tfrag<2>(v), p, acc[2]); acc[3] = mfma(tfrag<3>(v), p, acc[3]);
    acc[4] = mfma(tfrag<4>(v), p, acc[4]); acc[5] = mfma(tfrag<5>(v), p, acc[5]);
    acc[6] = mfma(tfrag<6>(v), p, acc[6]); acc[7] = mfma(tfrag<7>(v), p, acc[7]);
}
template <int NSLOT>
__device__ __forceinline__ void publish_cols(__amdgpu_buffer_rsrc_t rs, u32 base, const f32x4 (&acc)[8], int slot, int ng0, int lane, u32 T) {
    const int g = lane >> 4, b = (lane >> 2) & 3, rho = lane & 3;
    const u32 off0 = base + (u32)(((b * 64 + ng0 + 4 * g) * NSLOT + slot) * 64 + rho * 16);
#pragma unroll
    for (int r = 0; r < 4; ++r) {
        float a0 = (rho & 2) ? acc[4][r] : acc[0][r], a1 = (rho & 2) ? acc[6][r] : acc[2][r];
        float c0 = (rho & 2) ? acc[5][r] : acc[1][r], c1 = (rho & 2) ? acc[7][r] : acc[3][r];
        float v0 = (rho & 1) ? a1 : a0, v1 = (rho & 1) ? c1 : c0;
        st_unit(rs, off0 + (u32)(r * NSLOT * 64), mk_unit(__float_as_uint(v0), __float_as_uint(v1), T));
    }
}

#define STAMP(i) do { } while (0)

struct Ring { u32x4 r[16]; };

__device__ __forceinline__ void issue_k(Ring& R, gcchar* kb, u32 koff) {
#pragma unroll
    for (int i = 0; i < 8; ++i) R.r[i] = ldnt(kb + koff + (i >> 2) * 4096 + (i & 3) * 64);
}
__device__ __forceinline__ void issue_v(Ring& R, gcchar* vb, u32 voff) {
#pragma unroll
    for (int i = 0; i < 8; ++i) R.r[8 + i] = ldnt(vb + voff + i * 1024);
}
#define SB() __builtin_amdgcn_sched_barrier(0)

struct AttnState { f32x4 acc[8]; float m, l; };

template <bool ISSUE>
__device__ __forceinline__ void proc_tile(Ring& R, AttnState& S, const u32x4 (&qf)[4], gcchar* kn, gcchar* vn, u32 koff, u32 voff) {
    constexpr float SC = 0.08838834764831845f * 1.4426950408889634f;
    f32x4 s0 = {0.f, 0.f, 0.f, 0.f}, s1 = {0.f, 0.f, 0.f, 0.f};
#pragma unroll
    for (int st = 0; st < 4; ++st) { s0 = mfma(R.r[st], qf[st], s0); s1 = mfma(R.r[4 + st], qf[st], s1); }
    SB();
    if (ISSUE) issue_k(R, kn, koff);
    SB();
    float mx = fmaxf(fmaxf(fmaxf(s0[0], s0[1]), fmaxf(s0[2], s0[3])), fmaxf(fmaxf(s1[0], s1[1]), fmaxf(s1[2], s1[3])));
    mx = xrow_max(mx) * SC;
    if (__builtin_amdgcn_ballot_w64(mx > S.m) != 0ull) {
        const float mn = fmaxf(S.m, mx);
        const float al = __builtin_amdgcn_exp2f(S.m - mn);
        S.m = mn; S.l *= al;
#pragma unroll
        for (int d = 0; d < 8; ++d) S.acc[d] *= al;
    }
    float p[8];
#pragma unroll
    for (int j = 0; j < 4; ++j) { p[j] = __builtin_amdgcn_exp2f(__builtin_fmaf(s0[j], SC, -S.m)); p[4 + j] = __builtin_amdgcn_exp2f(__builtin_fmaf(s1[j], SC, -S.m)); }
    S.l += ((p[0] + p[1]) + (p[2] + p[3])) + ((p[4] + p[5]) + (p[6] + p[7]));
    u32x4 pf = {pack2(p[0], p[1]), pack2(p[2], p[3]), pack2(p[4], p[5]), pack2(p[6], p[7])};
    pv_tile(&R.r[8], pf, S.acc);
    SB();
    if (ISSUE) issue_v(R, vn, voff);
    SB();
}

__global__ void __launch_bounds__(320, 2) fused_decode_kernel(Params P) {
    __shared__ __attribute__((aligned(16))) char lds[LDS_BYTES];
    const int tid = threadIdx.x, lane = tid & 63;
    const int wave = __builtin_amdgcn_readfirstlane(tid >> 6);
    const int j = blockIdx.x;
    const int hkv = j & 7, pidx = j >> 3;
    const int b = pidx >> 3, split = pidx & 7;
    const int grp = b * 8 + hkv;
    const bool grp_mlp = grp < NMLP;
    const bool is_mlp = grp_mlp && split == 7;
    const __amdgpu_buffer_rsrc_t rs = __builtin_amdgcn_make_buffer_rsrc(P.ws, 0, WS_END, 0x00020000);
    const int g = lane >> 4, n16 = lane & 15;
    const int oh = hkv * 4 + (pidx & 3), okh = (pidx >> 2) & 1, ons = pidx >> 3;

    if (wave < 4) {
        STAMP(0);
        const int t_start = grp_mlp ? 65 * split : 64 * split;
        const int t_cnt = grp_mlp ? (split == 7 ? 57 : 65) : 64;
        const int nt = (t_cnt - wave + 3) >> 2;
        gcchar* kb = (gcchar*)P.k + (size_t)grp * (SK * DH * 2) + (size_t)(t_start + wave) * 8192;
        gcchar* vb = (gcchar*)P.v + (size_t)grp * (SK * DH * 2) + (size_t)(t_start + wave) * 8192;
        const u32 koff = (u32)((4 * (n16 & 3) + (n16 >> 2)) * 256 + g * 16);
        const u32 voff = (u32)(lane * 16);
        Ring A, B;
        AttnState S;
        if (is_mlp) {
            const int f0 = grp * 64;
            gcchar* w1b = (gcchar*)P.w1 + (size_t)(128 * wave) * (FF * 2) + f0 * 2;
            const u32 w1off = (u32)((32 * (n16 >> 3) + 8 * g) * (FF * 2) + (n16 & 7) * 16);
            gcchar* w2b = (gcchar*)P.w2 + (size_t)f0 * 1024 + 256 * wave;
            const u32 w2off = (u32)(8 * g * 1024 + n16 * 16);
#pragma unroll
            for (int i = 0; i < 16; ++i) {
                if (i >= 8) asm volatile("s_waitcnt vmcnt(7)" ::: "memory");
                A.r[i] = ldnt(w1b + w1off + ((i >> 3) * 64 + (i & 7)) * (FF * 2));
            }
#pragma unroll
            for (int i = 0; i < 16; ++i) {
                asm volatile("s_waitcnt vmcnt(7)" ::: "memory");
                B.r[i] = ldnt(w2b + w2off + ((i >> 3) * 32 + (i & 7)) * 1024);
            }
            const float b1v = bflo((u32)((const GAS unsigned short*)P.b1)[f0 + lane]);
            wg_barrier();
#pragma unroll
            for (int d = 0; d < 8; ++d) S.acc[d] = f32x4{0.f, 0.f, 0.f, 0.f};
#pragma unroll
            for (int tau = 0; tau < 2; ++tau) {
                const u32x4 xf = *(const u32x4*)(lds + L_XN + (n16 & 3) * 1024 + (128 * wave + 64 * tau + 32 * ((n16 >> 2) & 1) + 8 * g) * 2);
                pv_tile(&A.r[8 * tau], xf, S.acc);
            }
            SB();
            issue_k(A, kb, koff); issue_v(A, vb, voff);
            SB();
            if (n16 < 8 && (g >> 1) == (n16 >> 2)) {
                float* hp = (float*)(lds + L_HP) + ((wave * 2 + (n16 >> 2)) * 4 + (n16 & 3)) * 64 + 32 * (g & 1);
#pragma unroll
                for (int r = 0; r < 4; ++r) {
                    *(f32x4*)(hp + 8 * r) = f32x4{S.acc[0][r], S.acc[1][r], S.acc[2][r], S.acc[3][r]};
                    *(f32x4*)(hp + 8 * r + 4) = f32x4{S.acc[4][r], S.acc[5][r], S.acc[6][r], S.acc[7][r]};
                }
            }
            wg_barrier();
            {
                const float* hp = (const float*)(lds + L_HP) + wave * 64 + lane;
                float sum = 0.f;
#pragma unroll
                for (int i = 0; i < 8; ++i) sum += hp[i * 256];
                float h = bfr(bfr(sum) + b1v);
                h = h > 0.f ? h : 0.f;
                ((unsigned short*)(lds + L_H))[wave * 64 + lane] = (unsigned short)pack2(h * h, 0.f);
            }
            wg_barrier();
#pragma unroll
            for (int d = 0; d < 8; ++d) S.acc[d] = f32x4{0.f, 0.f, 0.f, 0.f};
#pragma unroll
            for (int tau = 0; tau < 2; ++tau) {
                const u32x4 hf = *(const u32x4*)(lds + L_H + (n16 >> 2) * 128 + (32 * tau + 8 * g) * 2);
                pv_tile(&B.r[8 * tau], hf, S.acc);
            }
            SB();
            issue_k(B, kb + 4 * 8192, koff); issue_v(B, vb + 4 * 8192, voff);
            SB();
            const u32 T = *(const u32*)(lds + L_T);
            publish_cols<32>(rs, WS_MP, S.acc, grp, 16 * wave, lane, T);
        } else {
#pragma unroll
            for (int i = 0; i < 8; ++i) A.r[i] = ldnt(kb + koff + (i >> 2) * 4096 + (i & 3) * 64);
#pragma unroll
            for (int i = 0; i < 8; ++i) { asm volatile("s_waitcnt vmcnt(7)" ::: "memory"); A.r[8 + i] = ldnt(vb + voff + i * 1024); }
#pragma unroll
            for (int i = 0; i < 8; ++i) { asm volatile("s_waitcnt vmcnt(7)" ::: "memory"); B.r[i] = ldnt(kb + 4 * 8192 + koff + (i >> 2) * 4096 + (i & 3) * 64); }
#pragma unroll
            for (int i = 0; i < 8; ++i) { asm volatile("s_waitcnt vmcnt(7)" ::: "memory"); B.r[8 + i] = ldnt(vb + 4 * 8192 + voff + i * 1024); }
        }
        STAMP(1);
        wg_barrier();
        STAMP(2);
        u32x4 qf[4];
#pragma unroll
        for (int st = 0; st < 4; ++st) qf[st] = *(const u32x4*)(lds + L_Q + (n16 & 3) * 256 + st * 64 + g * 16);
        const u32 T = *(const u32*)(lds + L_T);
#pragma unroll
        for (int d = 0; d < 8; ++d) S.acc[d] = f32x4{0.f, 0.f, 0.f, 0.f};
        S.m = -INFINITY; S.l = 0.f;
        int i = 0;
        gcchar* kn = kb + 8 * 8192; gcchar* vn = vb + 8 * 8192;
        for (; i + 3 < nt; i += 2) {
            proc_tile<true>(A, S, qf, kn, vn, koff, voff);
            proc_tile<true>(B, S, qf, kn + 4 * 8192, vn + 4 * 8192, koff, voff);
            kn += 8 * 8192; vn += 8 * 8192;
        }
        if (nt - i == 3) {
            proc_tile<true>(A, S, qf, kn, vn, koff, voff);
            proc_tile<false>(B, S, qf, kn, vn, koff, voff);
            proc_tile<false>(A, S, qf, kn, vn, koff, voff);
        } else {
            proc_tile<false>(A, S, qf, kn, vn, koff, voff);
            proc_tile<false>(B, S, qf, kn, vn, koff, voff);
        }
        STAMP(3);
        const float lw = xrow_sum(S.l);
        if (n16 < 4) {
            float* mo = (float*)(lds + L_MO) + (wave * 4 + n16) * 144 + g * 36;
#pragma unroll
            for (int r = 0; r < 4; ++r) {
                *(f32x4*)(mo + 8 * r) = f32x4{S.acc[0][r], S.acc[1][r], S.acc[2][r], S.acc[3][r]};
                *(f32x4*)(mo + 8 * r + 4) = f32x4{S.acc[4][r], S.acc[5][r], S.acc[6][r], S.acc[7][r]};
            }
            if (g == 0) *(f32x2*)(lds + L_ML + (wave * 4 + n16) * 8) = f32x2{S.m, lw};
        }
        wg_barrier();
        {
            f32x2 ml[4], ov[4];
#pragma unroll
            for (int w = 0; w < 4; ++w) {
                ml[w] = *(const f32x2*)(lds + L_ML + (w * 4 + wave) * 8);
                ov[w] = *(const f32x2*)((const float*)(lds + L_MO) + (w * 4 + wave) * 144 + (lane >> 4) * 36 + 2 * (lane & 15));
            }
            const float M = fmaxf(fmaxf(ml[0].x, ml[1].x), fmaxf(ml[2].x, ml[3].x));
            float L = 0.f, o0 = 0.f, o1 = 0.f;
#pragma unroll
            for (int w = 0; w < 4; ++w) {
                const float a = __builtin_amdgcn_exp2f(ml[w].x - M);
                L += a * ml[w].y; o0 += a * ov[w].x; o1 += a * ov[w].y;
            }
            const float inv = 1.0f / L;
            const u32 blk = (u32)((b * 32 + hkv * 4 + wave) * 8 + split);
            st_unit(rs, WS_PO + blk * 1024 + lane * 16, mk_unit(__float_as_uint(o0 * inv), __float_as_uint(o1 * inv), T));
            if (lane == 0) {
                const float lse = M + __builtin_amdgcn_logf(L);
                st_unit(rs, WS_PL + blk * 16, mk_unit(__float_as_uint(lse), 0u, T));
            }
        }
        STAMP(4);
        {
            const u32 blk0 = (u32)((wave * 32 + oh) * 8 + (lane >> 5));
            const u32 ooff = WS_PO + blk0 * 1024 + (u32)((32 * okh + (lane & 31)) * 16);
            const u32 loff = WS_PL + blk0 * 16;
            u32x4 uo[4], ul[4];
            for (u32 spin = 0; spin < SPIN_CAP; ++spin) {
                asm volatile("" ::: "memory");
                bool ok = true;
#pragma unroll
                for (int r = 0; r < 4; ++r) { uo[r] = ld_unit(rs, ooff + r * 2048); ul[r] = ld_unit(rs, loff + r * 32); }
#pragma unroll
                for (int r = 0; r < 4; ++r) ok = ok & unit_ok(uo[r], T) & unit_ok(ul[r], T);
                if (__builtin_amdgcn_ballot_w64(!ok) == 0ull) break;
                __builtin_amdgcn_s_sleep(1);
            }
            float M = fmaxf(fmaxf(__uint_as_float(ul[0].x), __uint_as_float(ul[1].x)), fmaxf(__uint_as_float(ul[2].x), __uint_as_float(ul[3].x)));
            M = x32_max(M);
            float W = 0.f, o0 = 0.f, o1 = 0.f;
#pragma unroll
            for (int r = 0; r < 4; ++r) {
                const float a = __builtin_amdgcn_exp2f(__uint_as_float(ul[r].x) - M);
                W += a; o0 += a * __uint_as_float(uo[r].x); o1 += a * __uint_as_float(uo[r].z);
            }
            W = x32_sum(W); o0 = x32_sum(o0); o1 = x32_sum(o1);
            const float inv = 1.0f / W;
            if (lane < 32) *(u32*)(lds + L_O + wave * 128 + lane * 4) = pack2(o0 * inv, o1 * inv);
        }
        STAMP(5);
        wg_barrier();
    } else {
        STAMP(0);
        u32x4 xr[4], wr[16];
#pragma unroll
        for (int bb = 0; bb < 4; ++bb) xr[bb] = *(gcu32x4*)((gcchar*)P.x + bb * 1024 + lane * 16);
        const u32 epoch = *(const GAS u32*)((gcchar*)P.ws + WS_EPOCH);
        {
            gcchar* wqb = (gcchar*)P.wq + (hkv * 512 + 16 * pidx) * 2;
            const u32 wqoff = (u32)((lane >> 1) * 16 * 8192 + (lane & 1) * 16);
#pragma unroll
            for (int i = 0; i < 16; ++i) wr[i] = ldnt(wqb + wqoff + i * 8192);
        }
        u32 T = epoch + 1u; if (T == 0u) T = 1u;
        T = __builtin_amdgcn_readfirstlane(T);
        u32 xn[4][4];
#pragma unroll
        for (int bb = 0; bb < 4; ++bb) {
            float v[8];
#pragma unroll
            for (int i = 0; i < 4; ++i) { v[2 * i] = bflo(xr[bb][i]); v[2 * i + 1] = bfhi(xr[bb][i]); }
            float s = ((v[0] + v[1]) + (v[2] + v[3])) + ((v[4] + v[5]) + (v[6] + v[7]));
            const float mean = wave_sum(s) * (1.0f / 512.0f);
            float q = 0.f;
#pragma unroll
            for (int i = 0; i < 8; ++i) { const float d = v[i] - mean; q += d * d; }
            const float var = bfr(wave_sum(q) * (1.0f / 512.0f));
            const float mb = bfr(mean);
            const float den = bfr(__builtin_sqrtf(bfr(var + 1e-5f)));
#pragma unroll
            for (int i = 0; i < 4; ++i) xn[bb][i] = pack2(bfr(v[2 * i] - mb) / den, bfr(v[2 * i + 1] - mb) / den);
        }
        if (lane == 0) *(u32*)(lds + L_T) = T;
        if (is_mlp) {
#pragma unroll
            for (int bb = 0; bb < 4; ++bb) *(u32x4*)(lds + L_XN + bb * 1024 + lane * 16) = u32x4{xn[bb][0], xn[bb][1], xn[bb][2], xn[bb][3]};
            wg_barrier();
        }
        STAMP(1);
        {
            float acc[4][8];
#pragma unroll
            for (int bb = 0; bb < 4; ++bb)
#pragma unroll
                for (int c = 0; c < 8; ++c) acc[bb][c] = 0.f;
#pragma unroll
            for (int a = 0; a < 8; ++a) {
                u32 xp[4];
#pragma unroll
                for (int bb = 0; bb < 4; ++bb) xp[bb] = (a < 4) ? dppu<EVEN>(xn[bb][a & 3]) : dppu<ODD>(xn[bb][a & 3]);
#pragma unroll
                for (int c = 0; c < 8; ++c) {
                    const u32 wp = __builtin_amdgcn_perm(wr[2 * a + 1][c >> 1], wr[2 * a][c >> 1], (c & 1) ? 0x07060302u : 0x05040100u);
#pragma unroll
                    for (int bb = 0; bb < 4; ++bb) acc[bb][c] = dot2(wp, xp[bb], acc[bb][c]);
                }
            }
            float r1[2][8], r2[8];
#pragma unroll
            for (int bb = 0; bb < 2; ++bb)
#pragma unroll
                for (int c = 0; c < 8; ++c) {
                    auto t = __builtin_amdgcn_permlane32_swap(__float_as_uint(acc[bb][c]), __float_as_uint(acc[bb + 2][c]), false, false);
                    r1[bb][c] = __uint_as_float(t[0]) + __uint_as_float(t[1]);
                }
#pragma unroll
            for (int c = 0; c < 8; ++c) {
                auto t = __builtin_amdgcn_permlane16_swap(__float_as_uint(r1[0][c]), __float_as_uint(r1[1][c]), false, false);
                float y = __uint_as_float(t[0]) + __uint_as_float(t[1]);
                y += dppf<XOR2>(y); y += dppf<ROR4>(y); y += dppf<ROR8>(y);
                r2[c] = y;
            }
            const bool sub = (n16 >> 1) & 1;
            const u32 v0 = sub ? pack2(r2[4], r2[5]) : pack2(r2[0], r2[1]);
            const u32 v1 = sub ? pack2(r2[6], r2[7]) : pack2(r2[2], r2[3]);
            if (n16 < 4) {
                const int u = 2 * (n16 & 1) + (n16 >> 1);
                st_unit(rs, WS_Q + (u32)((g * 8 + hkv) * 2048 + (4 * pidx + u) * 16), mk_unit(v0, v1, T));
            }
        }
        STAMP(2);
        if (is_mlp) { wg_barrier(); wg_barrier(); }
        {
            u32x4 u0, u1;
            const u32 off = WS_Q + (u32)(grp * 2048 + lane * 16);
            for (u32 spin = 0; spin < SPIN_CAP; ++spin) {
                asm volatile("" ::: "memory");
                u0 = ld_unit(rs, off); u1 = ld_unit(rs, off + 1024);
                if (__builtin_amdgcn_ballot_w64(!(unit_ok(u0, T) & unit_ok(u1, T))) == 0ull) break;
                __builtin_amdgcn_s_sleep(1);
            }
            *(u32x2*)(lds + L_Q + lane * 8) = u32x2{u0.x, u0.z};
            *(u32x2*)(lds + L_Q + 512 + lane * 8) = u32x2{u1.x, u1.z};
        }
        STAMP(3);
        wg_barrier();
        u32x4 wa[2][8];
        {
            u32x4 wo[16];
            gcchar* wob = (gcchar*)P.wo + (size_t)(oh * 128 + 64 * okh) * 1024 + 256 * ons;
            const u32 wooff = (u32)(8 * g * 1024 + n16 * 16);
#pragma unroll
            for (int i = 0; i < 16; ++i) wo[i] = ldnt(wob + wooff + ((i >> 3) * 32 + (i & 7)) * 1024);
#pragma unroll
            for (int tau = 0; tau < 2; ++tau) {
                wa[tau][0] = tfrag<0>(&wo[8 * tau]); wa[tau][1] = tfrag<1>(&wo[8 * tau]); wa[tau][2] = tfrag<2>(&wo[8 * tau]); wa[tau][3] = tfrag<3>(&wo[8 * tau]);
                wa[tau][4] = tfrag<4>(&wo[8 * tau]); wa[tau][5] = tfrag<5>(&wo[8 * tau]); wa[tau][6] = tfrag<6>(&wo[8 * tau]); wa[tau][7] = tfrag<7>(&wo[8 * tau]);
            }
        }
        const int fb = j >> 6, fng = j & 63;
        const int fu = lane & 3, fi = lane >> 2;
        const u32 xw = *(const GAS u32*)((gcchar*)P.x + fb * 1024 + fng * 16 + fu * 4);
        const u32 b2w = *(const GAS u32*)((gcchar*)P.b2 + fng * 16 + fu * 4);
        float g0, g1;
        {
            const u32 off = WS_MP + (u32)(((fb * 64 + fng) * 32 + fi) * 64 + fu * 16);
            const bool has1 = fi + 16 < NMLP;
            u32x4 u0, u1;
            for (u32 spin = 0; spin < SPIN_CAP; ++spin) {
                asm volatile("" ::: "memory");
                u0 = ld_unit(rs, off); u1 = ld_unit(rs, off + 1024);
                if (__builtin_amdgcn_ballot_w64(!(unit_ok(u0, T) & (unit_ok(u1, T) | !has1))) == 0ull) break;
                __builtin_amdgcn_s_sleep(16);
            }
            float a0 = __uint_as_float(u0.x) + (has1 ? __uint_as_float(u1.x) : 0.f);
            float a1 = __uint_as_float(u0.z) + (has1 ? __uint_as_float(u1.z) : 0.f);
            a0 += dppf<ROR4>(a0); a0 += dppf<ROR8>(a0); a0 = xrow_sum(a0);
            a1 += dppf<ROR4>(a1); a1 += dppf<ROR8>(a1); a1 = xrow_sum(a1);
            const float y0 = bfr(bfr(a0) + bflo(b2w)), y1 = bfr(bfr(a1) + bfhi(b2w));
            g0 = bfr(y0 / (1.0f + __expf(-y0)));
            g1 = bfr(y1 / (1.0f + __expf(-y1)));
        }
        STAMP(4);
        wg_barrier();
        wg_barrier();
        STAMP(5);
        {
            f32x4 acc[8];
#pragma unroll
            for (int d = 0; d < 8; ++d) acc[d] = f32x4{0.f, 0.f, 0.f, 0.f};
#pragma unroll
            for (int tau = 0; tau < 2; ++tau) {
                const u32x4 of = *(const u32x4*)(lds + L_O + (n16 >> 2) * 128 + (32 * tau + 8 * g) * 2);
#pragma unroll
                for (int d = 0; d < 8; ++d) acc[d] = mfma(wa[tau][d], of, acc[d]);
            }
            publish_cols<64>(rs, WS_SP, acc, oh * 2 + okh, 16 * ons, lane, T);
        }
        STAMP(6);
        {
            const u32 off = WS_SP + (u32)(((fb * 64 + fng) * 64 + fi) * 64 + fu * 16);
            u32x4 u[4];
            for (u32 spin = 0; spin < SPIN_CAP; ++spin) {
                asm volatile("" ::: "memory");
#pragma unroll
                for (int r = 0; r < 4; ++r) u[r] = ld_unit(rs, off + r * 1024);
                if (__builtin_amdgcn_ballot_w64(!(unit_ok(u[0], T) & unit_ok(u[1], T) & unit_ok(u[2], T) & unit_ok(u[3], T))) == 0ull) break;
                __builtin_amdgcn_s_sleep(1);
            }
            float a0 = (__uint_as_float(u[0].x) + __uint_as_float(u[1].x)) + (__uint_as_float(u[2].x) + __uint_as_float(u[3].x));
            float a1 = (__uint_as_float(u[0].z) + __uint_as_float(u[1].z)) + (__uint_as_float(u[2].z) + __uint_as_float(u[3].z));
            a0 += dppf<ROR4>(a0); a0 += dppf<ROR8>(a0); a0 = xrow_sum(a0);
            a1 += dppf<ROR4>(a1); a1 += dppf<ROR8>(a1); a1 = xrow_sum(a1);
            const float r0 = bflo(xw) + bfr(bfr(a0) * g0), r1 = bfhi(xw) + bfr(bfr(a1) * g1);
            if (lane < 4) *(GAS u32*)((GAS char*)P.out + fb * 1024 + fng * 16 + fu * 4) = pack2(r0, r1);
            if (j == 0 && lane == 0) *(GAS u32*)((GAS char*)P.ws + WS_EPOCH) = T;
        }
        STAMP(7);
    }
}

extern "C" void kernel_launch(void* const* d_in, const int* in_sizes, int n_in,
                              void* d_out, int out_size, void* d_ws, size_t ws_size,
                              hipStream_t stream) {
    Params P;
    P.x = d_in[0]; P.k = d_in[1]; P.v = d_in[2]; P.wq = d_in[3]; P.wo = d_in[4];
    P.w1 = d_in[6]; P.b1 = d_in[7]; P.w2 = d_in[8]; P.b2 = d_in[9];
    P.out = d_out; P.ws = d_ws;
    fused_decode_kernel<<<256, 320, 0, stream>>>(P);
}
```

```cpp
#include <hip/hip_runtime.h>
#include <hip/hip_bf16.h>
#include <stdint.h>

typedef unsigned int u32;
typedef u32 u32x4 __attribute__((ext_vector_type(4)));
typedef u32 u32x2 __attribute__((ext_vector_type(2)));
typedef float f32x4 __attribute__((ext_vector_type(4)));
typedef float f32x2 __attribute__((ext_vector_type(2)));
typedef __bf16 bf16x8 __attribute__((ext_vector_type(8)));
typedef __bf16 bf16x2 __attribute__((ext_vector_type(2)));
#define GAS __attribute__((address_space(1)))
typedef GAS const u32x4 gcu32x4;
typedef GAS const char gcchar;

constexpr int NB = 4, DM = 512, HKV = 8, SK = 16384, DH = 128, FF = 1792;
constexpr int NMLP = 28;
constexpr u32 WS_EPOCH = 0;
constexpr u32 WS_Q  = 0x1000;
constexpr u32 WS_PL = 0x20000;
constexpr u32 WS_MP = 0x30000;
constexpr u32 WS_PO = 0x100000;
constexpr u32 WS_SP = 0x200000;
constexpr u32 WS_END = 0x300000;
constexpr u32 SPIN_CAP = 1u << 17;
constexpr int L_Q   = 0;
constexpr int L_T   = 1024;
constexpr int L_XN  = 2048;
constexpr int L_HP  = 6144;
constexpr int L_H   = 14336;
constexpr int L_ML  = 14848;
constexpr int L_O   = 15104;
constexpr int L_MO  = 16384;
constexpr int L_END = 16384 + 4 * 4 * 144 * 4;
constexpr int LDS_BYTES = 84 * 1024;

struct Params {
    const void* x; const void* k; const void* v; const void* wq; const void* wo;
    const void* w1; const void* b1; const void* w2; const void* b2;
    void* out; void* ws;
};

__device__ __forceinline__ u32x4 ldnt(gcchar* p) { return __builtin_nontemporal_load((gcu32x4*)p); }
__device__ __forceinline__ float bflo(u32 w) { return __uint_as_float(w << 16); }
__device__ __forceinline__ float bfhi(u32 w) { return __uint_as_float(w & 0xffff0000u); }
__device__ __forceinline__ u32 pack2(float lo, float hi) {
    f32x2 v = {lo, hi};
    return __builtin_bit_cast(u32, __builtin_convertvector(v, bf16x2));
}
__device__ __forceinline__ float bfr(float x) { return bflo(pack2(x, 0.f)); }
__device__ __forceinline__ f32x4 mfma(u32x4 a, u32x4 b, f32x4 c) {
    return __builtin_amdgcn_mfma_f32_16x16x32_bf16(__builtin_bit_cast(bf16x8, a), __builtin_bit_cast(bf16x8, b), c, 0, 0, 0);
}
__device__ __forceinline__ float dot2(u32 a, u32 b, float c) {
    return __builtin_amdgcn_fdot2_f32_bf16(__builtin_bit_cast(bf16x2, a), __builtin_bit_cast(bf16x2, b), c, false);
}
template <int CTRL> __device__ __forceinline__ u32 dppu(u32 x) { return (u32)__builtin_amdgcn_mov_dpp((int)x, CTRL, 0xf, 0xf, true); }
template <int CTRL> __device__ __forceinline__ float dppf(float x) { return __uint_as_float(dppu<CTRL>(__float_as_uint(x))); }
constexpr int XOR1 = 0xB1, XOR2 = 0x4E, HMIR = 0x141, ROR4 = 0x124, ROR8 = 0x128, EVEN = 0xA0, ODD = 0xF5;
__device__ __forceinline__ float xrow_max(float x) {
    auto s = __builtin_amdgcn_permlane16_swap(__float_as_uint(x), __float_as_uint(x), false, false);
    x = fmaxf(__uint_as_float(s[0]), __uint_as_float(s[1]));
    auto t = __builtin_amdgcn_permlane32_swap(__float_as_uint(x), __float_as_uint(x), false, false);
    return fmaxf(__uint_as_float(t[0]), __uint_as_float(t[1]));
}
__device__ __forceinline__ float xrow_sum(float x) {
    auto s = __builtin_amdgcn_permlane16_swap(__float_as_uint(x), __float_as_uint(x), false, false);
    x = __uint_as_float(s[0]) + __uint_as_float(s[1]);
    auto t = __builtin_amdgcn_permlane32_swap(__float_as_uint(x), __float_as_uint(x), false, false);
    return __uint_as_float(t[0]) + __uint_as_float(t[1]);
}
__device__ __forceinline__ float x32_max(float x) {
    auto t = __builtin_amdgcn_permlane32_swap(__float_as_uint(x), __float_as_uint(x), false, false);
    return fmaxf(__uint_as_float(t[0]), __uint_as_float(t[1]));
}
__device__ __forceinline__ float x32_sum(float x) {
    auto t = __builtin_amdgcn_permlane32_swap(__float_as_uint(x), __float_as_uint(x), false, false);
    return __uint_as_float(t[0]) + __uint_as_float(t[1]);
}
__device__ __forceinline__ float wave_sum(float x) {
    x += dppf<XOR1>(x); x += dppf<XOR2>(x); x += dppf<ROR4>(x); x += dppf<ROR8>(x);
    return xrow_sum(x);
}
__device__ __forceinline__ void wg_barrier() { asm volatile("s_waitcnt lgkmcnt(0)\n\ts_barrier" ::: "memory"); }
__device__ __forceinline__ void raw_barrier() { asm volatile("s_barrier" ::: "memory"); }
constexpr u32 OOB = 0x40000000u;

__device__ __forceinline__ u32x4 mk_unit(u32 v0, u32 v1, u32 T) { u32x4 u = {v0, T ^ v1, v1, T ^ v0}; return u; }
__device__ __forceinline__ bool unit_ok(u32x4 u, u32 T) { return ((u.y ^ u.z) == T) & ((u.w ^ u.x) == T); }
__device__ __forceinline__ void st_unit(__amdgpu_buffer_rsrc_t rs, u32 off, u32x4 u) { __builtin_amdgcn_raw_buffer_store_b128(u, rs, off, 0, 16); }
__device__ __forceinline__ u32x4 ld_unit(__amdgpu_buffer_rsrc_t rs, u32 off) { return __builtin_amdgcn_raw_buffer_load_b128(rs, off, 0, 16); }

template <int DD> __device__ __forceinline__ u32x4 tfrag(const u32x4* v) {
    constexpr u32 sel = (DD & 1) ? 0x07060302u : 0x05040100u;
    u32x4 a;
    a.x = __builtin_amdgcn_perm(v[1][DD >> 1], v[0][DD >> 1], sel);
    a.y = __builtin_amdgcn_perm(v[3][DD >> 1], v[2][DD >> 1], sel);
    a.z = __builtin_amdgcn_perm(v[5][DD >> 1], v[4][DD >> 1], sel);
    a.w = __builtin_amdgcn_perm(v[7][DD >> 1], v[6][DD >> 1], sel);
    return a;
}
__device__ __forceinline__ void pv_tile(const u32x4* v, u32x4 p, f32x4 (&acc)[8]) {
    acc[0] = mfma(tfrag<0>(v), p, acc[0]); acc[1] = mfma(tfrag<1>(v), p, acc[1]);
    acc[2] = mfma(tfrag<2>(v), p, acc[2]); acc[3] = mfma(tfrag<3>(v), p, acc[3]);
    acc[4] = mfma(tfrag<4>(v), p, acc[4]); acc[5] = mfma(tfrag<5>(v), p, acc[5]);
    acc[6] = mfma(tfrag<6>(v), p, acc[6]); acc[7] = mfma(tfrag<7>(v), p, acc[7]);
}
template <int NSLOT>
__device__ __forceinline__ void publish_cols(__amdgpu_buffer_rsrc_t rs, u32 base, const f32x4 (&acc)[8], int slot, int ng0, int lane, u32 T) {
    const int g = lane >> 4, b = (lane >> 2) & 3, rho = lane & 3;
    const u32 off0 = base + (u32)(((b * 64 + ng0 + 4 * g) * NSLOT + slot) * 64 + rho * 16);
#pragma unroll
    for (int r = 0; r < 4; ++r) {
        float a0 = (rho & 2) ? acc[4][r] : acc[0][r], a1 = (rho & 2) ? acc[6][r] : acc[2][r];
        float c0 = (rho & 2) ? acc[5][r] : acc[1][r], c1 = (rho & 2) ? acc[7][r] : acc[3][r];
        float v0 = (rho & 1) ? a1 : a0, v1 = (rho & 1) ? c1 : c0;
        st_unit(rs, off0 + (u32)(r * NSLOT * 64), mk_unit(__float_as_uint(v0), __float_as_uint(v1), T));
    }
}

#define STAMP(i) do { } while (0)
#define STAMPV(i, v) do { } while (0)
#define FLUSH_STAMPS() do { } while (0)
#define DBG(x)

struct Ring { u32x4 r[16]; };

__device__ __forceinline__ void issue_k(Ring& R, gcchar* kb, u32 koff) {
#pragma unroll
    for (int i = 0; i < 8; ++i) R.r[i] = ldnt(kb + koff + (i >> 2) * 4096 + (i & 3) * 64);
}
__device__ __forceinline__ void issue_v(Ring& R, gcchar* vb, u32 voff) {
#pragma unroll
    for (int i = 0; i < 8; ++i) R.r[8 + i] = ldnt(vb + voff + i * 1024);
}
#define SB() __builtin_amdgcn_sched_barrier(0)
#define PACE_N 7
#define STR2(x) #x
#define STR(x) STR2(x)
#define PACE() asm volatile("s_waitcnt vmcnt(" STR(PACE_N) ")" ::: "memory")

struct AttnState { f32x4 acc[8]; float m, l; };

template <bool ISSUE>
__device__ __forceinline__ void proc_tile(Ring& R, AttnState& S, const u32x4 (&qf)[4], gcchar* kn, gcchar* vn, u32 koff, u32 voff) {
    constexpr float SCL = 0.08838834764831845f, LOG2E = 1.4426950408889634f;
    f32x4 s0 = {0.f, 0.f, 0.f, 0.f}, s1 = {0.f, 0.f, 0.f, 0.f};
#pragma unroll
    for (int st = 0; st < 4; ++st) { s0 = mfma(R.r[st], qf[st], s0); s1 = mfma(R.r[4 + st], qf[st], s1); }
    SB();
    if (ISSUE) issue_k(R, kn, koff);
    SB();
    float z[8];
#pragma unroll
    for (int j = 0; j < 2; ++j) {
        const u32 a = pack2(s0[2 * j], s0[2 * j + 1]), c = pack2(s1[2 * j], s1[2 * j + 1]);
        const u32 a2 = pack2(bflo(a) * SCL, bfhi(a) * SCL), c2 = pack2(bflo(c) * SCL, bfhi(c) * SCL);
        z[2 * j] = bflo(a2); z[2 * j + 1] = bfhi(a2); z[4 + 2 * j] = bflo(c2); z[4 + 2 * j + 1] = bfhi(c2);
    }
    float mx = fmaxf(fmaxf(fmaxf(z[0], z[1]), fmaxf(z[2], z[3])), fmaxf(fmaxf(z[4], z[5]), fmaxf(z[6], z[7])));
    mx = xrow_max(mx) * LOG2E;
    if (__builtin_amdgcn_ballot_w64(mx > S.m) != 0ull) {
        const float mn = fmaxf(S.m, mx);
        const float al = __builtin_amdgcn_exp2f(S.m - mn);
        S.m = mn; S.l *= al;
#pragma unroll
        for (int d = 0; d < 8; ++d) S.acc[d] *= al;
    }
    float p[8];
#pragma unroll
    for (int j = 0; j < 8; ++j) p[j] = __builtin_amdgcn_exp2f(__builtin_fmaf(z[j], LOG2E, -S.m));
    S.l += ((p[0] + p[1]) + (p[2] + p[3])) + ((p[4] + p[5]) + (p[6] + p[7]));
    u32x4 pf = {pack2(p[0], p[1]), pack2(p[2], p[3]), pack2(p[4], p[5]), pack2(p[6], p[7])};
    pv_tile(&R.r[8], pf, S.acc);
    SB();
    if (ISSUE) issue_v(R, vn, voff);
    SB();
}

__global__ void __launch_bounds__(320, 2) fused_decode_kernel(Params P) {
    __shared__ __attribute__((aligned(16))) char lds[LDS_BYTES];
    const int tid = threadIdx.x, lane = tid & 63;
    const int wave = __builtin_amdgcn_readfirstlane(tid >> 6);
    const int j = blockIdx.x;
    const int hkv = j & 7, pidx = j >> 3;
    const int b = pidx >> 3, split = pidx & 7;
    const int grp = b * 8 + hkv;
    const bool grp_mlp = grp < NMLP;
    const bool is_mlp = grp_mlp && split == 7;
    const __amdgpu_buffer_rsrc_t rs = __builtin_amdgcn_make_buffer_rsrc(P.ws, 0, WS_END, 0x00020000);
    const int g = lane >> 4, n16 = lane & 15;
    const int oh = hkv * 4 + (pidx & 3), okh = (pidx >> 2) & 1, ons = pidx >> 3;

    DBG(unsigned long long ts[NSTAMP]; for (int z = 0; z < NSTAMP; ++z) ts[z] = 0;)
    if (wave < 4) {
        STAMP(0);
        raw_barrier();
        const int t_start = grp_mlp ? 65 * split : 64 * split;
        const int t_cnt = grp_mlp ? (split == 7 ? 57 : 65) : 64;
        const int nt = (t_cnt - wave + 3) >> 2;
        gcchar* kb = (gcchar*)P.k + (size_t)grp * (SK * DH * 2) + (size_t)(t_start + wave) * 8192;
        gcchar* vb = (gcchar*)P.v + (size_t)grp * (SK * DH * 2) + (size_t)(t_start + wave) * 8192;
        const u32 koff = (u32)((4 * (n16 & 3) + (n16 >> 2)) * 256 + g * 16);
        const u32 voff = (u32)(lane * 16);
        Ring A, B;
        AttnState S;
        if (is_mlp) {
            const int f0 = grp * 64;
            gcchar* w1b = (gcchar*)P.w1 + (size_t)(128 * wave) * (FF * 2) + f0 * 2;
            const u32 w1off = (u32)((32 * (n16 >> 3) + 8 * g) * (FF * 2) + (n16 & 7) * 16);
            gcchar* w2b = (gcchar*)P.w2 + (size_t)f0 * 1024 + 256 * wave;
            const u32 w2off = (u32)(8 * g * 1024 + n16 * 16);
#pragma unroll
            for (int i = 0; i < 16; ++i) {
                if (i >= 8) PACE();
                A.r[i] = ldnt(w1b + w1off + ((i >> 3) * 64 + (i & 7)) * (FF * 2));
            }
#pragma unroll
            for (int i = 0; i < 16; ++i) {
                PACE();
                B.r[i] = ldnt(w2b + w2off + ((i >> 3) * 32 + (i & 7)) * 1024);
            }
            const float b1v = bflo((u32)((const GAS unsigned short*)P.b1)[f0 + lane]);
            wg_barrier();
#pragma unroll
            for (int d = 0; d < 8; ++d) S.acc[d] = f32x4{0.f, 0.f, 0.f, 0.f};
#pragma unroll
            for (int tau = 0; tau < 2; ++tau) {
                const u32x4 xf = *(const u32x4*)(lds + L_XN + (n16 & 3) * 1024 + (128 * wave + 64 * tau + 32 * ((n16 >> 2) & 1) + 8 * g) * 2);
                pv_tile(&A.r[8 * tau], xf, S.acc);
            }
            SB();
            issue_k(A, kb, koff); issue_v(A, vb, voff);
            SB();
            if (n16 < 8 && (g >> 1) == (n16 >> 2)) {
                float* hp = (float*)(lds + L_HP) + ((wave * 2 + (n16 >> 2)) * 4 + (n16 & 3)) * 64 + 32 * (g & 1);
#pragma unroll
                for (int r = 0; r < 4; ++r) {
                    *(f32x4*)(hp + 8 * r) = f32x4{S.acc[0][r], S.acc[1][r], S.acc[2][r], S.acc[3][r]};
                    *(f32x4*)(hp + 8 * r + 4) = f32x4{S.acc[4][r], S.acc[5][r], S.acc[6][r], S.acc[7][r]};
                }
            }
            wg_barrier();
            {
                const float* hp = (const float*)(lds + L_HP) + wave * 64 + lane;
                float sum = 0.f;
#pragma unroll
                for (int i = 0; i < 8; ++i) sum += hp[i * 256];
                float h = bfr(bfr(sum) + b1v);
                h = h > 0.f ? h : 0.f;
                ((unsigned short*)(lds + L_H))[wave * 64 + lane] = (unsigned short)pack2(h * h, 0.f);
            }
            wg_barrier();
#pragma unroll
            for (int d = 0; d < 8; ++d) S.acc[d] = f32x4{0.f, 0.f, 0.f, 0.f};
#pragma unroll
            for (int tau = 0; tau < 2; ++tau) {
                const u32x4 hf = *(const u32x4*)(lds + L_H + (n16 >> 2) * 128 + (32 * tau + 8 * g) * 2);
                pv_tile(&B.r[8 * tau], hf, S.acc);
            }
            SB();
            issue_k(B, kb + 4 * 8192, koff); issue_v(B, vb + 4 * 8192, voff);
            SB();
            const u32 T = *(const u32*)(lds + L_T);
            publish_cols<32>(rs, WS_MP, S.acc, grp, 16 * wave, lane, T);
        } else {
#pragma unroll
            for (int i = 0; i < 8; ++i) A.r[i] = ldnt(kb + koff + (i >> 2) * 4096 + (i & 3) * 64);
#pragma unroll
            for (int i = 0; i < 8; ++i) { PACE(); A.r[8 + i] = ldnt(vb + voff + i * 1024); }
#pragma unroll
            for (int i = 0; i < 8; ++i) { PACE(); B.r[i] = ldnt(kb + 4 * 8192 + koff + (i >> 2) * 4096 + (i & 3) * 64); }
#pragma unroll
            for (int i = 0; i < 8; ++i) { PACE(); B.r[8 + i] = ldnt(vb + 4 * 8192 + voff + i * 1024); }
        }
        STAMP(1);
        wg_barrier();
        STAMP(2);
        u32x4 qf[4];
#pragma unroll
        for (int st = 0; st < 4; ++st) qf[st] = *(const u32x4*)(lds + L_Q + (n16 & 3) * 256 + st * 64 + g * 16);
        const u32 T = *(const u32*)(lds + L_T);
#pragma unroll
        for (int d = 0; d < 8; ++d) S.acc[d] = f32x4{0.f, 0.f, 0.f, 0.f};
        S.m = -INFINITY; S.l = 0.f;
        int i = 0;
        gcchar* kn = kb + 8 * 8192; gcchar* vn = vb + 8 * 8192;
        for (; i + 3 < nt; i += 2) {
            proc_tile<true>(A, S, qf, kn, vn, koff, voff);
            proc_tile<true>(B, S, qf, kn + 4 * 8192, vn + 4 * 8192, koff, voff);
            kn += 8 * 8192; vn += 8 * 8192;
            DBG(if (i == 2) STAMP(8); if (i == 6) STAMP(9); if (i == 10) STAMP(10);)
        }
        if (nt - i == 3) {
            proc_tile<true>(A, S, qf, kn, vn, koff, voff);
            proc_tile<false>(B, S, qf, kn, vn, koff, voff);
            proc_tile<false>(A, S, qf, kn, vn, koff, voff);
        } else {
            proc_tile<false>(A, S, qf, kn, vn, koff, voff);
            proc_tile<false>(B, S, qf, kn, vn, koff, voff);
        }
        STAMP(3);
        const float lw = xrow_sum(S.l);
        if (n16 < 4) {
            float* mo = (float*)(lds + L_MO) + (wave * 4 + n16) * 144 + g * 36;
#pragma unroll
            for (int r = 0; r < 4; ++r) {
                *(f32x4*)(mo + 8 * r) = f32x4{S.acc[0][r], S.acc[1][r], S.acc[2][r], S.acc[3][r]};
                *(f32x4*)(mo + 8 * r + 4) = f32x4{S.acc[4][r], S.acc[5][r], S.acc[6][r], S.acc[7][r]};
            }
            if (g == 0) *(f32x2*)(lds + L_ML + (wave * 4 + n16) * 8) = f32x2{S.m, lw};
        }
        wg_barrier();
        {
            f32x2 ml[4], ov[4];
#pragma unroll
            for (int w = 0; w < 4; ++w) {
                ml[w] = *(const f32x2*)(lds + L_ML + (w * 4 + wave) * 8);
                ov[w] = *(const f32x2*)((const float*)(lds + L_MO) + (w * 4 + wave) * 144 + (lane >> 4) * 36 + 2 * (lane & 15));
            }
            const float M = fmaxf(fmaxf(ml[0].x, ml[1].x), fmaxf(ml[2].x, ml[3].x));
            float L = 0.f, o0 = 0.f, o1 = 0.f;
#pragma unroll
            for (int w = 0; w < 4; ++w) {
                const float a = __builtin_amdgcn_exp2f(ml[w].x - M);
                L += a * ml[w].y; o0 += a * ov[w].x; o1 += a * ov[w].y;
            }
            const float inv = 1.0f / L;
            const u32 blk = (u32)((b * 32 + hkv * 4 + wave) * 8 + split);
            st_unit(rs, WS_PO + blk * 1024 + lane * 16, mk_unit(__float_as_uint(o0 * inv), __float_as_uint(o1 * inv), T));
            if (lane == 0) {
                const float lse = M + __builtin_amdgcn_logf(L);
                st_unit(rs, WS_PL + blk * 16, mk_unit(__float_as_uint(lse), 0u, T));
            }
        }
        STAMP(4);
        {
            const int half = lane >> 5;
            const u32 blk0 = (u32)((wave * 32 + oh) * 8);
            const u32 ooff = WS_PO + (blk0 + half) * 1024 + (u32)((32 * okh + (lane & 31)) * 16);
            const u32 loff = lane < 8 ? WS_PL + (blk0 + lane) * 16 : OOB;
            u32x4 uo[4], ul;
            u32 want = 0, have = 0;
            u32 lseen = 0;
            for (u32 spin = 0; spin < SPIN_CAP; ++spin) {
                asm volatile("" ::: "memory");
                u32x4 t[4];
                ul = ld_unit(rs, loff);
#pragma unroll
                for (int r = 0; r < 4; ++r) t[r] = ld_unit(rs, ((want >> r) & 1u) ? ooff + r * 2048 : OOB);
#pragma unroll
                for (int r = 0; r < 4; ++r)
                    if (((want >> r) & 1u) && unit_ok(t[r], T)) { uo[r] = t[r]; have |= 1u << r; }
                lseen = (u32)__builtin_amdgcn_ballot_w64(unit_ok(ul, T)) & 0xffu;
                const bool done = lseen == 0xffu && __builtin_amdgcn_ballot_w64(have != 0xfu) == 0ull;
                if (done) break;
                const u32 gate = (__builtin_popcount(lseen) >= 6) ? 0xffu : lseen;
                const u32 gl = gate >> half;
                want = ((gl & 1u) | ((gl >> 1) & 2u) | ((gl >> 2) & 4u) | ((gl >> 3) & 8u)) & ~have;
                if (__builtin_amdgcn_ballot_w64(want != 0u) == 0ull || lseen != 0xffu) __builtin_amdgcn_s_sleep(2);
            }
            const float lse = __uint_as_float(ul.x);
            float M = fmaxf(lse, dppf<XOR1>(lse)); M = fmaxf(M, dppf<XOR2>(M)); M = fmaxf(M, dppf<HMIR>(M));
            const float al = __builtin_amdgcn_exp2f(lse - M);
            float W = al + dppf<XOR1>(al); W += dppf<XOR2>(W); W += dppf<HMIR>(W);
            const float inv = 1.0f / W;
            const u32 aw = __float_as_uint(al * inv);
#define WSEL(r) __uint_as_float(half ? __builtin_amdgcn_readlane(aw, 2 * (r) + 1) : __builtin_amdgcn_readlane(aw, 2 * (r)))
            const float a0 = WSEL(0), a1 = WSEL(1), a2 = WSEL(2), a3 = WSEL(3);
#undef WSEL
            float o0 = (a0 * __uint_as_float(uo[0].x) + a1 * __uint_as_float(uo[1].x)) + (a2 * __uint_as_float(uo[2].x) + a3 * __uint_as_float(uo[3].x));
            float o1 = (a0 * __uint_as_float(uo[0].z) + a1 * __uint_as_float(uo[1].z)) + (a2 * __uint_as_float(uo[2].z) + a3 * __uint_as_float(uo[3].z));
            o0 = x32_sum(o0); o1 = x32_sum(o1);
            if (lane < 32) *(u32*)(lds + L_O + wave * 128 + lane * 4) = pack2(o0, o1);
        }
        STAMP(5);
        wg_barrier();
        FLUSH_STAMPS();
    } else {
        STAMP(0);
        u32x4 xr[4], wr[16];
        const u32 epoch = *(const GAS u32*)((gcchar*)P.ws + WS_EPOCH);
#pragma unroll
        for (int bb = 0; bb < 4; ++bb) xr[bb] = *(gcu32x4*)((gcchar*)P.x + bb * 1024 + lane * 16);
        {
            gcchar* wqb = (gcchar*)P.wq + (hkv * 512 + 16 * pidx) * 2;
            const u32 wqoff = (u32)((lane >> 1) * 16 * 8192 + (lane & 1) * 16);
#pragma unroll
            for (int i = 0; i < 16; ++i) wr[i] = ldnt(wqb + wqoff + i * 8192);
        }
        SB();
        raw_barrier();
        SB();
        DBG(asm volatile("s_waitcnt vmcnt(16)" ::: "memory"); STAMP(8);)
        u32 T = epoch + 1u; if (T == 0u) T = 1u;
        T = __builtin_amdgcn_readfirstlane(T);
        u32 xn[4][4];
#pragma unroll
        for (int bb = 0; bb < 4; ++bb) {
            float v[8];
#pragma unroll
            for (int i = 0; i < 4; ++i) { v[2 * i] = bflo(xr[bb][i]); v[2 * i + 1] = bfhi(xr[bb][i]); }
            float s = ((v[0] + v[1]) + (v[2] + v[3])) + ((v[4] + v[5]) + (v[6] + v[7]));
            const float mean = wave_sum(s) * (1.0f / 512.0f);
            float q = 0.f;
#pragma unroll
            for (int i = 0; i < 8; ++i) { const float d = v[i] - mean; q += d * d; }
            const float var = bfr(wave_sum(q) * (1.0f / 512.0f));
            const float mb = bfr(mean);
            const float den = bfr(__builtin_sqrtf(bfr(var + 1e-5f)));
#pragma unroll
            for (int i = 0; i < 4; ++i) xn[bb][i] = pack2(bfr(v[2 * i] - mb) / den, bfr(v[2 * i + 1] - mb) / den);
        }
        if (lane == 0) *(u32*)(lds + L_T) = T;
        if (is_mlp) {
#pragma unroll
            for (int bb = 0; bb < 4; ++bb) *(u32x4*)(lds + L_XN + bb * 1024 + lane * 16) = u32x4{xn[bb][0], xn[bb][1], xn[bb][2], xn[bb][3]};
        }
        STAMP(1);
        DBG(asm volatile("s_waitcnt vmcnt(0)" ::: "memory"); STAMP(9);)
        {
            float acc[4][8];
#pragma unroll
            for (int bb = 0; bb < 4; ++bb)
#pragma unroll
                for (int c = 0; c < 8; ++c) acc[bb][c] = 0.f;
#pragma unroll
            for (int a = 0; a < 8; ++a) {
                u32 xp[4];
#pragma unroll
                for (int bb = 0; bb < 4; ++bb) xp[bb] = (a < 4) ? dppu<EVEN>(xn[bb][a & 3]) : dppu<ODD>(xn[bb][a & 3]);
#pragma unroll
                for (int c = 0; c < 8; ++c) {
                    const u32 wp = __builtin_amdgcn_perm(wr[2 * a + 1][c >> 1], wr[2 * a][c >> 1], (c & 1) ? 0x07060302u : 0x05040100u);
#pragma unroll
                    for (int bb = 0; bb < 4; ++bb) acc[bb][c] = dot2(wp, xp[bb], acc[bb][c]);
                }
            }
            float r1[2][8], r2[8];
#pragma unroll
            for (int bb = 0; bb < 2; ++bb)
#pragma unroll
                for (int c = 0; c < 8; ++c) {
                    auto t = __builtin_amdgcn_permlane32_swap(__float_as_uint(acc[bb][c]), __float_as_uint(acc[bb + 2][c]), false, false);
                    r1[bb][c] = __uint_as_float(t[0]) + __uint_as_float(t[1]);
                }
#pragma unroll
            for (int c = 0; c < 8; ++c) {
                auto t = __builtin_amdgcn_permlane16_swap(__float_as_uint(r1[0][c]), __float_as_uint(r1[1][c]), false, false);
                float y = __uint_as_float(t[0]) + __uint_as_float(t[1]);
                y += dppf<XOR2>(y); y += dppf<ROR4>(y); y += dppf<ROR8>(y);
                r2[c] = y;
            }
            const bool sub = (n16 >> 1) & 1;
            const u32 v0 = sub ? pack2(r2[4], r2[5]) : pack2(r2[0], r2[1]);
            const u32 v1 = sub ? pack2(r2[6], r2[7]) : pack2(r2[2], r2[3]);
            if (n16 < 4) {
                const int u = 2 * (n16 & 1) + (n16 >> 1);
                st_unit(rs, WS_Q + (u32)((g * 8 + hkv) * 2048 + (4 * pidx + u) * 16), mk_unit(v0, v1, T));
            }
        }
        STAMP(2);
        if (is_mlp) { wg_barrier(); wg_barrier(); wg_barrier(); }
        {
            u32x4 u0, u1;
            const u32 off = WS_Q + (u32)(grp * 2048 + lane * 16);
            for (u32 spin = 0; spin < SPIN_CAP; ++spin) {
                asm volatile("" ::: "memory");
                u0 = ld_unit(rs, off); u1 = ld_unit(rs, off + 1024);
                DBG(if (spin == 0) { asm volatile("s_waitcnt vmcnt(0)" ::: "memory"); STAMP(10); })
                if (__builtin_amdgcn_ballot_w64(!(unit_ok(u0, T) & unit_ok(u1, T))) == 0ull) { STAMPV(11, 1000000ull + spin); break; }
                __builtin_amdgcn_s_sleep(1);
            }
            *(u32x2*)(lds + L_Q + lane * 8) = u32x2{u0.x, u0.z};
            *(u32x2*)(lds + L_Q + 512 + lane * 8) = u32x2{u1.x, u1.z};
        }
        STAMP(3);
        wg_barrier();
        u32x4 wa[2][8];
        {
            u32x4 wo[16];
            gcchar* wob = (gcchar*)P.wo + (size_t)(oh * 128 + 64 * okh) * 1024 + 256 * ons;
            const u32 wooff = (u32)(8 * g * 1024 + n16 * 16);
#pragma unroll
            for (int i = 0; i < 16; ++i) wo[i] = ldnt(wob + wooff + ((i >> 3) * 32 + (i & 7)) * 1024);
#pragma unroll
            for (int tau = 0; tau < 2; ++tau) {
                wa[tau][0] = tfrag<0>(&wo[8 * tau]); wa[tau][1] = tfrag<1>(&wo[8 * tau]); wa[tau][2] = tfrag<2>(&wo[8 * tau]); wa[tau][3] = tfrag<3>(&wo[8 * tau]);
                wa[tau][4] = tfrag<4>(&wo[8 * tau]); wa[tau][5] = tfrag<5>(&wo[8 * tau]); wa[tau][6] = tfrag<6>(&wo[8 * tau]); wa[tau][7] = tfrag<7>(&wo[8 * tau]);
            }
        }
        const int fb = j >> 6, fng = j & 63;
        const int fu = lane & 3, fi = lane >> 2;
        const u32 xw = *(const GAS u32*)((gcchar*)P.x + fb * 1024 + fng * 16 + fu * 4);
        const u32 b2w = *(const GAS u32*)((gcchar*)P.b2 + fng * 16 + fu * 4);
        float g0, g1;
        {
            const u32 off = WS_MP + (u32)(((fb * 64 + fng) * 32 + fi) * 64 + fu * 16);
            const bool has1 = fi + 16 < NMLP;
            u32x4 u0, u1;
            for (u32 spin = 0; spin < SPIN_CAP; ++spin) {
                __builtin_amdgcn_s_sleep(127); __builtin_amdgcn_s_sleep(127);
                asm volatile("" ::: "memory");
                u0 = ld_unit(rs, off); u1 = ld_unit(rs, off + 1024);
                if (__builtin_amdgcn_ballot_w64(!(unit_ok(u0, T) & (unit_ok(u1, T) | !has1))) == 0ull) break;
            }
            float a0 = __uint_as_float(u0.x) + (has1 ? __uint_as_float(u1.x) : 0.f);
            float a1 = __uint_as_float(u0.z) + (has1 ? __uint_as_float(u1.z) : 0.f);
            a0 += dppf<ROR4>(a0); a0 += dppf<ROR8>(a0); a0 = xrow_sum(a0);
            a1 += dppf<ROR4>(a1); a1 += dppf<ROR8>(a1); a1 = xrow_sum(a1);
            const float y0 = bfr(bfr(a0) + bflo(b2w)), y1 = bfr(bfr(a1) + bfhi(b2w));
            g0 = bfr(y0 / (1.0f + __expf(-y0)));
            g1 = bfr(y1 / (1.0f + __expf(-y1)));
        }
        STAMP(4);
        wg_barrier();
        wg_barrier();
        STAMP(5);
        {
            f32x4 acc[8];
#pragma unroll
            for (int d = 0; d < 8; ++d) acc[d] = f32x4{0.f, 0.f, 0.f, 0.f};
#pragma unroll
            for (int tau = 0; tau < 2; ++tau) {
                const u32x4 of = *(const u32x4*)(lds + L_O + (n16 >> 2) * 128 + (32 * tau + 8 * g) * 2);
#pragma unroll
                for (int d = 0; d < 8; ++d) acc[d] = mfma(wa[tau][d], of, acc[d]);
            }
            publish_cols<64>(rs, WS_SP, acc, oh * 2 + okh, 16 * ons, lane, T);
        }
        STAMP(6);
        {
            const u32 off = WS_SP + (u32)(((fb * 64 + fng) * 64 + fi) * 64 + fu * 16);
            u32x4 u[4];
            u32 have = 0;
            for (u32 spin = 0; spin < SPIN_CAP; ++spin) {
                asm volatile("" ::: "memory");
                u32x4 t[4];
#pragma unroll
                for (int r = 0; r < 4; ++r) t[r] = ld_unit(rs, ((have >> r) & 1u) ? OOB : off + r * 1024);
#pragma unroll
                for (int r = 0; r < 4; ++r)
                    if (!((have >> r) & 1u) && unit_ok(t[r], T)) { u[r] = t[r]; have |= 1u << r; }
                if (__builtin_amdgcn_ballot_w64(have != 0xfu) == 0ull) break;
                __builtin_amdgcn_s_sleep(1);
            }
            float a0 = (__uint_as_float(u[0].x) + __uint_as_float(u[1].x)) + (__uint_as_float(u[2].x) + __uint_as_float(u[3].x));
            float a1 = (__uint_as_float(u[0].z) + __uint_as_float(u[1].z)) + (__uint_as_float(u[2].z) + __uint_as_float(u[3].z));
            a0 += dppf<ROR4>(a0); a0 += dppf<ROR8>(a0); a0 = xrow_sum(a0);
            a1 += dppf<ROR4>(a1); a1 += dppf<ROR8>(a1); a1 = xrow_sum(a1);
            const float r0 = bflo(xw) + bfr(bfr(a0) * g0), r1 = bfhi(xw) + bfr(bfr(a1) * g1);
            if (lane < 4) *(GAS u32*)((GAS char*)P.out + fb * 1024 + fng * 16 + fu * 4) = pack2(r0, r1);
            if (j == 0 && lane == 0) *(GAS u32*)((GAS char*)P.ws + WS_EPOCH) = T;
        }
        STAMP(7);
        FLUSH_STAMPS();
    }
}

extern "C" void kernel_launch(void* const* d_in, const int* in_sizes, int n_in,
                              void* d_out, int out_size, void* d_ws, size_t ws_size,
                              hipStream_t stream) {
    Params P;
    P.x = d_in[0]; P.k = d_in[1]; P.v = d_in[2]; P.wq = d_in[3]; P.wo = d_in[4];
    P.w1 = d_in[6]; P.b1 = d_in[7]; P.w2 = d_in[8]; P.b2 = d_in[9];
    P.out = d_out; P.ws = d_ws;
    fused_decode_kernel<<<256, 320, 0, stream>>>(P);
}
```

```cpp
#include <hip/hip_runtime.h>
#include <hip/hip_bf16.h>
#include <stdint.h>

typedef unsigned int u32;
typedef u32 u32x4 __attribute__((ext_vector_type(4)));
typedef u32 u32x2 __attribute__((ext_vector_type(2)));
typedef float f32x4 __attribute__((ext_vector_type(4)));
typedef float f32x2 __attribute__((ext_vector_type(2)));
typedef __bf16 bf16x8 __attribute__((ext_vector_type(8)));
typedef __bf16 bf16x2 __attribute__((ext_vector_type(2)));
#define GAS __attribute__((address_space(1)))
typedef GAS const u32x4 gcu32x4;
typedef GAS const char gcchar;

constexpr int NB = 4, DM = 512, HKV = 8, SK = 16384, DH = 128, FF = 1792;
constexpr int NHB = 28;
constexpr u32 WS_EPOCH = 0;
constexpr u32 WS_Q  = 0x1000;
constexpr u32 WS_PL = 0x20000;
constexpr u32 WS_PO = 0x100000;
constexpr u32 WS_SP = 0x200000;
constexpr u32 WS_MP = 0x300000;
constexpr u32 WS_H  = 0x12000;
constexpr int NSL2 = 56;
constexpr u32 WS_END = 0x400000;
constexpr u32 SPIN_CAP = 1u << 17;
constexpr int L_Q   = 0;
constexpr int L_T   = 1024;
constexpr int L_ML  = 8704;
constexpr int L_O   = 9216;
constexpr int L_PF  = 16384;
constexpr int LDS_BYTES = L_PF + 4 * 32768;

struct Params {
    const void* x; const void* k; const void* v; const void* wq; const void* wo;
    const void* w1; const void* b1; const void* w2; const void* b2;
    void* out; void* ws;
};

__device__ __forceinline__ u32x4 ldnt(gcchar* p) { return __builtin_nontemporal_load((gcu32x4*)p); }
__device__ __forceinline__ float bflo(u32 w) { return __uint_as_float(w << 16); }
__device__ __forceinline__ float bfhi(u32 w) { return __uint_as_float(w & 0xffff0000u); }
__device__ __forceinline__ u32 pack2(float lo, float hi) {
    f32x2 v = {lo, hi};
    return __builtin_bit_cast(u32, __builtin_convertvector(v, bf16x2));
}
__device__ __forceinline__ float bfr(float x) { return bflo(pack2(x, 0.f)); }
__device__ __forceinline__ f32x4 mfma(u32x4 a, u32x4 b, f32x4 c) {
    return __builtin_amdgcn_mfma_f32_16x16x32_bf16(__builtin_bit_cast(bf16x8, a), __builtin_bit_cast(bf16x8, b), c, 0, 0, 0);
}
__device__ __forceinline__ float dot2(u32 a, u32 b, float c) {
    return __builtin_amdgcn_fdot2_f32_bf16(__builtin_bit_cast(bf16x2, a), __builtin_bit_cast(bf16x2, b), c, false);
}
template <int CTRL> __device__ __forceinline__ u32 dppu(u32 x) { return (u32)__builtin_amdgcn_mov_dpp((int)x, CTRL, 0xf, 0xf, true); }
template <int CTRL> __device__ __forceinline__ float dppf(float x) { return __uint_as_float(dppu<CTRL>(__float_as_uint(x))); }
constexpr int XOR1 = 0xB1, XOR2 = 0x4E, HMIR = 0x141, ROR4 = 0x124, ROR8 = 0x128, EVEN = 0xA0, ODD = 0xF5;
__device__ __forceinline__ float xrow_max(float x) {
    auto s = __builtin_amdgcn_permlane16_swap(__float_as_uint(x), __float_as_uint(x), false, false);
    x = fmaxf(__uint_as_float(s[0]), __uint_as_float(s[1]));
    auto t = __builtin_amdgcn_permlane32_swap(__float_as_uint(x), __float_as_uint(x), false, false);
    return fmaxf(__uint_as_float(t[0]), __uint_as_float(t[1]));
}
__device__ __forceinline__ float xrow_sum(float x) {
    auto s = __builtin_amdgcn_permlane16_swap(__float_as_uint(x), __float_as_uint(x), false, false);
    x = __uint_as_float(s[0]) + __uint_as_float(s[1]);
    auto t = __builtin_amdgcn_permlane32_swap(__float_as_uint(x), __float_as_uint(x), false, false);
    return __uint_as_float(t[0]) + __uint_as_float(t[1]);
}
__device__ __forceinline__ float x32_max(float x) {
    auto t = __builtin_amdgcn_permlane32_swap(__float_as_uint(x), __float_as_uint(x), false, false);
    return fmaxf(__uint_as_float(t[0]), __uint_as_float(t[1]));
}
__device__ __forceinline__ float x32_sum(float x) {
    auto t = __builtin_amdgcn_permlane32_swap(__float_as_uint(x), __float_as_uint(x), false, false);
    return __uint_as_float(t[0]) + __uint_as_float(t[1]);
}
__device__ __forceinline__ float wave_sum(float x) {
    x += dppf<XOR1>(x); x += dppf<XOR2>(x); x += dppf<ROR4>(x); x += dppf<ROR8>(x);
    return xrow_sum(x);
}
__device__ __forceinline__ void wg_barrier() { asm volatile("s_waitcnt lgkmcnt(0)\n\ts_barrier" ::: "memory"); }
__device__ __forceinline__ void raw_barrier() { asm volatile("s_barrier" ::: "memory"); }
constexpr u32 OOB = 0x40000000u;

__device__ __forceinline__ u32x4 mk_unit(u32 v0, u32 v1, u32 T) { u32x4 u = {v0, T ^ v1, v1, T ^ v0}; return u; }
__device__ __forceinline__ bool unit_ok(u32x4 u, u32 T) { return ((u.y ^ u.z) == T) & ((u.w ^ u.x) == T); }
__device__ __forceinline__ void st_unit(__amdgpu_buffer_rsrc_t rs, u32 off, u32x4 u) { __builtin_amdgcn_raw_buffer_store_b128(u, rs, off, 0, 16); }
__device__ __forceinline__ u32x4 ld_unit(__amdgpu_buffer_rsrc_t rs, u32 off) { return __builtin_amdgcn_raw_buffer_load_b128(rs, off, 0, 16); }

template <int DD> __device__ __forceinline__ u32x4 tfrag(const u32x4* v) {
    constexpr u32 sel = (DD & 1) ? 0x07060302u : 0x05040100u;
    u32x4 a;
    a.x = __builtin_amdgcn_perm(v[1][DD >> 1], v[0][DD >> 1], sel);
    a.y = __builtin_amdgcn_perm(v[3][DD >> 1], v[2][DD >> 1], sel);
    a.z = __builtin_amdgcn_perm(v[5][DD >> 1], v[4][DD >> 1], sel);
    a.w = __builtin_amdgcn_perm(v[7][DD >> 1], v[6][DD >> 1], sel);
    return a;
}
__device__ __forceinline__ void pv_tile(const u32x4* v, u32x4 p, f32x4 (&acc)[8]) {
    acc[0] = mfma(tfrag<0>(v), p, acc[0]); acc[1] = mfma(tfrag<1>(v), p, acc[1]);
    acc[2] = mfma(tfrag<2>(v), p, acc[2]); acc[3] = mfma(tfrag<3>(v), p, acc[3]);
    acc[4] = mfma(tfrag<4>(v), p, acc[4]); acc[5] = mfma(tfrag<5>(v), p, acc[5]);
    acc[6] = mfma(tfrag<6>(v), p, acc[6]); acc[7] = mfma(tfrag<7>(v), p, acc[7]);
}
template <int NSLOT>
__device__ __forceinline__ void publish_cols(__amdgpu_buffer_rsrc_t rs, u32 base, const f32x4 (&acc)[8], int slot, int ng0, int lane, u32 T) {
    const int g = lane >> 4, b = (lane >> 2) & 3, rho = lane & 3;
    const u32 off0 = base + (u32)(((b * 64 + ng0 + 4 * g) * NSLOT + slot) * 64 + rho * 16);
#pragma unroll
    for (int r = 0; r < 4; ++r) {
        float a0 = (rho & 2) ? acc[4][r] : acc[0][r], a1 = (rho & 2) ? acc[6][r] : acc[2][r];
        float c0 = (rho & 2) ? acc[5][r] : acc[1][r], c1 = (rho & 2) ? acc[7][r] : acc[3][r];
        float v0 = (rho & 1) ? a1 : a0, v1 = (rho & 1) ? c1 : c0;
        st_unit(rs, off0 + (u32)(r * NSLOT * 64), mk_unit(__float_as_uint(v0), __float_as_uint(v1), T));
    }
}

#define STAMP(i) do { } while (0)
#define STAMPV(i, v) do { } while (0)
#define FLUSH_STAMPS() do { } while (0)
#define DBG(x)

struct Ring { u32x4 r[16]; };
typedef __amdgpu_buffer_rsrc_t rsrc_t;
constexpr int NT = 2;
__device__ __forceinline__ u32x4 ldb_nt(rsrc_t rs, u32 voff, u32 soff) { return __builtin_amdgcn_raw_buffer_load_b128(rs, voff, soff, NT); }
#define STR2(x) #x
#define STR(x) STR2(x)
#define LPACE_N 7
#define LPACE() asm volatile("s_waitcnt vmcnt(" STR(LPACE_N) ")" ::: "memory")
constexpr u32 TSTR = 4 * 8192;
__device__ __forceinline__ constexpr u32 kimm(int i) { return (u32)((i >> 2) * 4096 + (i & 3) * 64); }
__device__ __forceinline__ void issue_k(Ring& R, rsrc_t ks, u32 koff, u32 toff) {
#pragma unroll
    for (int i = 0; i < 8; ++i) { LPACE(); R.r[i] = ldb_nt(ks, koff + (i & 3) * 64, toff + (i >> 2) * 4096); }
}
__device__ __forceinline__ void issue_v(Ring& R, rsrc_t vs, u32 voff, u32 toff) {
#pragma unroll
    for (int i = 0; i < 8; ++i) { LPACE(); R.r[8 + i] = ldb_nt(vs, voff + (i & 3) * 1024, toff + (i >> 2) * 4096); }
}
__device__ __forceinline__ void dma16(u32 ldst, u32 voff, u32x4 srd, u32 soff) {
    u32 keep;
    asm volatile("s_mov_b32 %0, m0\n\ts_mov_b32 m0, %1\n\ts_nop 0\n\tbuffer_load_dwordx4 %2, %3, %4 offen nt lds\n\ts_mov_b32 m0, %0"
                 : "=&s"(keep) : "s"(ldst), "v"(voff), "s"(srd), "s"(soff) : "memory");
}
__device__ __forceinline__ u32x4 mk_srd(const void* base, u32 bytes) {
    const uint64_t a = (uint64_t)base;
    u32x4 d = {(u32)__builtin_amdgcn_readfirstlane((u32)a), (u32)__builtin_amdgcn_readfirstlane((u32)(a >> 32) & 0xffffu), bytes, 0x00020000u};
    return d;
}
#define SB() __builtin_amdgcn_sched_barrier(0)
#define PACE_N 7
#define PACE0 15
#define B0() raw_barrier()
#define MIDPRIO 3
#define SVC_PRIO_MID() __builtin_amdgcn_s_setprio(MIDPRIO)
#define SVC_PRIO_END() __builtin_amdgcn_s_setprio(3)
#define PACE() asm volatile("s_waitcnt vmcnt(" STR(PACE_N) ")" ::: "memory")

struct AttnState { f32x4 acc[8]; float m, l; };

__device__ __forceinline__ void qk_tile(const u32x4* k, const u32x4 (&qf)[4], f32x4& s0, f32x4& s1) {
    s0 = f32x4{0.f, 0.f, 0.f, 0.f}; s1 = f32x4{0.f, 0.f, 0.f, 0.f};
#pragma unroll
    for (int st = 0; st < 4; ++st) { s0 = mfma(k[st], qf[st], s0); s1 = mfma(k[4 + st], qf[st], s1); }
}
__device__ __forceinline__ u32x4 softmax_step(const f32x4 s0, const f32x4 s1, AttnState& S) {
    constexpr float SCL = 0.08838834764831845f, LOG2E = 1.4426950408889634f;
    float z[8];
#pragma unroll
    for (int j = 0; j < 2; ++j) {
        const u32 a = pack2(s0[2 * j], s0[2 * j + 1]), c = pack2(s1[2 * j], s1[2 * j + 1]);
        const u32 a2 = pack2(bflo(a) * SCL, bfhi(a) * SCL), c2 = pack2(bflo(c) * SCL, bfhi(c) * SCL);
        z[2 * j] = bflo(a2); z[2 * j + 1] = bfhi(a2); z[4 + 2 * j] = bflo(c2); z[4 + 2 * j + 1] = bfhi(c2);
    }
    float mx = fmaxf(fmaxf(fmaxf(z[0], z[1]), fmaxf(z[2], z[3])), fmaxf(fmaxf(z[4], z[5]), fmaxf(z[6], z[7])));
    mx = xrow_max(mx) * LOG2E;
    if (__builtin_amdgcn_ballot_w64(mx > S.m) != 0ull) {
        const float mn = fmaxf(S.m, mx);
        const float al = __builtin_amdgcn_exp2f(S.m - mn);
        S.m = mn; S.l *= al;
#pragma unroll
        for (int d = 0; d < 8; ++d) S.acc[d] *= al;
    }
    float p[8];
#pragma unroll
    for (int j = 0; j < 8; ++j) p[j] = __builtin_amdgcn_exp2f(__builtin_fmaf(z[j], LOG2E, -S.m));
    S.l += ((p[0] + p[1]) + (p[2] + p[3])) + ((p[4] + p[5]) + (p[6] + p[7]));
    u32x4 pf = {pack2(p[0], p[1]), pack2(p[2], p[3]), pack2(p[4], p[5]), pack2(p[6], p[7])};
    return pf;
}
__device__ __forceinline__ void proc_tile(Ring& R, AttnState& S, const u32x4 (&qf)[4], rsrc_t ks, rsrc_t vs, u32 koff, u32 voff, u32 toff) {
    f32x4 s0, s1;
    qk_tile(&R.r[0], qf, s0, s1);
    SB();
    issue_k(R, ks, koff, toff);
    SB();
    const u32x4 pf = softmax_step(s0, s1, S);
    pv_tile(&R.r[8], pf, S.acc);
    SB();
    issue_v(R, vs, voff, toff);
    SB();
}

__global__ void __launch_bounds__(320, 2) fused_decode_kernel(Params P) {
    __shared__ __attribute__((aligned(16))) char lds[LDS_BYTES];
    const int tid = threadIdx.x, lane = tid & 63;
    const int wave = __builtin_amdgcn_readfirstlane(tid >> 6);
    const int j = blockIdx.x;
    const int hkv = j & 7, pidx = j >> 3;
    const int b = pidx >> 3, split = pidx & 7;
    const int grp = b * 8 + hkv;
    const bool has_h = pidx < NHB;
    const int f0 = 8 * (hkv * NHB + pidx);
    const __amdgpu_buffer_rsrc_t rs = __builtin_amdgcn_make_buffer_rsrc(P.ws, 0, WS_END, 0x00020000);
    const int g = lane >> 4, n16 = lane & 15;
    const int oh = hkv * 4 + (pidx & 3), okh = (pidx >> 2) & 1, ons = pidx >> 3;

    DBG(unsigned long long ts[NSTAMP]; for (int z = 0; z < NSTAMP; ++z) ts[z] = 0;)
    if (wave < 4) {
        STAMP(0);
        B0();
        STAMP(12);
        const size_t tb = (size_t)grp * (SK * DH * 2) + (size_t)(64 * split + wave) * 8192;
        constexpr u32 TRANGE = 15 * TSTR + 8192;
        const rsrc_t ks = __builtin_amdgcn_make_buffer_rsrc((char*)P.k + tb, 0, TRANGE, 0x00020000);
        const rsrc_t vs = __builtin_amdgcn_make_buffer_rsrc((char*)P.v + tb, 0, TRANGE, 0x00020000);
        const u32 koff = (u32)((4 * (n16 & 3) + (n16 >> 2)) * 256 + g * 16);
        const u32 voff = (u32)(lane * 16);
        char* const pf_base = lds + L_PF + wave * 32768;
        {
            const u32x4 ksd = mk_srd((const char*)P.k + tb, TRANGE), vsd = mk_srd((const char*)P.v + tb, TRANGE);
            const u32 lb = (u32)__builtin_amdgcn_readfirstlane((u32)(uintptr_t)(__attribute__((address_space(3))) char*)pf_base);
#pragma unroll
            for (int t = 0; t < 2; ++t) {
#pragma unroll
                for (int i = 0; i < 8; ++i) { if (16 * t + i > PACE0) PACE(); dma16(lb + t * 16384 + i * 1024, koff, ksd, t * TSTR + kimm(i)); }
#pragma unroll
                for (int i = 0; i < 8; ++i) { if (16 * t + 8 + i > PACE0) PACE(); dma16(lb + t * 16384 + 8192 + i * 1024, voff, vsd, t * TSTR + i * 1024); }
                DBG(if (t == 0) STAMP(13); else STAMP(14);)
            }
        }
        Ring A, B;
        AttnState S;
#pragma unroll
        for (int i = 0; i < 8; ++i) { PACE(); A.r[i] = ldb_nt(ks, koff + (i & 3) * 64, 2 * TSTR + (i >> 2) * 4096); }
#pragma unroll
        for (int i = 0; i < 8; ++i) { PACE(); A.r[8 + i] = ldb_nt(vs, voff + (i & 3) * 1024, 2 * TSTR + (i >> 2) * 4096); }
#pragma unroll
        for (int i = 0; i < 8; ++i) { PACE(); DBG(if (i == 0) STAMP(15);) B.r[i] = ldb_nt(ks, koff + (i & 3) * 64, 3 * TSTR + (i >> 2) * 4096); }
#pragma unroll
        for (int i = 0; i < 8; ++i) { PACE(); B.r[8 + i] = ldb_nt(vs, voff + (i & 3) * 1024, 3 * TSTR + (i >> 2) * 4096); }
        asm volatile("s_waitcnt vmcnt(32)" ::: "memory");
        STAMP(1);
        wg_barrier();
        STAMP(2);
        u32x4 qf[4];
#pragma unroll
        for (int st = 0; st < 4; ++st) qf[st] = *(const u32x4*)(lds + L_Q + (n16 & 3) * 256 + st * 64 + g * 16);
        const u32 T = *(const u32*)(lds + L_T);
#pragma unroll
        for (int d = 0; d < 8; ++d) S.acc[d] = f32x4{0.f, 0.f, 0.f, 0.f};
        S.m = -INFINITY; S.l = 0.f;
#pragma unroll 1
        for (int t = 0; t < 2; ++t) {
            const char* sl = pf_base + t * 16384 + lane * 16;
            u32x4 c[8];
#pragma unroll
            for (int i = 0; i < 8; ++i) c[i] = *(const u32x4*)(sl + i * 1024);
            f32x4 s0, s1;
            qk_tile(c, qf, s0, s1);
            SB();
#pragma unroll
            for (int i = 0; i < 8; ++i) c[i] = *(const u32x4*)(sl + 8192 + i * 1024);
            const u32x4 pf = softmax_step(s0, s1, S);
            pv_tile(c, pf, S.acc);
        }
        STAMP(3);
#pragma unroll 1
        for (int i = 0; i < 7; ++i) {
            const u32 toff = (u32)(4 + 2 * i) * TSTR;
            proc_tile(A, S, qf, ks, vs, koff, voff, toff);
            proc_tile(B, S, qf, ks, vs, koff, voff, toff + TSTR);
            DBG(if (i == 0) STAMP(4); if (i == 1) STAMP(5); if (i == 2) STAMP(6); if (i == 4) STAMP(7); if (i == 5) STAMP(8);)
        }
        STAMP(9);
        const float lw = xrow_sum(S.l);
        if (n16 < 4) {
            float* mo = (float*)pf_base + n16 * 144 + g * 36;
#pragma unroll
            for (int r = 0; r < 4; ++r) {
                *(f32x4*)(mo + 8 * r) = f32x4{S.acc[0][r], S.acc[1][r], S.acc[2][r], S.acc[3][r]};
                *(f32x4*)(mo + 8 * r + 4) = f32x4{S.acc[4][r], S.acc[5][r], S.acc[6][r], S.acc[7][r]};
            }
            if (g == 0) *(f32x2*)(lds + L_ML + (wave * 4 + n16) * 8) = f32x2{S.m, lw};
        }
        wg_barrier();
        {
            f32x2 ml[4], ov[4];
#pragma unroll
            for (int w = 0; w < 4; ++w) {
                ml[w] = *(const f32x2*)(lds + L_ML + (w * 4 + wave) * 8);
                ov[w] = *(const f32x2*)((const float*)(lds + L_PF + w * 32768) + wave * 144 + (lane >> 4) * 36 + 2 * (lane & 15));
            }
            const float M = fmaxf(fmaxf(ml[0].x, ml[1].x), fmaxf(ml[2].x, ml[3].x));
            float L = 0.f, o0 = 0.f, o1 = 0.f;
#pragma unroll
            for (int w = 0; w < 4; ++w) {
                const float a = __builtin_amdgcn_exp2f(ml[w].x - M);
                L += a * ml[w].y; o0 += a * ov[w].x; o1 += a * ov[w].y;
            }
            const float inv = 1.0f / L;
            const u32 blk = (u32)((b * 32 + hkv * 4 + wave) * 8 + split);
            st_unit(rs, WS_PO + blk * 1024 + lane * 16, mk_unit(__float_as_uint(o0 * inv), __float_as_uint(o1 * inv), T));
            if (lane == 0) {
                const float lse = M + __builtin_amdgcn_logf(L);
                st_unit(rs, WS_PL + blk * 16, mk_unit(__float_as_uint(lse), 0u, T));
            }
        }
        STAMP(10);
        {
            const int half = lane >> 5;
            const u32 blk0 = (u32)((wave * 32 + oh) * 8);
            const u32 ooff = WS_PO + (blk0 + half) * 1024 + (u32)((32 * okh + (lane & 31)) * 16);
            const u32 loff = lane < 8 ? WS_PL + (blk0 + lane) * 16 : OOB;
            u32x4 uo[4], ul;
            u32 want = 0, have = 0;
            u32 lseen = 0;
            for (u32 spin = 0; spin < SPIN_CAP; ++spin) {
                asm volatile("" ::: "memory");
                u32x4 t[4];
                ul = ld_unit(rs, loff);
#pragma unroll
                for (int r = 0; r < 4; ++r) t[r] = ld_unit(rs, ((want >> r) & 1u) ? ooff + r * 2048 : OOB);
#pragma unroll
                for (int r = 0; r < 4; ++r)
                    if (((want >> r) & 1u) && unit_ok(t[r], T)) { uo[r] = t[r]; have |= 1u << r; }
                lseen = (u32)__builtin_amdgcn_ballot_w64(unit_ok(ul, T)) & 0xffu;
                const bool done = lseen == 0xffu && __builtin_amdgcn_ballot_w64(have != 0xfu) == 0ull;
                if (done) break;
                const u32 gate = (__builtin_popcount(lseen) >= 6) ? 0xffu : lseen;
                const u32 gl = gate >> half;
                want = ((gl & 1u) | ((gl >> 1) & 2u) | ((gl >> 2) & 4u) | ((gl >> 3) & 8u)) & ~have;
                if (__builtin_amdgcn_ballot_w64(want != 0u) == 0ull || lseen != 0xffu) __builtin_amdgcn_s_sleep(2);
            }
            const float lse = __uint_as_float(ul.x);
            float M = fmaxf(lse, dppf<XOR1>(lse)); M = fmaxf(M, dppf<XOR2>(M)); M = fmaxf(M, dppf<HMIR>(M));
            const float al = __builtin_amdgcn_exp2f(lse - M);
            float W = al + dppf<XOR1>(al); W += dppf<XOR2>(W); W += dppf<HMIR>(W);
            const float inv = 1.0f / W;
            const u32 aw = __float_as_uint(al * inv);
#define WSEL(r) __uint_as_float(half ? __builtin_amdgcn_readlane(aw, 2 * (r) + 1) : __builtin_amdgcn_readlane(aw, 2 * (r)))
            const float a0 = WSEL(0), a1 = WSEL(1), a2 = WSEL(2), a3 = WSEL(3);
#undef WSEL
            float o0 = (a0 * __uint_as_float(uo[0].x) + a1 * __uint_as_float(uo[1].x)) + (a2 * __uint_as_float(uo[2].x) + a3 * __uint_as_float(uo[3].x));
            float o1 = (a0 * __uint_as_float(uo[0].z) + a1 * __uint_as_float(uo[1].z)) + (a2 * __uint_as_float(uo[2].z) + a3 * __uint_as_float(uo[3].z));
            o0 = x32_sum(o0); o1 = x32_sum(o1);
            if (lane < 32) *(u32*)(lds + L_O + wave * 128 + lane * 4) = pack2(o0, o1);
        }
        STAMP(11);
        wg_barrier();
        FLUSH_STAMPS();
    } else {
        STAMP(0);
        __builtin_amdgcn_s_setprio(3);
        u32x4 xr[4], wr[16];
        const u32 epoch = *(const GAS u32*)((gcchar*)P.ws + WS_EPOCH);
#pragma unroll
        for (int bb = 0; bb < 4; ++bb) xr[bb] = *(gcu32x4*)((gcchar*)P.x + bb * 1024 + lane * 16);
        {
            gcchar* wqb = (gcchar*)P.wq + (hkv * 512 + 16 * pidx) * 2;
            const u32 wqoff = (u32)((lane >> 1) * 16 * 8192 + (lane & 1) * 16);
#pragma unroll
            for (int i = 0; i < 16; ++i) wr[i] = ldnt(wqb + wqoff + i * 8192);
        }
        SB();
        B0();
        SB();
        STAMP(12);
        DBG(asm volatile("s_waitcnt vmcnt(16)" ::: "memory"); STAMP(8);)
        u32 T = epoch + 1u; if (T == 0u) T = 1u;
        T = __builtin_amdgcn_readfirstlane(T);
        u32 xn[4][4];
#pragma unroll
        for (int bb = 0; bb < 4; ++bb) {
            float v[8];
#pragma unroll
            for (int i = 0; i < 4; ++i) { v[2 * i] = bflo(xr[bb][i]); v[2 * i + 1] = bfhi(xr[bb][i]); }
            float s = ((v[0] + v[1]) + (v[2] + v[3])) + ((v[4] + v[5]) + (v[6] + v[7]));
            const float mean = wave_sum(s) * (1.0f / 512.0f);
            float q = 0.f;
#pragma unroll
            for (int i = 0; i < 8; ++i) { const float d = v[i] - mean; q += d * d; }
            const float var = bfr(wave_sum(q) * (1.0f / 512.0f));
            const float mb = bfr(mean);
            const float den = bfr(__builtin_sqrtf(bfr(var + 1e-5f)));
#pragma unroll
            for (int i = 0; i < 4; ++i) xn[bb][i] = pack2(bfr(v[2 * i] - mb) / den, bfr(v[2 * i + 1] - mb) / den);
        }
        if (lane == 0) *(u32*)(lds + L_T) = T;
        STAMP(1);
        DBG(asm volatile("s_waitcnt vmcnt(0)" ::: "memory"); STAMP(9);)
        {
            float acc[4][8];
#pragma unroll
            for (int bb = 0; bb < 4; ++bb)
#pragma unroll
                for (int c = 0; c < 8; ++c) acc[bb][c] = 0.f;
#pragma unroll
            for (int a = 0; a < 8; ++a) {
                u32 xp[4];
#pragma unroll
                for (int bb = 0; bb < 4; ++bb) xp[bb] = (a < 4) ? dppu<EVEN>(xn[bb][a & 3]) : dppu<ODD>(xn[bb][a & 3]);
#pragma unroll
                for (int c = 0; c < 8; ++c) {
                    const u32 wp = __builtin_amdgcn_perm(wr[2 * a + 1][c >> 1], wr[2 * a][c >> 1], (c & 1) ? 0x07060302u : 0x05040100u);
#pragma unroll
                    for (int bb = 0; bb < 4; ++bb) acc[bb][c] = dot2(wp, xp[bb], acc[bb][c]);
                }
            }
            float r1[2][8], r2[8];
#pragma unroll
            for (int bb = 0; bb < 2; ++bb)
#pragma unroll
                for (int c = 0; c < 8; ++c) {
                    auto t = __builtin_amdgcn_permlane32_swap(__float_as_uint(acc[bb][c]), __float_as_uint(acc[bb + 2][c]), false, false);
                    r1[bb][c] = __uint_as_float(t[0]) + __uint_as_float(t[1]);
                }
#pragma unroll
            for (int c = 0; c < 8; ++c) {
                auto t = __builtin_amdgcn_permlane16_swap(__float_as_uint(r1[0][c]), __float_as_uint(r1[1][c]), false, false);
                float y = __uint_as_float(t[0]) + __uint_as_float(t[1]);
                y += dppf<XOR2>(y); y += dppf<ROR4>(y); y += dppf<ROR8>(y);
                r2[c] = y;
            }
            const bool sub = (n16 >> 1) & 1;
            const u32 v0 = sub ? pack2(r2[4], r2[5]) : pack2(r2[0], r2[1]);
            const u32 v1 = sub ? pack2(r2[6], r2[7]) : pack2(r2[2], r2[3]);
            if (n16 < 4) {
                const int u = 2 * (n16 & 1) + (n16 >> 1);
                st_unit(rs, WS_Q + (u32)((g * 8 + hkv) * 2048 + (4 * pidx + u) * 16), mk_unit(v0, v1, T));
            }
        }
        STAMP(2);
        {
            u32x4 u0, u1;
            const u32 off = WS_Q + (u32)(grp * 2048 + lane * 16);
            for (u32 spin = 0; spin < SPIN_CAP; ++spin) {
                asm volatile("" ::: "memory");
                u0 = ld_unit(rs, off); u1 = ld_unit(rs, off + 1024);
                if (__builtin_amdgcn_ballot_w64(!(unit_ok(u0, T) & unit_ok(u1, T))) == 0ull) { STAMPV(11, 1000000ull + spin); break; }
                __builtin_amdgcn_s_sleep(1);
            }
            *(u32x2*)(lds + L_Q + lane * 8) = u32x2{u0.x, u0.z};
            *(u32x2*)(lds + L_Q + 512 + lane * 8) = u32x2{u1.x, u1.z};
        }
        STAMP(3);
        wg_barrier();
        SVC_PRIO_MID();
        u32x4 w1r[8];
        u32x4 b1w;
        {
            const rsrc_t w1s = __builtin_amdgcn_make_buffer_rsrc((void*)P.w1, 0, DM * FF * 2, 0x00020000);
            const u32 w1off = has_h ? (u32)((8 * lane * FF + f0) * 2) : OOB;
#define W1STEP (FF * 2)
#pragma unroll
            for (int i = 0; i < 8; ++i) w1r[i] = __builtin_amdgcn_raw_buffer_load_b128(w1s, w1off, (u32)(i * W1STEP), 0);
            const rsrc_t b1s = __builtin_amdgcn_make_buffer_rsrc((void*)P.b1, 0, FF * 2, 0x00020000);
            b1w = __builtin_amdgcn_raw_buffer_load_b128(b1s, has_h ? (u32)(f0 * 2) : OOB, 0, 0);
        }
        const int rb2 = (hkv * NHB + pidx) >> 2, cb2 = pidx & 3;
        u32x4 w2r[8];
        {
            const rsrc_t w2s = __builtin_amdgcn_make_buffer_rsrc((void*)P.w2, 0, DM * FF * 2, 0x00020000);
            const u32 w2off = has_h ? (u32)((32 * rb2 + 8 * g) * 1024 + cb2 * 256 + n16 * 16) : OOB;
#pragma unroll
            for (int i = 0; i < 8; ++i) w2r[i] = ldb_nt(w2s, w2off, (u32)(i * 1024));
        }
        if (has_h) {
            float acc[4][8];
#pragma unroll
            for (int bb = 0; bb < 4; ++bb)
#pragma unroll
                for (int c = 0; c < 8; ++c) acc[bb][c] = 0.f;
#pragma unroll
            for (int a = 0; a < 4; ++a)
#pragma unroll
                for (int c = 0; c < 8; ++c) {
                    const u32 wp = __builtin_amdgcn_perm(w1r[2 * a + 1][c >> 1], w1r[2 * a][c >> 1], (c & 1) ? 0x07060302u : 0x05040100u);
#pragma unroll
                    for (int bb = 0; bb < 4; ++bb) acc[bb][c] = dot2(wp, xn[bb][a], acc[bb][c]);
                }
            float r1[2][8], hy[8];
#pragma unroll
            for (int bb = 0; bb < 2; ++bb)
#pragma unroll
                for (int c = 0; c < 8; ++c) {
                    auto t = __builtin_amdgcn_permlane32_swap(__float_as_uint(acc[bb][c]), __float_as_uint(acc[bb + 2][c]), false, false);
                    r1[bb][c] = __uint_as_float(t[0]) + __uint_as_float(t[1]);
                }
#pragma unroll
            for (int c = 0; c < 8; ++c) {
                auto t = __builtin_amdgcn_permlane16_swap(__float_as_uint(r1[0][c]), __float_as_uint(r1[1][c]), false, false);
                float y = __uint_as_float(t[0]) + __uint_as_float(t[1]);
                y += dppf<XOR1>(y); y += dppf<XOR2>(y); y += dppf<ROR4>(y); y += dppf<ROR8>(y);
                hy[c] = y;
            }
            u32 hp[4];
#pragma unroll
            for (int c = 0; c < 4; ++c) {
                float h0 = bfr(bfr(hy[2 * c]) + bflo(b1w[c])), h1 = bfr(bfr(hy[2 * c + 1]) + bfhi(b1w[c]));
                h0 = h0 > 0.f ? h0 : 0.f; h1 = h1 > 0.f ? h1 : 0.f;
                hp[c] = pack2(h0 * h0, h1 * h1);
            }
            const bool hi = lane & 1;
            if (n16 < 2)
                st_unit(rs, WS_H + (u32)((g * 448 + (f0 >> 2) + n16) * 16), mk_unit(hi ? hp[2] : hp[0], hi ? hp[3] : hp[1], T));
        }
        STAMP(13);
        if (has_h) {
            const u32 off = WS_H + (u32)(((n16 >> 2) * 448 + 8 * rb2 + 2 * g) * 16);
            u32x4 u0, u1;
            for (u32 spin = 0; spin < SPIN_CAP; ++spin) {
                asm volatile("" ::: "memory");
                u0 = ld_unit(rs, off); u1 = ld_unit(rs, off + 16);
                if (__builtin_amdgcn_ballot_w64(!(unit_ok(u0, T) & unit_ok(u1, T))) == 0ull) break;
                __builtin_amdgcn_s_sleep(16);
            }
            f32x4 acc[8];
#pragma unroll
            for (int d = 0; d < 8; ++d) acc[d] = f32x4{0.f, 0.f, 0.f, 0.f};
            pv_tile(w2r, u32x4{u0.x, u0.z, u1.x, u1.z}, acc);
            publish_cols<64>(rs, WS_MP, acc, rb2, 16 * cb2, lane, T);
        }
        STAMP(10);
        u32x4 wa[2][8];
        {
            u32x4 wo[16];
            gcchar* wob = (gcchar*)P.wo + (size_t)(oh * 128 + 64 * okh) * 1024 + 256 * ons;
            const u32 wooff = (u32)(8 * g * 1024 + n16 * 16);
#pragma unroll
            for (int i = 0; i < 16; ++i) wo[i] = ldnt(wob + wooff + ((i >> 3) * 32 + (i & 7)) * 1024);
#pragma unroll
            for (int tau = 0; tau < 2; ++tau) {
                wa[tau][0] = tfrag<0>(&wo[8 * tau]); wa[tau][1] = tfrag<1>(&wo[8 * tau]); wa[tau][2] = tfrag<2>(&wo[8 * tau]); wa[tau][3] = tfrag<3>(&wo[8 * tau]);
                wa[tau][4] = tfrag<4>(&wo[8 * tau]); wa[tau][5] = tfrag<5>(&wo[8 * tau]); wa[tau][6] = tfrag<6>(&wo[8 * tau]); wa[tau][7] = tfrag<7>(&wo[8 * tau]);
            }
        }
        const int fb = j >> 6, fng = j & 63;
        const int fu = lane & 3, fi = lane >> 2;
        const u32 xw = *(const GAS u32*)((gcchar*)P.x + fb * 1024 + fng * 16 + fu * 4);
        const u32 b2w = *(const GAS u32*)((gcchar*)P.b2 + fng * 16 + fu * 4);
        float g0, g1;
        {
            const u32 off = WS_MP + (u32)(((fb * 64 + fng) * 64 + fi) * 64 + fu * 16);
            const bool has3 = fi + 48 < NSL2;
            u32x4 u0, u1, u2, u3;
            for (u32 spin = 0; spin < SPIN_CAP; ++spin) {
                asm volatile("" ::: "memory");
                u0 = ld_unit(rs, off); u1 = ld_unit(rs, off + 1024); u2 = ld_unit(rs, off + 2048); u3 = ld_unit(rs, has3 ? off + 3072 : OOB);
                if (__builtin_amdgcn_ballot_w64(!(unit_ok(u0, T) & unit_ok(u1, T) & unit_ok(u2, T) & (unit_ok(u3, T) | !has3))) == 0ull) break;
                __builtin_amdgcn_s_sleep(64);
            }
            float a0 = (__uint_as_float(u0.x) + __uint_as_float(u1.x)) + (__uint_as_float(u2.x) + (has3 ? __uint_as_float(u3.x) : 0.f));
            float a1 = (__uint_as_float(u0.z) + __uint_as_float(u1.z)) + (__uint_as_float(u2.z) + (has3 ? __uint_as_float(u3.z) : 0.f));
            a0 += dppf<ROR4>(a0); a0 += dppf<ROR8>(a0); a0 = xrow_sum(a0);
            a1 += dppf<ROR4>(a1); a1 += dppf<ROR8>(a1); a1 = xrow_sum(a1);
            const float y0 = bfr(bfr(a0) + bflo(b2w)), y1 = bfr(bfr(a1) + bfhi(b2w));
            g0 = bfr(y0 / (1.0f + __expf(-y0)));
            g1 = bfr(y1 / (1.0f + __expf(-y1)));
        }
        SVC_PRIO_END();
        STAMP(4);
        wg_barrier();
        wg_barrier();
        STAMP(5);
        {
            f32x4 acc[8];
#pragma unroll
            for (int d = 0; d < 8; ++d) acc[d] = f32x4{0.f, 0.f, 0.f, 0.f};
#pragma unroll
            for (int tau = 0; tau < 2; ++tau) {
                const u32x4 of = *(const u32x4*)(lds + L_O + (n16 >> 2) * 128 + (32 * tau + 8 * g) * 2);
#pragma unroll
                for (int d = 0; d < 8; ++d) acc[d] = mfma(wa[tau][d], of, acc[d]);
            }
            publish_cols<64>(rs, WS_SP, acc, oh * 2 + okh, 16 * ons, lane, T);
        }
        STAMP(6);
        {
            const u32 off = WS_SP + (u32)(((fb * 64 + fng) * 64 + fi) * 64 + fu * 16);
            u32x4 u[4];
            u32 have = 0;
            for (u32 spin = 0; spin < SPIN_CAP; ++spin) {
                asm volatile("" ::: "memory");
                u32x4 t[4];
#pragma unroll
                for (int r = 0; r < 4; ++r) t[r] = ld_unit(rs, ((have >> r) & 1u) ? OOB : off + r * 1024);
#pragma unroll
                for (int r = 0; r < 4; ++r)
                    if (!((have >> r) & 1u) && unit_ok(t[r], T)) { u[r] = t[r]; have |= 1u << r; }
                if (__builtin_amdgcn_ballot_w64(have != 0xfu) == 0ull) break;
                __builtin_amdgcn_s_sleep(1);
            }
            float a0 = (__uint_as_float(u[0].x) + __uint_as_float(u[1].x)) + (__uint_as_float(u[2].x) + __uint_as_float(u[3].x));
            float a1 = (__uint_as_float(u[0].z) + __uint_as_float(u[1].z)) + (__uint_as_float(u[2].z) + __uint_as_float(u[3].z));
            a0 += dppf<ROR4>(a0); a0 += dppf<ROR8>(a0); a0 = xrow_sum(a0);
            a1 += dppf<ROR4>(a1); a1 += dppf<ROR8>(a1); a1 = xrow_sum(a1);
            const float r0 = bflo(xw) + bfr(bfr(a0) * g0), r1 = bfhi(xw) + bfr(bfr(a1) * g1);
            if (lane < 4) *(GAS u32*)((GAS char*)P.out + fb * 1024 + fng * 16 + fu * 4) = pack2(r0, r1);
            if (j == 0 && lane == 0) *(GAS u32*)((GAS char*)P.ws + WS_EPOCH) = T;
        }
        STAMP(7);
        FLUSH_STAMPS();
    }
}

extern "C" void kernel_launch(void* const* d_in, const int* in_sizes, int n_in,
                              void* d_out, int out_size, void* d_ws, size_t ws_size,
                              hipStream_t stream) {
    Params P;
    P.x = d_in[0]; P.k = d_in[1]; P.v = d_in[2]; P.wq = d_in[3]; P.wo = d_in[4];
    P.w1 = d_in[6]; P.b1 = d_in[7]; P.w2 = d_in[8]; P.b2 = d_in[9];
    P.out = d_out; P.ws = d_ws;
    fused_decode_kernel<<<256, 320, 0, stream>>>(P);
}
```

```cpp
#include <hip/hip_runtime.h>
#include <hip/hip_bf16.h>
#include <stdint.h>
#include <stdio.h>
#define STAMPS 1

typedef unsigned int u32;
typedef u32 u32x4 __attribute__((ext_vector_type(4)));
typedef u32 u32x2 __attribute__((ext_vector_type(2)));
typedef float f32x4 __attribute__((ext_vector_type(4)));
typedef float f32x2 __attribute__((ext_vector_type(2)));
typedef __bf16 bf16x8 __attribute__((ext_vector_type(8)));
typedef __bf16 bf16x2 __attribute__((ext_vector_type(2)));
#define GAS __attribute__((address_space(1)))
typedef GAS const u32x4 gcu32x4;
typedef GAS const char gcchar;

constexpr int NB = 4, DM = 512, HKV = 8, SK = 16384, DH = 128, FF = 1792;
constexpr int NMLP = 28;
constexpr u32 WS_EPOCH = 0;
constexpr u32 WS_Q  = 0x1000;
constexpr u32 WS_PL = 0x20000;
constexpr u32 WS_MP = 0x30000;
constexpr u32 WS_PO = 0x100000;
constexpr u32 WS_SP = 0x200000;
constexpr u32 WS_END = 0x300000;
constexpr u32 SPIN_CAP = 1u << 17;
constexpr int L_Q   = 0;
constexpr int L_T   = 1024;
constexpr int L_XN  = 2048;
constexpr int L_HP  = 6144;
constexpr int L_H   = 14336;
constexpr int L_ML  = 14848;
constexpr int L_O   = 15104;
constexpr int L_MO  = 16384;
constexpr int L_END = 16384 + 4 * 4 * 144 * 4;
constexpr int LDS_BYTES = 84 * 1024;

struct Params {
    const void* x; const void* k; const void* v; const void* wq; const void* wo;
    const void* w1; const void* b1; const void* w2; const void* b2;
    void* out; void* ws;
};

__device__ __forceinline__ u32x4 ldnt(gcchar* p) { return __builtin_nontemporal_load((gcu32x4*)p); }
__device__ __forceinline__ float bflo(u32 w) { return __uint_as_float(w << 16); }
__device__ __forceinline__ float bfhi(u32 w) { return __uint_as_float(w & 0xffff0000u); }
__device__ __forceinline__ u32 pack2(float lo, float hi) {
    f32x2 v = {lo, hi};
    return __builtin_bit_cast(u32, __builtin_convertvector(v, bf16x2));
}
__device__ __forceinline__ float bfr(float x) { return bflo(pack2(x, 0.f)); }
__device__ __forceinline__ f32x4 mfma(u32x4 a, u32x4 b, f32x4 c) {
    return __builtin_amdgcn_mfma_f32_16x16x32_bf16(__builtin_bit_cast(bf16x8, a), __builtin_bit_cast(bf16x8, b), c, 0, 0, 0);
}
__device__ __forceinline__ float dot2(u32 a, u32 b, float c) {
    return __builtin_amdgcn_fdot2_f32_bf16(__builtin_bit_cast(bf16x2, a), __builtin_bit_cast(bf16x2, b), c, false);
}
template <int CTRL> __device__ __forceinline__ u32 dppu(u32 x) { return (u32)__builtin_amdgcn_mov_dpp((int)x, CTRL, 0xf, 0xf, true); }
template <int CTRL> __device__ __forceinline__ float dppf(float x) { return __uint_as_float(dppu<CTRL>(__float_as_uint(x))); }
constexpr int XOR1 = 0xB1, XOR2 = 0x4E, HMIR = 0x141, ROR4 = 0x124, ROR8 = 0x128, EVEN = 0xA0, ODD = 0xF5;
__device__ __forceinline__ float xrow_max(float x) {
    auto s = __builtin_amdgcn_permlane16_swap(__float_as_uint(x), __float_as_uint(x), false, false);
    x = fmaxf(__uint_as_float(s[0]), __uint_as_float(s[1]));
    auto t = __builtin_amdgcn_permlane32_swap(__float_as_uint(x), __float_as_uint(x), false, false);
    return fmaxf(__uint_as_float(t[0]), __uint_as_float(t[1]));
}
__device__ __forceinline__ float xrow_sum(float x) {
    auto s = __builtin_amdgcn_permlane16_swap(__float_as_uint(x), __float_as_uint(x), false, false);
    x = __uint_as_float(s[0]) + __uint_as_float(s[1]);
    auto t = __builtin_amdgcn_permlane32_swap(__float_as_uint(x), __float_as_uint(x), false, false);
    return __uint_as_float(t[0]) + __uint_as_float(t[1]);
}
__device__ __forceinline__ float x32_max(float x) {
    auto t = __builtin_amdgcn_permlane32_swap(__float_as_uint(x), __float_as_uint(x), false, false);
    return fmaxf(__uint_as_float(t[0]), __uint_as_float(t[1]));
}
__device__ __forceinline__ float x32_sum(float x) {
    auto t = __builtin_amdgcn_permlane32_swap(__float_as_uint(x), __float_as_uint(x), false, false);
    return __uint_as_float(t[0]) + __uint_as_float(t[1]);
}
__device__ __forceinline__ float wave_sum(float x) {
    x += dppf<XOR1>(x); x += dppf<XOR2>(x); x += dppf<ROR4>(x); x += dppf<ROR8>(x);
    return xrow_sum(x);
}
__device__ __forceinline__ void wg_barrier() { asm volatile("s_waitcnt lgkmcnt(0)\n\ts_barrier" ::: "memory"); }
__device__ __forceinline__ void raw_barrier() { asm volatile("s_barrier" ::: "memory"); }
constexpr u32 OOB = 0x40000000u;

__device__ __forceinline__ u32x4 mk_unit(u32 v0, u32 v1, u32 T) { u32x4 u = {v0, T ^ v1, v1, T ^ v0}; return u; }
__device__ __forceinline__ bool unit_ok(u32x4 u, u32 T) { return ((u.y ^ u.z) == T) & ((u.w ^ u.x) == T); }
__device__ __forceinline__ void st_unit(__amdgpu_buffer_rsrc_t rs, u32 off, u32x4 u) { __builtin_amdgcn_raw_buffer_store_b128(u, rs, off, 0, 16); }
__device__ __forceinline__ u32x4 ld_unit(__amdgpu_buffer_rsrc_t rs, u32 off) { return __builtin_amdgcn_raw_buffer_load_b128(rs, off, 0, 16); }

template <int DD> __device__ __forceinline__ u32x4 tfrag(const u32x4* v) {
    constexpr u32 sel = (DD & 1) ? 0x07060302u : 0x05040100u;
    u32x4 a;
    a.x = __builtin_amdgcn_perm(v[1][DD >> 1], v[0][DD >> 1], sel);
    a.y = __builtin_amdgcn_perm(v[3][DD >> 1], v[2][DD >> 1], sel);
    a.z = __builtin_amdgcn_perm(v[5][DD >> 1], v[4][DD >> 1], sel);
    a.w = __builtin_amdgcn_perm(v[7][DD >> 1], v[6][DD >> 1], sel);
    return a;
}
__device__ __forceinline__ void pv_tile(const u32x4* v, u32x4 p, f32x4 (&acc)[8]) {
    acc[0] = mfma(tfrag<0>(v), p, acc[0]); acc[1] = mfma(tfrag<1>(v), p, acc[1]);
    acc[2] = mfma(tfrag<2>(v), p, acc[2]); acc[3] = mfma(tfrag<3>(v), p, acc[3]);
    acc[4] = mfma(tfrag<4>(v), p, acc[4]); acc[5] = mfma(tfrag<5>(v), p, acc[5]);
    acc[6] = mfma(tfrag<6>(v), p, acc[6]); acc[7] = mfma(tfrag<7>(v), p, acc[7]);
}
template <int NSLOT>
__device__ __forceinline__ void publish_cols(__amdgpu_buffer_rsrc_t rs, u32 base, const f32x4 (&acc)[8], int slot, int ng0, int lane, u32 T) {
    const int g = lane >> 4, b = (lane >> 2) & 3, rho = lane & 3;
    const u32 off0 = base + (u32)(((b * 64 + ng0 + 4 * g) * NSLOT + slot) * 64 + rho * 16);
#pragma unroll
    for (int r = 0; r < 4; ++r) {
        float a0 = (rho & 2) ? acc[4][r] : acc[0][r], a1 = (rho & 2) ? acc[6][r] : acc[2][r];
        float c0 = (rho & 2) ? acc[5][r] : acc[1][r], c1 = (rho & 2) ? acc[7][r] : acc[3][r];
        float v0 = (rho & 1) ? a1 : a0, v1 = (rho & 1) ? c1 : c0;
        st_unit(rs, off0 + (u32)(r * NSLOT * 64), mk_unit(__float_as_uint(v0), __float_as_uint(v1), T));
    }
}

#define NSTAMP 12
#define STAMP(i) do { ts[i] = __builtin_amdgcn_s_memrealtime(); } while (0)
#define STAMPV(i, v) do { ts[i] = (v); } while (0)
#define FLUSH_STAMPS() do { if (lane == 0) { for (int z = 0; z < NSTAMP; ++z) ((unsigned long long*)((char*)P.ws + WS_END))[(blockIdx.x * 5 + wave) * 16 + z] = ts[z]; } } while (0)
#define DBG(x) x

struct Ring { u32x4 r[16]; };

__device__ __forceinline__ void issue_k(Ring& R, gcchar* kb, u32 koff) {
#pragma unroll
    for (int i = 0; i < 8; ++i) R.r[i] = ldnt(kb + koff + (i >> 2) * 4096 + (i & 3) * 64);
}
__device__ __forceinline__ void issue_v(Ring& R, gcchar* vb, u32 voff) {
#pragma unroll
    for (int i = 0; i < 8; ++i) R.r[8 + i] = ldnt(vb + voff + i * 1024);
}
#define SB() __builtin_amdgcn_sched_barrier(0)
#define PACE_N 7
#define STR2(x) #x
#define STR(x) STR2(x)
#define PACE() asm volatile("s_waitcnt vmcnt(" STR(PACE_N) ")" ::: "memory")

struct AttnState { f32x4 acc[8]; float m, l; };

template <bool ISSUE>
__device__ __forceinline__ void proc_tile(Ring& R, AttnState& S, const u32x4 (&qf)[4], gcchar* kn, gcchar* vn, u32 koff, u32 voff) {
    constexpr float SCL = 0.08838834764831845f, LOG2E = 1.4426950408889634f;
    f32x4 s0 = {0.f, 0.f, 0.f, 0.f}, s1 = {0.f, 0.f, 0.f, 0.f};
#pragma unroll
    for (int st = 0; st < 4; ++st) { s0 = mfma(R.r[st], qf[st], s0); s1 = mfma(R.r[4 + st], qf[st], s1); }
    SB();
    if (ISSUE) issue_k(R, kn, koff);
    SB();
    float z[8];
#pragma unroll
    for (int j = 0; j < 2; ++j) {
        const u32 a = pack2(s0[2 * j], s0[2 * j + 1]), c = pack2(s1[2 * j], s1[2 * j + 1]);
        const u32 a2 = pack2(bflo(a) * SCL, bfhi(a) * SCL), c2 = pack2(bflo(c) * SCL, bfhi(c) * SCL);
        z[2 * j] = bflo(a2); z[2 * j + 1] = bfhi(a2); z[4 + 2 * j] = bflo(c2); z[4 + 2 * j + 1] = bfhi(c2);
    }
    float mx = fmaxf(fmaxf(fmaxf(z[0], z[1]), fmaxf(z[2], z[3])), fmaxf(fmaxf(z[4], z[5]), fmaxf(z[6], z[7])));
    mx = xrow_max(mx) * LOG2E;
    if (__builtin_amdgcn_ballot_w64(mx > S.m) != 0ull) {
        const float mn = fmaxf(S.m, mx);
        const float al = __builtin_amdgcn_exp2f(S.m - mn);
        S.m = mn; S.l *= al;
#pragma unroll
        for (int d = 0; d < 8; ++d) S.acc[d] *= al;
    }
    float p[8];
#pragma unroll
    for (int j = 0; j < 8; ++j) p[j] = __builtin_amdgcn_exp2f(__builtin_fmaf(z[j], LOG2E, -S.m));
    S.l += ((p[0] + p[1]) + (p[2] + p[3])) + ((p[4] + p[5]) + (p[6] + p[7]));
    u32x4 pf = {pack2(p[0], p[1]), pack2(p[2], p[3]), pack2(p[4], p[5]), pack2(p[6], p[7])};
    pv_tile(&R.r[8], pf, S.acc);
    SB();
    if (ISSUE) issue_v(R, vn, voff);
    SB();
}

__global__ void __launch_bounds__(320, 2) fused_decode_kernel(Params P) {
    __shared__ __attribute__((aligned(16))) char lds[LDS_BYTES];
    const int tid = threadIdx.x, lane = tid & 63;
    const int wave = __builtin_amdgcn_readfirstlane(tid >> 6);
    const int j = blockIdx.x;
    const int hkv = j & 7, pidx = j >> 3;
    const int b = pidx >> 3, split = pidx & 7;
    const int grp = b * 8 + hkv;
    const bool grp_mlp = grp < NMLP;
    const bool is_mlp = grp_mlp && split == 7;
    const __amdgpu_buffer_rsrc_t rs = __builtin_amdgcn_make_buffer_rsrc(P.ws, 0, WS_END, 0x00020000);
    const int g = lane >> 4, n16 = lane & 15;
    const int oh = hkv * 4 + (pidx & 3), okh = (pidx >> 2) & 1, ons = pidx >> 3;

    DBG(unsigned long long ts[NSTAMP]; for (int z = 0; z < NSTAMP; ++z) ts[z] = 0;)
    if (wave < 4) {
        STAMP(0);
        raw_barrier();
        const int t_start = grp_mlp ? 65 * split : 64 * split;
        const int t_cnt = grp_mlp ? (split == 7 ? 57 : 65) : 64;
        const int nt = (t_cnt - wave + 3) >> 2;
        gcchar* kb = (gcchar*)P.k + (size_t)grp * (SK * DH * 2) + (size_t)(t_start + wave) * 8192;
        gcchar* vb = (gcchar*)P.v + (size_t)grp * (SK * DH * 2) + (size_t)(t_start + wave) * 8192;
        const u32 koff = (u32)((4 * (n16 & 3) + (n16 >> 2)) * 256 + g * 16);
        const u32 voff = (u32)(lane * 16);
        Ring A, B;
        AttnState S;
        if (is_mlp) {
            const int f0 = grp * 64;
            gcchar* w1b = (gcchar*)P.w1 + (size_t)(128 * wave) * (FF * 2) + f0 * 2;
            const u32 w1off = (u32)((32 * (n16 >> 3) + 8 * g) * (FF * 2) + (n16 & 7) * 16);
            gcchar* w2b = (gcchar*)P.w2 + (size_t)f0 * 1024 + 256 * wave;
            const u32 w2off = (u32)(8 * g * 1024 + n16 * 16);
#pragma unroll
            for (int i = 0; i < 16; ++i) {
                if (i >= 8) PACE();
                A.r[i] = ldnt(w1b + w1off + ((i >> 3) * 64 + (i & 7)) * (FF * 2));
            }
#pragma unroll
            for (int i = 0; i < 16; ++i) {
                PACE();
                B.r[i] = ldnt(w2b + w2off + ((i >> 3) * 32 + (i & 7)) * 1024);
            }
            const float b1v = bflo((u32)((const GAS unsigned short*)P.b1)[f0 + lane]);
            wg_barrier();
#pragma unroll
            for (int d = 0; d < 8; ++d) S.acc[d] = f32x4{0.f, 0.f, 0.f, 0.f};
#pragma unroll
            for (int tau = 0; tau < 2; ++tau) {
                const u32x4 xf = *(const u32x4*)(lds + L_XN + (n16 & 3) * 1024 + (128 * wave + 64 * tau + 32 * ((n16 >> 2) & 1) + 8 * g) * 2);
                pv_tile(&A.r[8 * tau], xf, S.acc);
            }
            SB();
            issue_k(A, kb, koff); issue_v(A, vb, voff);
            SB();
            if (n16 < 8 && (g >> 1) == (n16 >> 2)) {
                float* hp = (float*)(lds + L_HP) + ((wave * 2 + (n16 >> 2)) * 4 + (n16 & 3)) * 64 + 32 * (g & 1);
#pragma unroll
                for (int r = 0; r < 4; ++r) {
                    *(f32x4*)(hp + 8 * r) = f32x4{S.acc[0][r], S.acc[1][r], S.acc[2][r], S.acc[3][r]};
                    *(f32x4*)(hp + 8 * r + 4) = f32x4{S.acc[4][r], S.acc[5][r], S.acc[6][r], S.acc[7][r]};
                }
            }
            wg_barrier();
            {
                const float* hp = (const float*)(lds + L_HP) + wave * 64 + lane;
                float sum = 0.f;
#pragma unroll
                for (int i = 0; i < 8; ++i) sum += hp[i * 256];
                float h = bfr(bfr(sum) + b1v);
                h = h > 0.f ? h : 0.f;
                ((unsigned short*)(lds + L_H))[wave * 64 + lane] = (unsigned short)pack2(h * h, 0.f);
            }
            wg_barrier();
#pragma unroll
            for (int d = 0; d < 8; ++d) S.acc[d] = f32x4{0.f, 0.f, 0.f, 0.f};
#pragma unroll
            for (int tau = 0; tau < 2; ++tau) {
                const u32x4 hf = *(const u32x4*)(lds + L_H + (n16 >> 2) * 128 + (32 * tau + 8 * g) * 2);
                pv_tile(&B.r[8 * tau], hf, S.acc);
            }
            SB();
            issue_k(B, kb + 4 * 8192, koff); issue_v(B, vb + 4 * 8192, voff);
            SB();
            const u32 T = *(const u32*)(lds + L_T);
            publish_cols<32>(rs, WS_MP, S.acc, grp, 16 * wave, lane, T);
        } else {
#pragma unroll
            for (int i = 0; i < 8; ++i) A.r[i] = ldnt(kb + koff + (i >> 2) * 4096 + (i & 3) * 64);
#pragma unroll
            for (int i = 0; i < 8; ++i) { PACE(); A.r[8 + i] = ldnt(vb + voff + i * 1024); }
#pragma unroll
            for (int i = 0; i < 8; ++i) { PACE(); B.r[i] = ldnt(kb + 4 * 8192 + koff + (i >> 2) * 4096 + (i & 3) * 64); }
#pragma unroll
            for (int i = 0; i < 8; ++i) { PACE(); B.r[8 + i] = ldnt(vb + 4 * 8192 + voff + i * 1024); }
        }
        STAMP(1);
        wg_barrier();
        STAMP(2);
        u32x4 qf[4];
#pragma unroll
        for (int st = 0; st < 4; ++st) qf[st] = *(const u32x4*)(lds + L_Q + (n16 & 3) * 256 + st * 64 + g * 16);
        const u32 T = *(const u32*)(lds + L_T);
#pragma unroll
        for (int d = 0; d < 8; ++d) S.acc[d] = f32x4{0.f, 0.f, 0.f, 0.f};
        S.m = -INFINITY; S.l = 0.f;
        int i = 0;
        gcchar* kn = kb + 8 * 8192; gcchar* vn = vb + 8 * 8192;
        for (; i + 3 < nt; i += 2) {
            proc_tile<true>(A, S, qf, kn, vn, koff, voff);
            proc_tile<true>(B, S, qf, kn + 4 * 8192, vn + 4 * 8192, koff, voff);
            kn += 8 * 8192; vn += 8 * 8192;
            DBG(if (i == 2) STAMP(8); if (i == 6) STAMP(9); if (i == 10) STAMP(10);)
        }
        if (nt - i == 3) {
            proc_tile<true>(A, S, qf, kn, vn, koff, voff);
            proc_tile<false>(B, S, qf, kn, vn, koff, voff);
            proc_tile<false>(A, S, qf, kn, vn, koff, voff);
        } else {
            proc_tile<false>(A, S, qf, kn, vn, koff, voff);
            proc_tile<false>(B, S, qf, kn, vn, koff, voff);
        }
        STAMP(3);
        const float lw = xrow_sum(S.l);
        if (n16 < 4) {
            float* mo = (float*)(lds + L_MO) + (wave * 4 + n16) * 144 + g * 36;
#pragma unroll
            for (int r = 0; r < 4; ++r) {
                *(f32x4*)(mo + 8 * r) = f32x4{S.acc[0][r], S.acc[1][r], S.acc[2][r], S.acc[3][r]};
                *(f32x4*)(mo + 8 * r + 4) = f32x4{S.acc[4][r], S.acc[5][r], S.acc[6][r], S.acc[7][r]};
            }
            if (g == 0) *(f32x2*)(lds + L_ML + (wave * 4 + n16) * 8) = f32x2{S.m, lw};
        }
        wg_barrier();
        {
            f32x2 ml[4], ov[4];
#pragma unroll
            for (int w = 0; w < 4; ++w) {
                ml[w] = *(const f32x2*)(lds + L_ML + (w * 4 + wave) * 8);
                ov[w] = *(const f32x2*)((const float*)(lds + L_MO) + (w * 4 + wave) * 144 + (lane >> 4) * 36 + 2 * (lane & 15));
            }
            const float M = fmaxf(fmaxf(ml[0].x, ml[1].x), fmaxf(ml[2].x, ml[3].x));
            float L = 0.f, o0 = 0.f, o1 = 0.f;
#pragma unroll
            for (int w = 0; w < 4; ++w) {
                const float a = __builtin_amdgcn_exp2f(ml[w].x - M);
                L += a * ml[w].y; o0 += a * ov[w].x; o1 += a * ov[w].y;
            }
            const float inv = 1.0f / L;
            const u32 blk = (u32)((b * 32 + hkv * 4 + wave) * 8 + split);
            st_unit(rs, WS_PO + blk * 1024 + lane * 16, mk_unit(__float_as_uint(o0 * inv), __float_as_uint(o1 * inv), T));
            if (lane == 0) {
                const float lse = M + __builtin_amdgcn_logf(L);
                st_unit(rs, WS_PL + blk * 16, mk_unit(__float_as_uint(lse), 0u, T));
            }
        }
        STAMP(4);
        {
            const int half = lane >> 5;
            const u32 blk0 = (u32)((wave * 32 + oh) * 8);
            const u32 ooff = WS_PO + (blk0 + half) * 1024 + (u32)((32 * okh + (lane & 31)) * 16);
            const u32 loff = lane < 8 ? WS_PL + (blk0 + lane) * 16 : OOB;
            u32x4 uo[4], ul;
            u32 want = 0, have = 0;
            u32 lseen = 0;
            for (u32 spin = 0; spin < SPIN_CAP; ++spin) {
                asm volatile("" ::: "memory");
                u32x4 t[4];
                ul = ld_unit(rs, loff);
#pragma unroll
                for (int r = 0; r < 4; ++r) t[r] = ld_unit(rs, ((want >> r) & 1u) ? ooff + r * 2048 : OOB);
#pragma unroll
                for (int r = 0; r < 4; ++r)
                    if (((want >> r) & 1u) && unit_ok(t[r], T)) { uo[r] = t[r]; have |= 1u << r; }
                lseen = (u32)__builtin_amdgcn_ballot_w64(unit_ok(ul, T)) & 0xffu;
                const bool done = lseen == 0xffu && __builtin_amdgcn_ballot_w64(have != 0xfu) == 0ull;
                if (done) break;
                const u32 gate = (__builtin_popcount(lseen) >= 6) ? 0xffu : lseen;
                const u32 gl = gate >> half;
                want = ((gl & 1u) | ((gl >> 1) & 2u) | ((gl >> 2) & 4u) | ((gl >> 3) & 8u)) & ~have;
                if (__builtin_amdgcn_ballot_w64(want != 0u) == 0ull || lseen != 0xffu) __builtin_amdgcn_s_sleep(2);
            }
            const float lse = __uint_as_float(ul.x);
            float M = fmaxf(lse, dppf<XOR1>(lse)); M = fmaxf(M, dppf<XOR2>(M)); M = fmaxf(M, dppf<HMIR>(M));
            const float al = __builtin_amdgcn_exp2f(lse - M);
            float W = al + dppf<XOR1>(al); W += dppf<XOR2>(W); W += dppf<HMIR>(W);
            const float inv = 1.0f / W;
            const u32 aw = __float_as_uint(al * inv);
#define WSEL(r) __uint_as_float(half ? __builtin_amdgcn_readlane(aw, 2 * (r) + 1) : __builtin_amdgcn_readlane(aw, 2 * (r)))
            const float a0 = WSEL(0), a1 = WSEL(1), a2 = WSEL(2), a3 = WSEL(3);
#undef WSEL
            float o0 = (a0 * __uint_as_float(uo[0].x) + a1 * __uint_as_float(uo[1].x)) + (a2 * __uint_as_float(uo[2].x) + a3 * __uint_as_float(uo[3].x));
            float o1 = (a0 * __uint_as_float(uo[0].z) + a1 * __uint_as_float(uo[1].z)) + (a2 * __uint_as_float(uo[2].z) + a3 * __uint_as_float(uo[3].z));
            o0 = x32_sum(o0); o1 = x32_sum(o1);
            if (lane < 32) *(u32*)(lds + L_O + wave * 128 + lane * 4) = pack2(o0, o1);
        }
        STAMP(5);
        wg_barrier();
        FLUSH_STAMPS();
    } else {
        STAMP(0);
        u32x4 xr[4], wr[16];
        const u32 epoch = *(const GAS u32*)((gcchar*)P.ws + WS_EPOCH);
#pragma unroll
        for (int bb = 0; bb < 4; ++bb) xr[bb] = *(gcu32x4*)((gcchar*)P.x + bb * 1024 + lane * 16);
        {
            gcchar* wqb = (gcchar*)P.wq + (hkv * 512 + 16 * pidx) * 2;
            const u32 wqoff = (u32)((lane >> 1) * 16 * 8192 + (lane & 1) * 16);
#pragma unroll
            for (int i = 0; i < 16; ++i) wr[i] = ldnt(wqb + wqoff + i * 8192);
        }
        SB();
        raw_barrier();
        SB();
        DBG(asm volatile("s_waitcnt vmcnt(16)" ::: "memory"); STAMP(8);)
        u32 T = epoch + 1u; if (T == 0u) T = 1u;
        T = __builtin_amdgcn_readfirstlane(T);
        u32 xn[4][4];
#pragma unroll
        for (int bb = 0; bb < 4; ++bb) {
            float v[8];
#pragma unroll
            for (int i = 0; i < 4; ++i) { v[2 * i] = bflo(xr[bb][i]); v[2 * i + 1] = bfhi(xr[bb][i]); }
            float s = ((v[0] + v[1]) + (v[2] + v[3])) + ((v[4] + v[5]) + (v[6] + v[7]));
            const float mean = wave_sum(s) * (1.0f / 512.0f);
            float q = 0.f;
#pragma unroll
            for (int i = 0; i < 8; ++i) { const float d = v[i] - mean; q += d * d; }
            const float var = bfr(wave_sum(q) * (1.0f / 512.0f));
            const float mb = bfr(mean);
            const float den = bfr(__builtin_sqrtf(bfr(var + 1e-5f)));
#pragma unroll
            for (int i = 0; i < 4; ++i) xn[bb][i] = pack2(bfr(v[2 * i] - mb) / den, bfr(v[2 * i + 1] - mb) / den);
        }
        if (lane == 0) *(u32*)(lds + L_T) = T;
        if (is_mlp) {
#pragma unroll
            for (int bb = 0; bb < 4; ++bb) *(u32x4*)(lds + L_XN + bb * 1024 + lane * 16) = u32x4{xn[bb][0], xn[bb][1], xn[bb][2], xn[bb][3]};
        }
        STAMP(1);
        DBG(asm volatile("s_waitcnt vmcnt(0)" ::: "memory"); STAMP(9);)
        {
            float acc[4][8];
#pragma unroll
            for (int bb = 0; bb < 4; ++bb)
#pragma unroll
                for (int c = 0; c < 8; ++c) acc[bb][c] = 0.f;
#pragma unroll
            for (int a = 0; a < 8; ++a) {
                u32 xp[4];
#pragma unroll
                for (int bb = 0; bb < 4; ++bb) xp[bb] = (a < 4) ? dppu<EVEN>(xn[bb][a & 3]) : dppu<ODD>(xn[bb][a & 3]);
#pragma unroll
                for (int c = 0; c < 8; ++c) {
                    const u32 wp = __builtin_amdgcn_perm(wr[2 * a + 1][c >> 1], wr[2 * a][c >> 1], (c & 1) ? 0x07060302u : 0x05040100u);
#pragma unroll
                    for (int bb = 0; bb < 4; ++bb) acc[bb][c] = dot2(wp, xp[bb], acc[bb][c]);
                }
            }
            float r1[2][8], r2[8];
#pragma unroll
            for (int bb = 0; bb < 2; ++bb)
#pragma unroll
                for (int c = 0; c < 8; ++c) {
                    auto t = __builtin_amdgcn_permlane32_swap(__float_as_uint(acc[bb][c]), __float_as_uint(acc[bb + 2][c]), false, false);
                    r1[bb][c] = __uint_as_float(t[0]) + __uint_as_float(t[1]);
                }
#pragma unroll
            for (int c = 0; c < 8; ++c) {
                auto t = __builtin_amdgcn_permlane16_swap(__float_as_uint(r1[0][c]), __float_as_uint(r1[1][c]), false, false);
                float y = __uint_as_float(t[0]) + __uint_as_float(t[1]);
                y += dppf<XOR2>(y); y += dppf<ROR4>(y); y += dppf<ROR8>(y);
                r2[c] = y;
            }
            const bool sub = (n16 >> 1) & 1;
            const u32 v0 = sub ? pack2(r2[4], r2[5]) : pack2(r2[0], r2[1]);
            const u32 v1 = sub ? pack2(r2[6], r2[7]) : pack2(r2[2], r2[3]);
            if (n16 < 4) {
                const int u = 2 * (n16 & 1) + (n16 >> 1);
                st_unit(rs, WS_Q + (u32)((g * 8 + hkv) * 2048 + (4 * pidx + u) * 16), mk_unit(v0, v1, T));
            }
        }
        STAMP(2);
        if (is_mlp) { wg_barrier(); wg_barrier(); wg_barrier(); }
        {
            u32x4 u0, u1;
            const u32 off = WS_Q + (u32)(grp * 2048 + lane * 16);
            for (u32 spin = 0; spin < SPIN_CAP; ++spin) {
                asm volatile("" ::: "memory");
                u0 = ld_unit(rs, off); u1 = ld_unit(rs, off + 1024);
                DBG(if (spin == 0) { asm volatile("s_waitcnt vmcnt(0)" ::: "memory"); STAMP(10); })
                if (__builtin_amdgcn_ballot_w64(!(unit_ok(u0, T) & unit_ok(u1, T))) == 0ull) { STAMPV(11, 1000000ull + spin); break; }
                __builtin_amdgcn_s_sleep(1);
            }
            *(u32x2*)(lds + L_Q + lane * 8) = u32x2{u0.x, u0.z};
            *(u32x2*)(lds + L_Q + 512 + lane * 8) = u32x2{u1.x, u1.z};
        }
        STAMP(3);
        wg_barrier();
        u32x4 wa[2][8];
        {
            u32x4 wo[16];
            gcchar* wob = (gcchar*)P.wo + (size_t)(oh * 128 + 64 * okh) * 1024 + 256 * ons;
            const u32 wooff = (u32)(8 * g * 1024 + n16 * 16);
#pragma unroll
            for (int i = 0; i < 16; ++i) wo[i] = ldnt(wob + wooff + ((i >> 3) * 32 + (i & 7)) * 1024);
#pragma unroll
            for (int tau = 0; tau < 2; ++tau) {
                wa[tau][0] = tfrag<0>(&wo[8 * tau]); wa[tau][1] = tfrag<1>(&wo[8 * tau]); wa[tau][2] = tfrag<2>(&wo[8 * tau]); wa[tau][3] = tfrag<3>(&wo[8 * tau]);
                wa[tau][4] = tfrag<4>(&wo[8 * tau]); wa[tau][5] = tfrag<5>(&wo[8 * tau]); wa[tau][6] = tfrag<6>(&wo[8 * tau]); wa[tau][7] = tfrag<7>(&wo[8 * tau]);
            }
        }
        const int fb = j >> 6, fng = j & 63;
        const int fu = lane & 3, fi = lane >> 2;
        const u32 xw = *(const GAS u32*)((gcchar*)P.x + fb * 1024 + fng * 16 + fu * 4);
        const u32 b2w = *(const GAS u32*)((gcchar*)P.b2 + fng * 16 + fu * 4);
        float g0, g1;
        {
            const u32 off = WS_MP + (u32)(((fb * 64 + fng) * 32 + fi) * 64 + fu * 16);
            const bool has1 = fi + 16 < NMLP;
            u32x4 u0, u1;
            for (u32 spin = 0; spin < SPIN_CAP; ++spin) {
                __builtin_amdgcn_s_sleep(127); __builtin_amdgcn_s_sleep(127);
                asm volatile("" ::: "memory");
                u0 = ld_unit(rs, off); u1 = ld_unit(rs, off + 1024);
                if (__builtin_amdgcn_ballot_w64(!(unit_ok(u0, T) & (unit_ok(u1, T) | !has1))) == 0ull) break;
            }
            float a0 = __uint_as_float(u0.x) + (has1 ? __uint_as_float(u1.x) : 0.f);
            float a1 = __uint_as_float(u0.z) + (has1 ? __uint_as_float(u1.z) : 0.f);
            a0 += dppf<ROR4>(a0); a0 += dppf<ROR8>(a0); a0 = xrow_sum(a0);
            a1 += dppf<ROR4>(a1); a1 += dppf<ROR8>(a1); a1 = xrow_sum(a1);
            const float y0 = bfr(bfr(a0) + bflo(b2w)), y1 = bfr(bfr(a1) + bfhi(b2w));
            g0 = bfr(y0 / (1.0f + __expf(-y0)));
            g1 = bfr(y1 / (1.0f + __expf(-y1)));
        }
        STAMP(4);
        wg_barrier();
        wg_barrier();
        STAMP(5);
        {
            f32x4 acc[8];
#pragma unroll
            for (int d = 0; d < 8; ++d) acc[d] = f32x4{0.f, 0.f, 0.f, 0.f};
#pragma unroll
            for (int tau = 0; tau < 2; ++tau) {
                const u32x4 of = *(const u32x4*)(lds + L_O + (n16 >> 2) * 128 + (32 * tau + 8 * g) * 2);
#pragma unroll
                for (int d = 0; d < 8; ++d) acc[d] = mfma(wa[tau][d], of, acc[d]);
            }
            publish_cols<64>(rs, WS_SP, acc, oh * 2 + okh, 16 * ons, lane, T);
        }
        STAMP(6);
        {
            const u32 off = WS_SP + (u32)(((fb * 64 + fng) * 64 + fi) * 64 + fu * 16);
            u32x4 u[4];
            u32 have = 0;
            for (u32 spin = 0; spin < SPIN_CAP; ++spin) {
                asm volatile("" ::: "memory");
                u32x4 t[4];
#pragma unroll
                for (int r = 0; r < 4; ++r) t[r] = ld_unit(rs, ((have >> r) & 1u) ? OOB : off + r * 1024);
#pragma unroll
                for (int r = 0; r < 4; ++r)
                    if (!((have >> r) & 1u) && unit_ok(t[r], T)) { u[r] = t[r]; have |= 1u << r; }
                if (__builtin_amdgcn_ballot_w64(have != 0xfu) == 0ull) break;
                __builtin_amdgcn_s_sleep(1);
            }
            float a0 = (__uint_as_float(u[0].x) + __uint_as_float(u[1].x)) + (__uint_as_float(u[2].x) + __uint_as_float(u[3].x));
            float a1 = (__uint_as_float(u[0].z) + __uint_as_float(u[1].z)) + (__uint_as_float(u[2].z) + __uint_as_float(u[3].z));
            a0 += dppf<ROR4>(a0); a0 += dppf<ROR8>(a0); a0 = xrow_sum(a0);
            a1 += dppf<ROR4>(a1); a1 += dppf<ROR8>(a1); a1 = xrow_sum(a1);
            const float r0 = bflo(xw) + bfr(bfr(a0) * g0), r1 = bfhi(xw) + bfr(bfr(a1) * g1);
            if (lane < 4) *(GAS u32*)((GAS char*)P.out + fb * 1024 + fng * 16 + fu * 4) = pack2(r0, r1);
            if (j == 0 && lane == 0) *(GAS u32*)((GAS char*)P.ws + WS_EPOCH) = T;
        }
        STAMP(7);
        FLUSH_STAMPS();
    }
}

__global__ void print_kernel(const unsigned long long* st) {
    const int lane = threadIdx.x;
    unsigned long long t0 = ~0ull;
    for (int w = lane; w < 1280; w += 64) { unsigned long long t = st[w * 16]; t0 = t < t0 ? t : t0; }
    for (int o = 32; o; o >>= 1) { unsigned long long u = __shfl_xor(t0, o); t0 = u < t0 ? u : t0; }
    int mx[2][12], sm[2][12], mn[2][12];
#pragma unroll
    for (int c = 0; c < 2; ++c)
#pragma unroll
        for (int s = 0; s < 12; ++s) {
            int a = 0, b = 0, m = 1 << 30;
            for (int wg = lane; wg < 256; wg += 64)
                for (int w = (c ? 4 : 0); w < (c ? 5 : 4); ++w) {
                    long long d = (long long)(st[(wg * 5 + w) * 16 + s] - t0);
                    int t = (d < 0 || d > 1000000) ? 0 : (int)d;
                    a = t > a ? t : a; b += t; m = t < m ? t : m;
                }
            for (int o = 32; o; o >>= 1) { int u = __shfl_xor(a, o); a = u > a ? u : a; b += __shfl_xor(b, o); u = __shfl_xor(m, o); m = u < m ? u : m; }
            mx[c][s] = a; sm[c][s] = b / (c ? 256 : 1024); mn[c][s] = m;
        }
    if (lane == 0 && ((t0 >> 3) & 7) == 0)
        printf("ST str0 %d %d %d s1 %d %d %d s2 %d %d %d s8 %d %d s3 %d %d %d s4 %d %d s5 %d %d | svc0 %d %d s8 %d %d s1 %d %d s9 %d %d s2 %d %d s3 %d %d s4 %d %d s5 %d %d s6 %d %d s7 %d %d\n",
            mn[0][0], sm[0][0], mx[0][0], mn[0][1], sm[0][1], mx[0][1], mn[0][2], sm[0][2], mx[0][2], sm[0][8], mx[0][8], mn[0][3], sm[0][3], mx[0][3], sm[0][4], mx[0][4], sm[0][5], mx[0][5],
            sm[1][0], mx[1][0], sm[1][8], mx[1][8], sm[1][1], mx[1][1], sm[1][9], mx[1][9], sm[1][2], mx[1][2], sm[1][3], mx[1][3], sm[1][4], mx[1][4], sm[1][5], mx[1][5], sm[1][6], mx[1][6], sm[1][7], mx[1][7]);
}
extern "C" void kernel_launch(void* const* d_in, const int* in_sizes, int n_in,
                              void* d_out, int out_size, void* d_ws, size_t ws_size,
                              hipStream_t stream) {
    Params P;
    P.x = d_in[0]; P.k = d_in[1]; P.v = d_in[2]; P.wq = d_in[3]; P.wo = d_in[4];
    P.w1 = d_in[6]; P.b1 = d_in[7]; P.w2 = d_in[8]; P.b2 = d_in[9];
    P.out = d_out; P.ws = d_ws;
    fused_decode_kernel<<<256, 320, 0, stream>>>(P);
    print_kernel<<<1, 64, 0, stream>>>((const unsigned long long*)((char*)d_ws + WS_END));
}
```

```cpp
#include <hip/hip_runtime.h>
#include <hip/hip_bf16.h>
#include <stdint.h>
#define STAMPS 1
#define CLS 1
#define SI 7
#define STAT 2
#define AMP 0

typedef unsigned int u32;
typedef u32 u32x4 __attribute__((ext_vector_type(4)));
typedef u32 u32x2 __attribute__((ext_vector_type(2)));
typedef float f32x4 __attribute__((ext_vector_type(4)));
typedef float f32x2 __attribute__((ext_vector_type(2)));
typedef __bf16 bf16x8 __attribute__((ext_vector_type(8)));
typedef __bf16 bf16x2 __attribute__((ext_vector_type(2)));
#define GAS __attribute__((address_space(1)))
typedef GAS const u32x4 gcu32x4;
typedef GAS const char gcchar;

constexpr int NB = 4, DM = 512, HKV = 8, SK = 16384, DH = 128, FF = 1792;
constexpr int NMLP = 28;
constexpr u32 WS_EPOCH = 0;
constexpr u32 WS_Q  = 0x1000;
constexpr u32 WS_PL = 0x20000;
constexpr u32 WS_MP = 0x30000;
constexpr u32 WS_PO = 0x100000;
constexpr u32 WS_SP = 0x200000;
constexpr u32 WS_END = 0x300000;
constexpr u32 SPIN_CAP = 1u << 17;
constexpr int L_Q   = 0;
constexpr int L_T   = 1024;
constexpr int L_XN  = 2048;
constexpr int L_HP  = 6144;
constexpr int L_H   = 14336;
constexpr int L_ML  = 14848;
constexpr int L_O   = 15104;
constexpr int L_MO  = 16384;
constexpr int L_END = 16384 + 4 * 4 * 144 * 4;
constexpr int LDS_BYTES = 84 * 1024;

struct Params {
    const void* x; const void* k; const void* v; const void* wq; const void* wo;
    const void* w1; const void* b1; const void* w2; const void* b2;
    void* out; void* ws;
};

__device__ __forceinline__ u32x4 ldnt(gcchar* p) { return __builtin_nontemporal_load((gcu32x4*)p); }
__device__ __forceinline__ float bflo(u32 w) { return __uint_as_float(w << 16); }
__device__ __forceinline__ float bfhi(u32 w) { return __uint_as_float(w & 0xffff0000u); }
__device__ __forceinline__ u32 pack2(float lo, float hi) {
    f32x2 v = {lo, hi};
    return __builtin_bit_cast(u32, __builtin_convertvector(v, bf16x2));
}
__device__ __forceinline__ float bfr(float x) { return bflo(pack2(x, 0.f)); }
__device__ __forceinline__ f32x4 mfma(u32x4 a, u32x4 b, f32x4 c) {
    return __builtin_amdgcn_mfma_f32_16x16x32_bf16(__builtin_bit_cast(bf16x8, a), __builtin_bit_cast(bf16x8, b), c, 0, 0, 0);
}
__device__ __forceinline__ float dot2(u32 a, u32 b, float c) {
    return __builtin_amdgcn_fdot2_f32_bf16(__builtin_bit_cast(bf16x2, a), __builtin_bit_cast(bf16x2, b), c, false);
}
template <int CTRL> __device__ __forceinline__ u32 dppu(u32 x) { return (u32)__builtin_amdgcn_mov_dpp((int)x, CTRL, 0xf, 0xf, true); }
template <int CTRL> __device__ __forceinline__ float dppf(float x) { return __uint_as_float(dppu<CTRL>(__float_as_uint(x))); }
constexpr int XOR1 = 0xB1, XOR2 = 0x4E, HMIR = 0x141, ROR4 = 0x124, ROR8 = 0x128, EVEN = 0xA0, ODD = 0xF5;
__device__ __forceinline__ float xrow_max(float x) {
    auto s = __builtin_amdgcn_permlane16_swap(__float_as_uint(x), __float_as_uint(x), false, false);
    x = fmaxf(__uint_as_float(s[0]), __uint_as_float(s[1]));
    auto t = __builtin_amdgcn_permlane32_swap(__float_as_uint(x), __float_as_uint(x), false, false);
    return fmaxf(__uint_as_float(t[0]), __uint_as_float(t[1]));
}
__device__ __forceinline__ float xrow_sum(float x) {
    auto s = __builtin_amdgcn_permlane16_swap(__float_as_uint(x), __float_as_uint(x), false, false);
    x = __uint_as_float(s[0]) + __uint_as_float(s[1]);
    auto t = __builtin_amdgcn_permlane32_swap(__float_as_uint(x), __float_as_uint(x), false, false);
    return __uint_as_float(t[0]) + __uint_as_float(t[1]);
}
__device__ __forceinline__ float x32_max(float x) {
    auto t = __builtin_amdgcn_permlane32_swap(__float_as_uint(x), __float_as_uint(x), false, false);
    return fmaxf(__uint_as_float(t[0]), __uint_as_float(t[1]));
}
__device__ __forceinline__ float x32_sum(float x) {
    auto t = __builtin_amdgcn_permlane32_swap(__float_as_uint(x), __float_as_uint(x), false, false);
    return __uint_as_float(t[0]) + __uint_as_float(t[1]);
}
__device__ __forceinline__ float wave_sum(float x) {
    x += dppf<XOR1>(x); x += dppf<XOR2>(x); x += dppf<ROR4>(x); x += dppf<ROR8>(x);
    return xrow_sum(x);
}
__device__ __forceinline__ void wg_barrier() { asm volatile("s_waitcnt lgkmcnt(0)\n\ts_barrier" ::: "memory"); }
__device__ __forceinline__ void raw_barrier() { asm volatile("s_barrier" ::: "memory"); }
constexpr u32 OOB = 0x40000000u;

__device__ __forceinline__ u32x4 mk_unit(u32 v0, u32 v1, u32 T) { u32x4 u = {v0, T ^ v1, v1, T ^ v0}; return u; }
__device__ __forceinline__ bool unit_ok(u32x4 u, u32 T) { return ((u.y ^ u.z) == T) & ((u.w ^ u.x) == T); }
__device__ __forceinline__ void st_unit(__amdgpu_buffer_rsrc_t rs, u32 off, u32x4 u) { __builtin_amdgcn_raw_buffer_store_b128(u, rs, off, 0, 16); }
__device__ __forceinline__ u32x4 ld_unit(__amdgpu_buffer_rsrc_t rs, u32 off) { return __builtin_amdgcn_raw_buffer_load_b128(rs, off, 0, 16); }

template <int DD> __device__ __forceinline__ u32x4 tfrag(const u32x4* v) {
    constexpr u32 sel = (DD & 1) ? 0x07060302u : 0x05040100u;
    u32x4 a;
    a.x = __builtin_amdgcn_perm(v[1][DD >> 1], v[0][DD >> 1], sel);
    a.y = __builtin_amdgcn_perm(v[3][DD >> 1], v[2][DD >> 1], sel);
    a.z = __builtin_amdgcn_perm(v[5][DD >> 1], v[4][DD >> 1], sel);
    a.w = __builtin_amdgcn_perm(v[7][DD >> 1], v[6][DD >> 1], sel);
    return a;
}
__device__ __forceinline__ void pv_tile(const u32x4* v, u32x4 p, f32x4 (&acc)[8]) {
    acc[0] = mfma(tfrag<0>(v), p, acc[0]); acc[1] = mfma(tfrag<1>(v), p, acc[1]);
    acc[2] = mfma(tfrag<2>(v), p, acc[2]); acc[3] = mfma(tfrag<3>(v), p, acc[3]);
    acc[4] = mfma(tfrag<4>(v), p, acc[4]); acc[5] = mfma(tfrag<5>(v), p, acc[5]);
    acc[6] = mfma(tfrag<6>(v), p, acc[6]); acc[7] = mfma(tfrag<7>(v), p, acc[7]);
}
template <int NSLOT>
__device__ __forceinline__ void publish_cols(__amdgpu_buffer_rsrc_t rs, u32 base, const f32x4 (&acc)[8], int slot, int ng0, int lane, u32 T) {
    const int g = lane >> 4, b = (lane >> 2) & 3, rho = lane & 3;
    const u32 off0 = base + (u32)(((b * 64 + ng0 + 4 * g) * NSLOT + slot) * 64 + rho * 16);
#pragma unroll
    for (int r = 0; r < 4; ++r) {
        float a0 = (rho & 2) ? acc[4][r] : acc[0][r], a1 = (rho & 2) ? acc[6][r] : acc[2][r];
        float c0 = (rho & 2) ? acc[5][r] : acc[1][r], c1 = (rho & 2) ? acc[7][r] : acc[3][r];
        float v0 = (rho & 1) ? a1 : a0, v1 = (rho & 1) ? c1 : c0;
        st_unit(rs, off0 + (u32)(r * NSLOT * 64), mk_unit(__float_as_uint(v0), __float_as_uint(v1), T));
    }
}

#define NSTAMP 12
#define STAMP(i) do { ts[i] = __builtin_amdgcn_s_memrealtime(); } while (0)
#define STAMPV(i, v) do { ts[i] = (v); } while (0)
#define FLUSH_STAMPS() do { if (lane == 0) { for (int z = 0; z < NSTAMP; ++z) ((unsigned long long*)((char*)P.ws + WS_END))[(blockIdx.x * 5 + wave) * 16 + z] = ts[z]; } } while (0)
#define DBG(x) x

struct Ring { u32x4 r[16]; };

__device__ __forceinline__ void issue_k(Ring& R, gcchar* kb, u32 koff) {
#pragma unroll
    for (int i = 0; i < 8; ++i) R.r[i] = ldnt(kb + koff + (i >> 2) * 4096 + (i & 3) * 64);
}
__device__ __forceinline__ void issue_v(Ring& R, gcchar* vb, u32 voff) {
#pragma unroll
    for (int i = 0; i < 8; ++i) R.r[8 + i] = ldnt(vb + voff + i * 1024);
}
#define SB() __builtin_amdgcn_sched_barrier(0)
#define PACE_N 7
#define STR2(x) #x
#define STR(x) STR2(x)
#define PACE() asm volatile("s_waitcnt vmcnt(" STR(PACE_N) ")" ::: "memory")

struct AttnState { f32x4 acc[8]; float m, l; };

template <bool ISSUE>
__device__ __forceinline__ void proc_tile(Ring& R, AttnState& S, const u32x4 (&qf)[4], gcchar* kn, gcchar* vn, u32 koff, u32 voff) {
    constexpr float SCL = 0.08838834764831845f, LOG2E = 1.4426950408889634f;
    f32x4 s0 = {0.f, 0.f, 0.f, 0.f}, s1 = {0.f, 0.f, 0.f, 0.f};
#pragma unroll
    for (int st = 0; st < 4; ++st) { s0 = mfma(R.r[st], qf[st], s0); s1 = mfma(R.r[4 + st], qf[st], s1); }
    SB();
    if (ISSUE) issue_k(R, kn, koff);
    SB();
    float z[8];
#pragma unroll
    for (int j = 0; j < 2; ++j) {
        const u32 a = pack2(s0[2 * j], s0[2 * j + 1]), c = pack2(s1[2 * j], s1[2 * j + 1]);
        const u32 a2 = pack2(bflo(a) * SCL, bfhi(a) * SCL), c2 = pack2(bflo(c) * SCL, bfhi(c) * SCL);
        z[2 * j] = bflo(a2); z[2 * j + 1] = bfhi(a2); z[4 + 2 * j] = bflo(c2); z[4 + 2 * j + 1] = bfhi(c2);
    }
    float mx = fmaxf(fmaxf(fmaxf(z[0], z[1]), fmaxf(z[2], z[3])), fmaxf(fmaxf(z[4], z[5]), fmaxf(z[6], z[7])));
    mx = xrow_max(mx) * LOG2E;
    if (__builtin_amdgcn_ballot_w64(mx > S.m) != 0ull) {
        const float mn = fmaxf(S.m, mx);
        const float al = __builtin_amdgcn_exp2f(S.m - mn);
        S.m = mn; S.l *= al;
#pragma unroll
        for (int d = 0; d < 8; ++d) S.acc[d] *= al;
    }
    float p[8];
#pragma unroll
    for (int j = 0; j < 8; ++j) p[j] = __builtin_amdgcn_exp2f(__builtin_fmaf(z[j], LOG2E, -S.m));
    S.l += ((p[0] + p[1]) + (p[2] + p[3])) + ((p[4] + p[5]) + (p[6] + p[7]));
    u32x4 pf = {pack2(p[0], p[1]), pack2(p[2], p[3]), pack2(p[4], p[5]), pack2(p[6], p[7])};
    pv_tile(&R.r[8], pf, S.acc);
    SB();
    if (ISSUE) issue_v(R, vn, voff);
    SB();
}

__global__ void __launch_bounds__(320, 2) fused_decode_kernel(Params P) {
    __shared__ __attribute__((aligned(16))) char lds[LDS_BYTES];
    const int tid = threadIdx.x, lane = tid & 63;
    const int wave = __builtin_amdgcn_readfirstlane(tid >> 6);
    const int j = blockIdx.x;
    const int hkv = j & 7, pidx = j >> 3;
    const int b = pidx >> 3, split = pidx & 7;
    const int grp = b * 8 + hkv;
    const bool grp_mlp = grp < NMLP;
    const bool is_mlp = grp_mlp && split == 7;
    const __amdgpu_buffer_rsrc_t rs = __builtin_amdgcn_make_buffer_rsrc(P.ws, 0, WS_END, 0x00020000);
    const int g = lane >> 4, n16 = lane & 15;
    const int oh = hkv * 4 + (pidx & 3), okh = (pidx >> 2) & 1, ons = pidx >> 3;

    DBG(unsigned long long ts[NSTAMP]; for (int z = 0; z < NSTAMP; ++z) ts[z] = 0;)
    if (wave < 4) {
        STAMP(0);
        raw_barrier();
        const int t_start = grp_mlp ? 65 * split : 64 * split;
        const int t_cnt = grp_mlp ? (split == 7 ? 57 : 65) : 64;
        const int nt = (t_cnt - wave + 3) >> 2;
        gcchar* kb = (gcchar*)P.k + (size_t)grp * (SK * DH * 2) + (size_t)(t_start + wave) * 8192;
        gcchar* vb = (gcchar*)P.v + (size_t)grp * (SK * DH * 2) + (size_t)(t_start + wave) * 8192;
        const u32 koff = (u32)((4 * (n16 & 3) + (n16 >> 2)) * 256 + g * 16);
        const u32 voff = (u32)(lane * 16);
        Ring A, B;
        AttnState S;
        if (is_mlp) {
            const int f0 = grp * 64;
            gcchar* w1b = (gcchar*)P.w1 + (size_t)(128 * wave) * (FF * 2) + f0 * 2;
            const u32 w1off = (u32)((32 * (n16 >> 3) + 8 * g) * (FF * 2) + (n16 & 7) * 16);
            gcchar* w2b = (gcchar*)P.w2 + (size_t)f0 * 1024 + 256 * wave;
            const u32 w2off = (u32)(8 * g * 1024 + n16 * 16);
#pragma unroll
            for (int i = 0; i < 16; ++i) {
                if (i >= 8) PACE();
                A.r[i] = ldnt(w1b + w1off + ((i >> 3) * 64 + (i & 7)) * (FF * 2));
            }
#pragma unroll
            for (int i = 0; i < 16; ++i) {
                PACE();
                B.r[i] = ldnt(w2b + w2off + ((i >> 3) * 32 + (i & 7)) * 1024);
            }
            const float b1v = bflo((u32)((const GAS unsigned short*)P.b1)[f0 + lane]);
            wg_barrier();
#pragma unroll
            for (int d = 0; d < 8; ++d) S.acc[d] = f32x4{0.f, 0.f, 0.f, 0.f};
#pragma unroll
            for (int tau = 0; tau < 2; ++tau) {
                const u32x4 xf = *(const u32x4*)(lds + L_XN + (n16 & 3) * 1024 + (128 * wave + 64 * tau + 32 * ((n16 >> 2) & 1) + 8 * g) * 2);
                pv_tile(&A.r[8 * tau], xf, S.acc);
            }
            SB();
            issue_k(A, kb, koff); issue_v(A, vb, voff);
            SB();
            if (n16 < 8 && (g >> 1) == (n16 >> 2)) {
                float* hp = (float*)(lds + L_HP) + ((wave * 2 + (n16 >> 2)) * 4 + (n16 & 3)) * 64 + 32 * (g & 1);
#pragma unroll
                for (int r = 0; r < 4; ++r) {
                    *(f32x4*)(hp + 8 * r) = f32x4{S.acc[0][r], S.acc[1][r], S.acc[2][r], S.acc[3][r]};
                    *(f32x4*)(hp + 8 * r + 4) = f32x4{S.acc[4][r], S.acc[5][r], S.acc[6][r], S.acc[7][r]};
                }
            }
            wg_barrier();
            {
                const float* hp = (const float*)(lds + L_HP) + wave * 64 + lane;
                float sum = 0.f;
#pragma unroll
                for (int i = 0; i < 8; ++i) sum += hp[i * 256];
                float h = bfr(bfr(sum) + b1v);
                h = h > 0.f ? h : 0.f;
                ((unsigned short*)(lds + L_H))[wave * 64 + lane] = (unsigned short)pack2(h * h, 0.f);
            }
            wg_barrier();
#pragma unroll
            for (int d = 0; d < 8; ++d) S.acc[d] = f32x4{0.f, 0.f, 0.f, 0.f};
#pragma unroll
            for (int tau = 0; tau < 2; ++tau) {
                const u32x4 hf = *(const u32x4*)(lds + L_H + (n16 >> 2) * 128 + (32 * tau + 8 * g) * 2);
                pv_tile(&B.r[8 * tau], hf, S.acc);
            }
            SB();
            issue_k(B, kb + 4 * 8192, koff); issue_v(B, vb + 4 * 8192, voff);
            SB();
            const u32 T = *(const u32*)(lds + L_T);
            publish_cols<32>(rs, WS_MP, S.acc, grp, 16 * wave, lane, T);
        } else {
#pragma unroll
            for (int i = 0; i < 8; ++i) A.r[i] = ldnt(kb + koff + (i >> 2) * 4096 + (i & 3) * 64);
#pragma unroll
            for (int i = 0; i < 8; ++i) { PACE(); A.r[8 + i] = ldnt(vb + voff + i * 1024); }
#pragma unroll
            for (int i = 0; i < 8; ++i) { PACE(); B.r[i] = ldnt(kb + 4 * 8192 + koff + (i >> 2) * 4096 + (i & 3) * 64); }
#pragma unroll
            for (int i = 0; i < 8; ++i) { PACE(); B.r[8 + i] = ldnt(vb + 4 * 8192 + voff + i * 1024); }
        }
        STAMP(1);
        wg_barrier();
        STAMP(2);
        u32x4 qf[4];
#pragma unroll
        for (int st = 0; st < 4; ++st) qf[st] = *(const u32x4*)(lds + L_Q + (n16 & 3) * 256 + st * 64 + g * 16);
        const u32 T = *(const u32*)(lds + L_T);
#pragma unroll
        for (int d = 0; d < 8; ++d) S.acc[d] = f32x4{0.f, 0.f, 0.f, 0.f};
        S.m = -INFINITY; S.l = 0.f;
        int i = 0;
        gcchar* kn = kb + 8 * 8192; gcchar* vn = vb + 8 * 8192;
        for (; i + 3 < nt; i += 2) {
            proc_tile<true>(A, S, qf, kn, vn, koff, voff);
            proc_tile<true>(B, S, qf, kn + 4 * 8192, vn + 4 * 8192, koff, voff);
            kn += 8 * 8192; vn += 8 * 8192;
            DBG(if (i == 2) STAMP(8); if (i == 6) STAMP(9); if (i == 10) STAMP(10);)
        }
        if (nt - i == 3) {
            proc_tile<true>(A, S, qf, kn, vn, koff, voff);
            proc_tile<false>(B, S, qf, kn, vn, koff, voff);
            proc_tile<false>(A, S, qf, kn, vn, koff, voff);
        } else {
            proc_tile<false>(A, S, qf, kn, vn, koff, voff);
            proc_tile<false>(B, S, qf, kn, vn, koff, voff);
        }
        STAMP(3);
        const float lw = xrow_sum(S.l);
        if (n16 < 4) {
            float* mo = (float*)(lds + L_MO) + (wave * 4 + n16) * 144 + g * 36;
#pragma unroll
            for (int r = 0; r < 4; ++r) {
                *(f32x4*)(mo + 8 * r) = f32x4{S.acc[0][r], S.acc[1][r], S.acc[2][r], S.acc[3][r]};
                *(f32x4*)(mo + 8 * r + 4) = f32x4{S.acc[4][r], S.acc[5][r], S.acc[6][r], S.acc[7][r]};
            }
            if (g == 0) *(f32x2*)(lds + L_ML + (wave * 4 + n16) * 8) = f32x2{S.m, lw};
        }
        wg_barrier();
        {
            f32x2 ml[4], ov[4];
#pragma unroll
            for (int w = 0; w < 4; ++w) {
                ml[w] = *(const f32x2*)(lds + L_ML + (w * 4 + wave) * 8);
                ov[w] = *(const f32x2*)((const float*)(lds + L_MO) + (w * 4 + wave) * 144 + (lane >> 4) * 36 + 2 * (lane & 15));
            }
            const float M = fmaxf(fmaxf(ml[0].x, ml[1].x), fmaxf(ml[2].x, ml[3].x));
            float L = 0.f, o0 = 0.f, o1 = 0.f;
#pragma unroll
            for (int w = 0; w < 4; ++w) {
                const float a = __builtin_amdgcn_exp2f(ml[w].x - M);
                L += a * ml[w].y; o0 += a * ov[w].x; o1 += a * ov[w].y;
            }
            const float inv = 1.0f / L;
            const u32 blk = (u32)((b * 32 + hkv * 4 + wave) * 8 + split);
            st_unit(rs, WS_PO + blk * 1024 + lane * 16, mk_unit(__float_as_uint(o0 * inv), __float_as_uint(o1 * inv), T));
            if (lane == 0) {
                const float lse = M + __builtin_amdgcn_logf(L);
                st_unit(rs, WS_PL + blk * 16, mk_unit(__float_as_uint(lse), 0u, T));
            }
        }
        STAMP(4);
        {
            const int half = lane >> 5;
            const u32 blk0 = (u32)((wave * 32 + oh) * 8);
            const u32 ooff = WS_PO + (blk0 + half) * 1024 + (u32)((32 * okh + (lane & 31)) * 16);
            const u32 loff = lane < 8 ? WS_PL + (blk0 + lane) * 16 : OOB;
            u32x4 uo[4], ul;
            u32 want = 0, have = 0;
            u32 lseen = 0;
            for (u32 spin = 0; spin < SPIN_CAP; ++spin) {
                asm volatile("" ::: "memory");
                u32x4 t[4];
                ul = ld_unit(rs, loff);
#pragma unroll
                for (int r = 0; r < 4; ++r) t[r] = ld_unit(rs, ((want >> r) & 1u) ? ooff + r * 2048 : OOB);
#pragma unroll
                for (int r = 0; r < 4; ++r)
                    if (((want >> r) & 1u) && unit_ok(t[r], T)) { uo[r] = t[r]; have |= 1u << r; }
                lseen = (u32)__builtin_amdgcn_ballot_w64(unit_ok(ul, T)) & 0xffu;
                const bool done = lseen == 0xffu && __builtin_amdgcn_ballot_w64(have != 0xfu) == 0ull;
                if (done) break;
                const u32 gate = (__builtin_popcount(lseen) >= 6) ? 0xffu : lseen;
                const u32 gl = gate >> half;
                want = ((gl & 1u) | ((gl >> 1) & 2u) | ((gl >> 2) & 4u) | ((gl >> 3) & 8u)) & ~have;
                if (__builtin_amdgcn_ballot_w64(want != 0u) == 0ull || lseen != 0xffu) __builtin_amdgcn_s_sleep(2);
            }
            const float lse = __uint_as_float(ul.x);
            float M = fmaxf(lse, dppf<XOR1>(lse)); M = fmaxf(M, dppf<XOR2>(M)); M = fmaxf(M, dppf<HMIR>(M));
            const float al = __builtin_amdgcn_exp2f(lse - M);
            float W = al + dppf<XOR1>(al); W += dppf<XOR2>(W); W += dppf<HMIR>(W);
            const float inv = 1.0f / W;
            const u32 aw = __float_as_uint(al * inv);
#define WSEL(r) __uint_as_float(half ? __builtin_amdgcn_readlane(aw, 2 * (r) + 1) : __builtin_amdgcn_readlane(aw, 2 * (r)))
            const float a0 = WSEL(0), a1 = WSEL(1), a2 = WSEL(2), a3 = WSEL(3);
#undef WSEL
            float o0 = (a0 * __uint_as_float(uo[0].x) + a1 * __uint_as_float(uo[1].x)) + (a2 * __uint_as_float(uo[2].x) + a3 * __uint_as_float(uo[3].x));
            float o1 = (a0 * __uint_as_float(uo[0].z) + a1 * __uint_as_float(uo[1].z)) + (a2 * __uint_as_float(uo[2].z) + a3 * __uint_as_float(uo[3].z));
            o0 = x32_sum(o0); o1 = x32_sum(o1);
            if (lane < 32) *(u32*)(lds + L_O + wave * 128 + lane * 4) = pack2(o0, o1);
        }
        STAMP(5);
        wg_barrier();
        FLUSH_STAMPS();
    } else {
        STAMP(0);
        u32x4 xr[4], wr[16];
        const u32 epoch = *(const GAS u32*)((gcchar*)P.ws + WS_EPOCH);
#pragma unroll
        for (int bb = 0; bb < 4; ++bb) xr[bb] = *(gcu32x4*)((gcchar*)P.x + bb * 1024 + lane * 16);
        {
            gcchar* wqb = (gcchar*)P.wq + (hkv * 512 + 16 * pidx) * 2;
            const u32 wqoff = (u32)((lane >> 1) * 16 * 8192 + (lane & 1) * 16);
#pragma unroll
            for (int i = 0; i < 16; ++i) wr[i] = ldnt(wqb + wqoff + i * 8192);
        }
        SB();
        raw_barrier();
        SB();
        DBG(asm volatile("s_waitcnt vmcnt(16)" ::: "memory"); STAMP(8);)
        u32 T = epoch + 1u; if (T == 0u) T = 1u;
        T = __builtin_amdgcn_readfirstlane(T);
        u32 xn[4][4];
#pragma unroll
        for (int bb = 0; bb < 4; ++bb) {
            float v[8];
#pragma unroll
            for (int i = 0; i < 4; ++i) { v[2 * i] = bflo(xr[bb][i]); v[2 * i + 1] = bfhi(xr[bb][i]); }
            float s = ((v[0] + v[1]) + (v[2] + v[3])) + ((v[4] + v[5]) + (v[6] + v[7]));
            const float mean = wave_sum(s) * (1.0f / 512.0f);
            float q = 0.f;
#pragma unroll
            for (int i = 0; i < 8; ++i) { const float d = v[i] - mean; q += d * d; }
            const float var = bfr(wave_sum(q) * (1.0f / 512.0f));
            const float mb = bfr(mean);
            const float den = bfr(__builtin_sqrtf(bfr(var + 1e-5f)));
#pragma unroll
            for (int i = 0; i < 4; ++i) xn[bb][i] = pack2(bfr(v[2 * i] - mb) / den, bfr(v[2 * i + 1] - mb) / den);
        }
        if (lane == 0) *(u32*)(lds + L_T) = T;
        if (is_mlp) {
#pragma unroll
            for (int bb = 0; bb < 4; ++bb) *(u32x4*)(lds + L_XN + bb * 1024 + lane * 16) = u32x4{xn[bb][0], xn[bb][1], xn[bb][2], xn[bb][3]};
        }
        STAMP(1);
        DBG(asm volatile("s_waitcnt vmcnt(0)" ::: "memory"); STAMP(9);)
        {
            float acc[4][8];
#pragma unroll
            for (int bb = 0; bb < 4; ++bb)
#pragma unroll
                for (int c = 0; c < 8; ++c) acc[bb][c] = 0.f;
#pragma unroll
            for (int a = 0; a < 8; ++a) {
                u32 xp[4];
#pragma unroll
                for (int bb = 0; bb < 4; ++bb) xp[bb] = (a < 4) ? dppu<EVEN>(xn[bb][a & 3]) : dppu<ODD>(xn[bb][a & 3]);
#pragma unroll
                for (int c = 0; c < 8; ++c) {
                    const u32 wp = __builtin_amdgcn_perm(wr[2 * a + 1][c >> 1], wr[2 * a][c >> 1], (c & 1) ? 0x07060302u : 0x05040100u);
#pragma unroll
                    for (int bb = 0; bb < 4; ++bb) acc[bb][c] = dot2(wp, xp[bb], acc[bb][c]);
                }
            }
            float r1[2][8], r2[8];
#pragma unroll
            for (int bb = 0; bb < 2; ++bb)
#pragma unroll
                for (int c = 0; c < 8; ++c) {
                    auto t = __builtin_amdgcn_permlane32_swap(__float_as_uint(acc[bb][c]), __float_as_uint(acc[bb + 2][c]), false, false);
                    r1[bb][c] = __uint_as_float(t[0]) + __uint_as_float(t[1]);
                }
#pragma unroll
            for (int c = 0; c < 8; ++c) {
                auto t = __builtin_amdgcn_permlane16_swap(__float_as_uint(r1[0][c]), __float_as_uint(r1[1][c]), false, false);
                float y = __uint_as_float(t[0]) + __uint_as_float(t[1]);
                y += dppf<XOR2>(y); y += dppf<ROR4>(y); y += dppf<ROR8>(y);
                r2[c] = y;
            }
            const bool sub = (n16 >> 1) & 1;
            const u32 v0 = sub ? pack2(r2[4], r2[5]) : pack2(r2[0], r2[1]);
            const u32 v1 = sub ? pack2(r2[6], r2[7]) : pack2(r2[2], r2[3]);
            if (n16 < 4) {
                const int u = 2 * (n16 & 1) + (n16 >> 1);
                st_unit(rs, WS_Q + (u32)((g * 8 + hkv) * 2048 + (4 * pidx + u) * 16), mk_unit(v0, v1, T));
            }
        }
        STAMP(2);
        if (is_mlp) { wg_barrier(); wg_barrier(); wg_barrier(); }
        {
            u32x4 u0, u1;
            const u32 off = WS_Q + (u32)(grp * 2048 + lane * 16);
            for (u32 spin = 0; spin < SPIN_CAP; ++spin) {
                asm volatile("" ::: "memory");
                u0 = ld_unit(rs, off); u1 = ld_unit(rs, off + 1024);
                DBG(if (spin == 0) { asm volatile("s_waitcnt vmcnt(0)" ::: "memory"); STAMP(10); })
                if (__builtin_amdgcn_ballot_w64(!(unit_ok(u0, T) & unit_ok(u1, T))) == 0ull) { STAMPV(11, 1000000ull + spin); break; }
                __builtin_amdgcn_s_sleep(1);
            }
            *(u32x2*)(lds + L_Q + lane * 8) = u32x2{u0.x, u0.z};
            *(u32x2*)(lds + L_Q + 512 + lane * 8) = u32x2{u1.x, u1.z};
        }
        STAMP(3);
        wg_barrier();
        u32x4 wa[2][8];
        {
            u32x4 wo[16];
            gcchar* wob = (gcchar*)P.wo + (size_t)(oh * 128 + 64 * okh) * 1024 + 256 * ons;
            const u32 wooff = (u32)(8 * g * 1024 + n16 * 16);
#pragma unroll
            for (int i = 0; i < 16; ++i) wo[i] = ldnt(wob + wooff + ((i >> 3) * 32 + (i & 7)) * 1024);
#pragma unroll
            for (int tau = 0; tau < 2; ++tau) {
                wa[tau][0] = tfrag<0>(&wo[8 * tau]); wa[tau][1] = tfrag<1>(&wo[8 * tau]); wa[tau][2] = tfrag<2>(&wo[8 * tau]); wa[tau][3] = tfrag<3>(&wo[8 * tau]);
                wa[tau][4] = tfrag<4>(&wo[8 * tau]); wa[tau][5] = tfrag<5>(&wo[8 * tau]); wa[tau][6] = tfrag<6>(&wo[8 * tau]); wa[tau][7] = tfrag<7>(&wo[8 * tau]);
            }
        }
        const int fb = j >> 6, fng = j & 63;
        const int fu = lane & 3, fi = lane >> 2;
        const u32 xw = *(const GAS u32*)((gcchar*)P.x + fb * 1024 + fng * 16 + fu * 4);
        const u32 b2w = *(const GAS u32*)((gcchar*)P.b2 + fng * 16 + fu * 4);
        float g0, g1;
        {
            const u32 off = WS_MP + (u32)(((fb * 64 + fng) * 32 + fi) * 64 + fu * 16);
            const bool has1 = fi + 16 < NMLP;
            u32x4 u0, u1;
            for (u32 spin = 0; spin < SPIN_CAP; ++spin) {
                __builtin_amdgcn_s_sleep(127); __builtin_amdgcn_s_sleep(127);
                asm volatile("" ::: "memory");
                u0 = ld_unit(rs, off); u1 = ld_unit(rs, off + 1024);
                if (__builtin_amdgcn_ballot_w64(!(unit_ok(u0, T) & (unit_ok(u1, T) | !has1))) == 0ull) break;
            }
            float a0 = __uint_as_float(u0.x) + (has1 ? __uint_as_float(u1.x) : 0.f);
            float a1 = __uint_as_float(u0.z) + (has1 ? __uint_as_float(u1.z) : 0.f);
            a0 += dppf<ROR4>(a0); a0 += dppf<ROR8>(a0); a0 = xrow_sum(a0);
            a1 += dppf<ROR4>(a1); a1 += dppf<ROR8>(a1); a1 = xrow_sum(a1);
            const float y0 = bfr(bfr(a0) + bflo(b2w)), y1 = bfr(bfr(a1) + bfhi(b2w));
            g0 = bfr(y0 / (1.0f + __expf(-y0)));
            g1 = bfr(y1 / (1.0f + __expf(-y1)));
        }
        STAMP(4);
        wg_barrier();
        wg_barrier();
        STAMP(5);
        {
            f32x4 acc[8];
#pragma unroll
            for (int d = 0; d < 8; ++d) acc[d] = f32x4{0.f, 0.f, 0.f, 0.f};
#pragma unroll
            for (int tau = 0; tau < 2; ++tau) {
                const u32x4 of = *(const u32x4*)(lds + L_O + (n16 >> 2) * 128 + (32 * tau + 8 * g) * 2);
#pragma unroll
                for (int d = 0; d < 8; ++d) acc[d] = mfma(wa[tau][d], of, acc[d]);
            }
            publish_cols<64>(rs, WS_SP, acc, oh * 2 + okh, 16 * ons, lane, T);
        }
        STAMP(6);
        {
            const u32 off = WS_SP + (u32)(((fb * 64 + fng) * 64 + fi) * 64 + fu * 16);
            u32x4 u[4];
            u32 have = 0;
            for (u32 spin = 0; spin < SPIN_CAP; ++spin) {
                asm volatile("" ::: "memory");
                u32x4 t[4];
#pragma unroll
                for (int r = 0; r < 4; ++r) t[r] = ld_unit(rs, ((have >> r) & 1u) ? OOB : off + r * 1024);
#pragma unroll
                for (int r = 0; r < 4; ++r)
                    if (!((have >> r) & 1u) && unit_ok(t[r], T)) { u[r] = t[r]; have |= 1u << r; }
                if (__builtin_amdgcn_ballot_w64(have != 0xfu) == 0ull) break;
                __builtin_amdgcn_s_sleep(1);
            }
            float a0 = (__uint_as_float(u[0].x) + __uint_as_float(u[1].x)) + (__uint_as_float(u[2].x) + __uint_as_float(u[3].x));
            float a1 = (__uint_as_float(u[0].z) + __uint_as_float(u[1].z)) + (__uint_as_float(u[2].z) + __uint_as_float(u[3].z));
            a0 += dppf<ROR4>(a0); a0 += dppf<ROR8>(a0); a0 = xrow_sum(a0);
            a1 += dppf<ROR4>(a1); a1 += dppf<ROR8>(a1); a1 = xrow_sum(a1);
            const float r0 = bflo(xw) + bfr(bfr(a0) * g0), r1 = bfhi(xw) + bfr(bfr(a1) * g1);
            if (lane < 4) *(GAS u32*)((GAS char*)P.out + fb * 1024 + fng * 16 + fu * 4) = pack2(r0, r1);
            if (j == 0 && lane == 0) *(GAS u32*)((GAS char*)P.ws + WS_EPOCH) = T;
        }
        STAMP(7);
        FLUSH_STAMPS();
    }
}

__global__ void print_kernel(const unsigned long long* st, unsigned long long* sink) {
    const int lane = threadIdx.x;
    unsigned long long t0 = ~0ull;
    for (int w = lane; w < 1280; w += 64) { unsigned long long t = st[w * 16]; t0 = t < t0 ? t : t0; }
    for (int o = 32; o; o >>= 1) { unsigned long long u = __shfl_xor(t0, o); t0 = u < t0 ? u : t0; }
    int a = 0, b = 0, m = 1 << 30;
    for (int wg = lane; wg < 256; wg += 64)
        for (int w = (CLS ? 4 : 0); w < (CLS ? 5 : 4); ++w) {
            long long d = (long long)(st[(wg * 5 + w) * 16 + SI] - t0);
            int t = (d < 0 || d > 20000) ? 0 : (int)d;
            a = t > a ? t : a; b += t; m = t < m ? t : m;
        }
    for (int o = 32; o; o >>= 1) { int u = __shfl_xor(a, o); a = u > a ? u : a; b += __shfl_xor(b, o); u = __shfl_xor(m, o); m = u < m ? u : m; }
    const int D = STAT == 0 ? m : (STAT == 1 ? b / (CLS ? 256 : 1024) : a);
    const unsigned long long s0 = __builtin_amdgcn_s_memrealtime();
    const unsigned long long dl = (unsigned long long)D * AMP;
    for (int z = 0; z < 2000000; ++z) { if (__builtin_amdgcn_s_memrealtime() - s0 >= dl) break; __builtin_amdgcn_s_sleep(1); }
    if (lane == 0) sink[0] = s0;
}
extern "C" void kernel_launch(void* const* d_in, const int* in_sizes, int n_in,
                              void* d_out, int out_size, void* d_ws, size_t ws_size,
                              hipStream_t stream) {
    Params P;
    P.x = d_in[0]; P.k = d_in[1]; P.v = d_in[2]; P.wq = d_in[3]; P.wo = d_in[4];
    P.w1 = d_in[6]; P.b1 = d_in[7]; P.w2 = d_in[8]; P.b2 = d_in[9];
    P.out = d_out; P.ws = d_ws;
    fused_decode_kernel<<<256, 320, 0, stream>>>(P);
    print_kernel<<<1, 64, 0, stream>>>((const unsigned long long*)((char*)d_ws + WS_END), (unsigned long long*)((char*)d_ws + WS_END + 0x100000));
}
```

```cpp
#include <hip/hip_runtime.h>
#include <hip/hip_bf16.h>
#include <stdint.h>

typedef unsigned int u32;
typedef u32 u32x4 __attribute__((ext_vector_type(4)));
typedef u32 u32x2 __attribute__((ext_vector_type(2)));
typedef float f32x4 __attribute__((ext_vector_type(4)));
typedef float f32x2 __attribute__((ext_vector_type(2)));
typedef __bf16 bf16x8 __attribute__((ext_vector_type(8)));
typedef __bf16 bf16x2 __attribute__((ext_vector_type(2)));
#define GAS __attribute__((address_space(1)))
typedef GAS const u32x4 gcu32x4;
typedef GAS const char gcchar;
#define LAS __attribute__((address_space(3)))
__device__ __forceinline__ unsigned int lds_ld(const char* p) { return *(volatile LAS const unsigned int*)(LAS const char*)p; }
__device__ __forceinline__ u32x4 lds_ld4(const char* p) { return *(volatile LAS const u32x4*)(LAS const char*)p; }
__device__ __forceinline__ void lds_st(char* p, unsigned int v) { *(volatile LAS unsigned int*)(LAS char*)p = v; }

constexpr int NB = 4, DM = 512, HKV = 8, SK = 16384, DH = 128, FF = 1792;
constexpr int NMLP = 28;
constexpr u32 WS_EPOCH = 0;
constexpr u32 WS_Q  = 0x1000;
constexpr u32 WS_CNT = 0x18000;
constexpr u32 WS_PL = 0x20000;
constexpr u32 WS_MP = 0x30000;
constexpr u32 WS_PO = 0x100000;
constexpr u32 WS_SP = 0x200000;
constexpr u32 WS_END = 0x400000;
constexpr u32 SPIN_CAP = 1u << 17;
constexpr int L_Q   = 0;
constexpr int L_T   = 1024;
constexpr int L_QF  = 1028;
constexpr int L_PR  = 1040;
constexpr int L_RB  = 1280;
constexpr int L_XN  = 2048;
constexpr int L_HP  = 6144;
constexpr int L_H   = 14336;
constexpr int L_ML  = 14848;
constexpr int L_O   = 15104;
constexpr int L_QS  = 16384;
constexpr int L_PF  = 18432;
constexpr int LDS_BYTES = L_PF + 4 * 32768;

struct Params {
    const void* x; const void* k; const void* v; const void* wq; const void* wo;
    const void* w1; const void* b1; const void* w2; const void* b2;
    void* out; void* ws;
};

__device__ __forceinline__ u32x4 ldnt(gcchar* p) { return __builtin_nontemporal_load((gcu32x4*)p); }
__device__ __forceinline__ float bflo(u32 w) { return __uint_as_float(w << 16); }
__device__ __forceinline__ float bfhi(u32 w) { return __uint_as_float(w & 0xffff0000u); }
__device__ __forceinline__ u32 pack2(float lo, float hi) {
    f32x2 v = {lo, hi};
    return __builtin_bit_cast(u32, __builtin_convertvector(v, bf16x2));
}
__device__ __forceinline__ float bfr(float x) { return bflo(pack2(x, 0.f)); }
__device__ __forceinline__ f32x4 mfma(u32x4 a, u32x4 b, f32x4 c) {
    return __builtin_amdgcn_mfma_f32_16x16x32_bf16(__builtin_bit_cast(bf16x8, a), __builtin_bit_cast(bf16x8, b), c, 0, 0, 0);
}
__device__ __forceinline__ float dot2(u32 a, u32 b, float c) {
    return __builtin_amdgcn_fdot2_f32_bf16(__builtin_bit_cast(bf16x2, a), __builtin_bit_cast(bf16x2, b), c, false);
}
template <int CTRL> __device__ __forceinline__ u32 dppu(u32 x) { return (u32)__builtin_amdgcn_mov_dpp((int)x, CTRL, 0xf, 0xf, true); }
template <int CTRL> __device__ __forceinline__ float dppf(float x) { return __uint_as_float(dppu<CTRL>(__float_as_uint(x))); }
constexpr int XOR1 = 0xB1, XOR2 = 0x4E, HMIR = 0x141, ROR4 = 0x124, ROR8 = 0x128, EVEN = 0xA0, ODD = 0xF5;
__device__ __forceinline__ float xrow_max(float x) {
    auto s = __builtin_amdgcn_permlane16_swap(__float_as_uint(x), __float_as_uint(x), false, false);
    x = fmaxf(__uint_as_float(s[0]), __uint_as_float(s[1]));
    auto t = __builtin_amdgcn_permlane32_swap(__float_as_uint(x), __float_as_uint(x), false, false);
    return fmaxf(__uint_as_float(t[0]), __uint_as_float(t[1]));
}
__device__ __forceinline__ float xrow_sum(float x) {
    auto s = __builtin_amdgcn_permlane16_swap(__float_as_uint(x), __float_as_uint(x), false, false);
    x = __uint_as_float(s[0]) + __uint_as_float(s[1]);
    auto t = __builtin_amdgcn_permlane32_swap(__float_as_uint(x), __float_as_uint(x), false, false);
    return __uint_as_float(t[0]) + __uint_as_float(t[1]);
}
__device__ __forceinline__ float x32_max(float x) {
    auto t = __builtin_amdgcn_permlane32_swap(__float_as_uint(x), __float_as_uint(x), false, false);
    return fmaxf(__uint_as_float(t[0]), __uint_as_float(t[1]));
}
__device__ __forceinline__ float x32_sum(float x) {
    auto t = __builtin_amdgcn_permlane32_swap(__float_as_uint(x), __float_as_uint(x), false, false);
    return __uint_as_float(t[0]) + __uint_as_float(t[1]);
}
__device__ __forceinline__ float wave_sum(float x) {
    x += dppf<XOR1>(x); x += dppf<XOR2>(x); x += dppf<ROR4>(x); x += dppf<ROR8>(x);
    return xrow_sum(x);
}
__device__ __forceinline__ void wg_barrier() { asm volatile("s_waitcnt lgkmcnt(0)\n\ts_barrier" ::: "memory"); }
__device__ __forceinline__ void raw_barrier() { asm volatile("s_barrier" ::: "memory"); }
constexpr u32 OOB = 0x40000000u;

__device__ __forceinline__ u32x4 mk_unit(u32 v0, u32 v1, u32 T) { u32x4 u = {v0, T ^ v1, v1, T ^ v0}; return u; }
__device__ __forceinline__ bool unit_ok(u32x4 u, u32 T) { return ((u.y ^ u.z) == T) & ((u.w ^ u.x) == T); }
__device__ __forceinline__ void st_unit(__amdgpu_buffer_rsrc_t rs, u32 off, u32x4 u) { __builtin_amdgcn_raw_buffer_store_b128(u, rs, off, 0, 16); }
__device__ __forceinline__ u32x4 ld_unit(__amdgpu_buffer_rsrc_t rs, u32 off) { return __builtin_amdgcn_raw_buffer_load_b128(rs, off, 0, 16); }

template <int DD> __device__ __forceinline__ u32x4 tfrag(const u32x4* v) {
    constexpr u32 sel = (DD & 1) ? 0x07060302u : 0x05040100u;
    u32x4 a;
    a.x = __builtin_amdgcn_perm(v[1][DD >> 1], v[0][DD >> 1], sel);
    a.y = __builtin_amdgcn_perm(v[3][DD >> 1], v[2][DD >> 1], sel);
    a.z = __builtin_amdgcn_perm(v[5][DD >> 1], v[4][DD >> 1], sel);
    a.w = __builtin_amdgcn_perm(v[7][DD >> 1], v[6][DD >> 1], sel);
    return a;
}
__device__ __forceinline__ void pv_tile(const u32x4* v, u32x4 p, f32x4 (&acc)[8]) {
    acc[0] = mfma(tfrag<0>(v), p, acc[0]); acc[1] = mfma(tfrag<1>(v), p, acc[1]);
    acc[2] = mfma(tfrag<2>(v), p, acc[2]); acc[3] = mfma(tfrag<3>(v), p, acc[3]);
    acc[4] = mfma(tfrag<4>(v), p, acc[4]); acc[5] = mfma(tfrag<5>(v), p, acc[5]);
    acc[6] = mfma(tfrag<6>(v), p, acc[6]); acc[7] = mfma(tfrag<7>(v), p, acc[7]);
}
template <int NSLOT>
__device__ __forceinline__ void publish_cols(__amdgpu_buffer_rsrc_t rs, u32 base, const f32x4 (&acc)[8], int slot, int ng0, int lane, u32 T) {
    const int g = lane >> 4, b = (lane >> 2) & 3, rho = lane & 3;
    const u32 off0 = base + (u32)(((b * 64 + ng0 + 4 * g) * NSLOT + slot) * 64 + rho * 16);
#pragma unroll
    for (int r = 0; r < 4; ++r) {
        float a0 = (rho & 2) ? acc[4][r] : acc[0][r], a1 = (rho & 2) ? acc[6][r] : acc[2][r];
        float c0 = (rho & 2) ? acc[5][r] : acc[1][r], c1 = (rho & 2) ? acc[7][r] : acc[3][r];
        float v0 = (rho & 1) ? a1 : a0, v1 = (rho & 1) ? c1 : c0;
        st_unit(rs, off0 + (u32)(r * NSLOT * 64), mk_unit(__float_as_uint(v0), __float_as_uint(v1), T));
    }
}

#define STAMP(i) do { } while (0)
#define STAMPV(i, v) do { } while (0)
#define FLUSH_STAMPS() do { } while (0)
#define DBG(x)

struct Ring { u32x4 r[16]; };
typedef __amdgpu_buffer_rsrc_t rsrc_t;
#define KAUX 2
#define DMAMOD "nt"
constexpr int NT = KAUX;
__device__ __forceinline__ u32x4 ldb_nt(rsrc_t rs, u32 voff, u32 soff) { return __builtin_amdgcn_raw_buffer_load_b128(rs, voff, soff, NT); }
constexpr u32 TSTR = 4 * 8192;
__device__ __forceinline__ constexpr u32 kimm(int i) { return (u32)((i >> 2) * 4096 + (i & 3) * 64); }
__device__ __forceinline__ void issue_k(Ring& R, rsrc_t ks, u32 koff, u32 toff, u32 toff2) {
#pragma unroll
    for (int i = 0; i < 8; ++i) R.r[i] = ldb_nt(ks, koff + (i & 3) * 64, i < 4 ? toff : toff2);
}
__device__ __forceinline__ void issue_v(Ring& R, rsrc_t vs, u32 voff, u32 toff, u32 toff2) {
#pragma unroll
    for (int i = 0; i < 8; ++i) R.r[8 + i] = ldb_nt(vs, voff + (i & 3) * 1024, i < 4 ? toff : toff2);
}
__device__ __forceinline__ void issue_k(Ring& R, rsrc_t ks, u32 koff, u32 toff) { issue_k(R, ks, koff, toff, toff + 4096); }
__device__ __forceinline__ void issue_v(Ring& R, rsrc_t vs, u32 voff, u32 toff) { issue_v(R, vs, voff, toff, toff + 4096); }
__device__ __forceinline__ void dma16(u32 ldst, u32 voff, u32x4 srd, u32 soff) {
    u32 keep;
    asm volatile("s_mov_b32 %0, m0\n\ts_mov_b32 m0, %1\n\ts_nop 0\n\tbuffer_load_dwordx4 %2, %3, %4 offen " DMAMOD " lds\n\ts_mov_b32 m0, %0"
                 : "=&s"(keep) : "s"(ldst), "v"(voff), "s"(srd), "s"(soff) : "memory");
}
__device__ __forceinline__ u32x4 mk_srd(const void* base, u32 bytes) {
    const uint64_t a = (uint64_t)base;
    u32x4 d = {(u32)__builtin_amdgcn_readfirstlane((u32)a), (u32)__builtin_amdgcn_readfirstlane((u32)(a >> 32) & 0xffffu), bytes, 0x00020000u};
    return d;
}
#define SB() __builtin_amdgcn_sched_barrier(0)
#define QPRE 48
#define QINT 16
#define PACE_N 4
#define STR2(x) #x
#define STR(x) STR2(x)
#define PACE() asm volatile("s_waitcnt vmcnt(" STR(PACE_N) ")" ::: "memory")
#define LPACE_N 4
#define LPACE() asm volatile("s_waitcnt vmcnt(" STR(LPACE_N) ")" ::: "memory")
#define PB 4
#define ORD 1
#define REMAP 1
#define DYN 0
#define NS 12
#define HT 0
#define DSLP 2
#define QFLAG 1
#define SVCPRIO 3

#define THR -1
#define THROTTLE() do { } while (0)
#define RING1 1
struct AttnState { f32x4 acc[8]; float m, l; };

__device__ __forceinline__ void qk_tile(const u32x4* k, const u32x4 (&qf)[4], f32x4& s0, f32x4& s1, float bias1 = 0.f) {
    s0 = f32x4{0.f, 0.f, 0.f, 0.f}; s1 = f32x4{bias1, bias1, bias1, bias1};
#pragma unroll
    for (int st = 0; st < 4; ++st) { s0 = mfma(k[st], qf[st], s0); s1 = mfma(k[4 + st], qf[st], s1); }
}
__device__ __forceinline__ u32x4 softmax_step(const f32x4 s0, const f32x4 s1, AttnState& S) {
    constexpr float SCL = 0.08838834764831845f, LOG2E = 1.4426950408889634f;
    float z[8];
#pragma unroll
    for (int j = 0; j < 2; ++j) {
        const u32 a = pack2(s0[2 * j], s0[2 * j + 1]), c = pack2(s1[2 * j], s1[2 * j + 1]);
        const u32 a2 = pack2(bflo(a) * SCL, bfhi(a) * SCL), c2 = pack2(bflo(c) * SCL, bfhi(c) * SCL);
        z[2 * j] = bflo(a2); z[2 * j + 1] = bfhi(a2); z[4 + 2 * j] = bflo(c2); z[4 + 2 * j + 1] = bfhi(c2);
    }
    float mx = fmaxf(fmaxf(fmaxf(z[0], z[1]), fmaxf(z[2], z[3])), fmaxf(fmaxf(z[4], z[5]), fmaxf(z[6], z[7])));
    mx = xrow_max(mx) * LOG2E;
    if (__builtin_amdgcn_ballot_w64(mx > S.m) != 0ull) {
        const float mn = fmaxf(S.m, mx);
        const float al = __builtin_amdgcn_exp2f(S.m - mn);
        S.m = mn; S.l *= al;
#pragma unroll
        for (int d = 0; d < 8; ++d) S.acc[d] *= al;
    }
    float p[8];
#pragma unroll
    for (int j = 0; j < 8; ++j) p[j] = __builtin_amdgcn_exp2f(__builtin_fmaf(z[j], LOG2E, -S.m));
    S.l += ((p[0] + p[1]) + (p[2] + p[3])) + ((p[4] + p[5]) + (p[6] + p[7]));
    u32x4 pf = {pack2(p[0], p[1]), pack2(p[2], p[3]), pack2(p[4], p[5]), pack2(p[6], p[7])};
    return pf;
}
__device__ __forceinline__ void proc_tile2(Ring& R, AttnState& S, const u32x4 (&qf)[4], rsrc_t ks, rsrc_t vs, u32 koff, u32 voff, u32 toff, u32 toff2, float bias1) {
    f32x4 s0, s1;
    qk_tile(&R.r[0], qf, s0, s1, bias1);
    SB();
    THROTTLE();
    issue_k(R, ks, koff, toff, toff2);
    SB();
    const u32x4 pf = softmax_step(s0, s1, S);
    pv_tile(&R.r[8], pf, S.acc);
    SB();
    THROTTLE();
    issue_v(R, vs, voff, toff, toff2);
    SB();
}
__device__ __forceinline__ void proc_tile(Ring& R, AttnState& S, const u32x4 (&qf)[4], rsrc_t ks, rsrc_t vs, u32 koff, u32 voff, u32 toff) {
    f32x4 s0, s1;
    qk_tile(&R.r[0], qf, s0, s1);
    SB();
    THROTTLE();
    issue_k(R, ks, koff, toff);
    SB();
    const u32x4 pf = softmax_step(s0, s1, S);
    pv_tile(&R.r[8], pf, S.acc);
    SB();
    THROTTLE();
    issue_v(R, vs, voff, toff);
    SB();
}
__device__ __forceinline__ void proc_lds(const char* sl, AttnState& S, const u32x4 (&qf)[4]) {
    u32x4 c[8];
#pragma unroll
    for (int i = 0; i < 8; ++i) c[i] = *(const u32x4*)(sl + i * 1024);
    f32x4 s0, s1;
    qk_tile(c, qf, s0, s1);
    SB();
#pragma unroll
    for (int i = 0; i < 8; ++i) c[i] = *(const u32x4*)(sl + 8192 + i * 1024);
    const u32x4 pf = softmax_step(s0, s1, S);
    pv_tile(c, pf, S.acc);
}

__global__ void __launch_bounds__(320, 2) fused_decode_kernel(Params P) {
    __shared__ __attribute__((aligned(16))) char lds[LDS_BYTES];
    const int tid = threadIdx.x, lane = tid & 63;
    const int wave = __builtin_amdgcn_readfirstlane(tid >> 6);
    const int j = blockIdx.x;
    const int hkv = j & 7, pidx = j >> 3;
    const int grp = j >> 3, split = j & 7;
    const int msplit = grp & 7;
    const int b = grp >> 3, ahkv = grp & 7;
    const bool grp_mlp = grp < NMLP;
    const bool is_mlp = grp_mlp && split == msplit;
    const __amdgpu_buffer_rsrc_t rs = __builtin_amdgcn_make_buffer_rsrc(P.ws, 0, WS_END, 0x00020000);
    const int g = lane >> 4, n16 = lane & 15;
    const int oh = hkv * 4 + (pidx & 3), okh = (pidx >> 2) & 1, ons = pidx >> 3;

    DBG(unsigned long long ts[NSTAMP]; for (int z = 0; z < NSTAMP; ++z) ts[z] = 0;)
    if (wave < 4) {
        STAMP(0);
        raw_barrier();
        const int rank = (split - msplit - 1) & 7;
        const int t_start = grp_mlp ? 65 * rank : 64 * rank;
        const int t_cnt = grp_mlp ? (rank == 7 ? 57 : 65) : 64;
        const int nt = (t_cnt - wave + 3) >> 2;
        const size_t tb = (size_t)grp * (SK * DH * 2);
        constexpr u32 trange = SK * DH * 2;
        const u32 t0 = (u32)(t_start + wave) * 8192;
        const rsrc_t ks = __builtin_amdgcn_make_buffer_rsrc((char*)P.k + tb, 0, trange, 0x00020000);
        const rsrc_t vs = __builtin_amdgcn_make_buffer_rsrc((char*)P.v + tb, 0, trange, 0x00020000);
        const u32 koff = (u32)((4 * (n16 & 3) + (n16 >> 2)) * 256 + g * 16);
        const u32 voff = (u32)(lane * 16);
        char* const pf_base = lds + L_PF + wave * 32768;
        Ring A, B;
        AttnState S;
        if (is_mlp) {
            const int f0 = grp * 64;
            gcchar* w1b = (gcchar*)P.w1 + (size_t)(128 * wave) * (FF * 2) + f0 * 2;
            const u32 w1off = (u32)((32 * (n16 >> 3) + 8 * g) * (FF * 2) + (n16 & 7) * 16);
            gcchar* w2b = (gcchar*)P.w2 + (size_t)f0 * 1024 + 256 * wave;
            const u32 w2off = (u32)(8 * g * 1024 + n16 * 16);
#pragma unroll
            for (int i = 0; i < 16; ++i) {
                if (i >= 8) PACE();
                A.r[i] = ldnt(w1b + w1off + ((i >> 3) * 64 + (i & 7)) * (FF * 2));
            }
#pragma unroll
            for (int i = 0; i < 16; ++i) {
                PACE();
                B.r[i] = ldnt(w2b + w2off + ((i >> 3) * 32 + (i & 7)) * 1024);
            }
            const float b1v = bflo((u32)((const GAS unsigned short*)P.b1)[f0 + lane]);
            wg_barrier();
#pragma unroll
            for (int d = 0; d < 8; ++d) S.acc[d] = f32x4{0.f, 0.f, 0.f, 0.f};
#pragma unroll
            for (int tau = 0; tau < 2; ++tau) {
                const u32x4 xf = *(const u32x4*)(lds + L_XN + (n16 & 3) * 1024 + (128 * wave + 64 * tau + 32 * ((n16 >> 2) & 1) + 8 * g) * 2);
                pv_tile(&A.r[8 * tau], xf, S.acc);
            }
            SB();
            issue_k(A, ks, koff, t0); issue_v(A, vs, voff, t0);
            SB();
            if (n16 < 8 && (g >> 1) == (n16 >> 2)) {
                float* hp = (float*)(lds + L_HP) + ((wave * 2 + (n16 >> 2)) * 4 + (n16 & 3)) * 64 + 32 * (g & 1);
#pragma unroll
                for (int r = 0; r < 4; ++r) {
                    *(f32x4*)(hp + 8 * r) = f32x4{S.acc[0][r], S.acc[1][r], S.acc[2][r], S.acc[3][r]};
                    *(f32x4*)(hp + 8 * r + 4) = f32x4{S.acc[4][r], S.acc[5][r], S.acc[6][r], S.acc[7][r]};
                }
            }
            wg_barrier();
            {
                const float* hp = (const float*)(lds + L_HP) + wave * 64 + lane;
                float sum = 0.f;
#pragma unroll
                for (int i = 0; i < 8; ++i) sum += hp[i * 256];
                float h = bfr(bfr(sum) + b1v);
                h = h > 0.f ? h : 0.f;
                ((unsigned short*)(lds + L_H))[wave * 64 + lane] = (unsigned short)pack2(h * h, 0.f);
            }
            wg_barrier();
#pragma unroll
            for (int d = 0; d < 8; ++d) S.acc[d] = f32x4{0.f, 0.f, 0.f, 0.f};
#pragma unroll
            for (int tau = 0; tau < 2; ++tau) {
                const u32x4 hf = *(const u32x4*)(lds + L_H + (n16 >> 2) * 128 + (32 * tau + 8 * g) * 2);
                pv_tile(&B.r[8 * tau], hf, S.acc);
            }
            SB();
            issue_k(B, ks, koff, t0 + TSTR); issue_v(B, vs, voff, t0 + TSTR);
            SB();
            const u32 T = *(const u32*)(lds + L_T);
            publish_cols<32>(rs, WS_MP, S.acc, grp, 16 * wave, lane, T);
        } else {
#pragma unroll
            for (int i = 0; i < 8; ++i) A.r[i] = ldb_nt(ks, koff + (i & 3) * 64, t0 + (i >> 2) * 4096);
#pragma unroll
            for (int i = 0; i < 8; ++i) { if (i % PB == 0) PACE(); A.r[8 + i] = ldb_nt(vs, voff + (i & 3) * 1024, t0 + (i >> 2) * 4096); }
#pragma unroll
            for (int i = 0; i < 8; ++i) { if (i % PB == 0) PACE(); B.r[i] = ldb_nt(ks, koff + (i & 3) * 64, t0 + TSTR + (i >> 2) * 4096); }
#pragma unroll
            for (int i = 0; i < 8; ++i) { if (i % PB == 0) PACE(); B.r[8 + i] = ldb_nt(vs, voff + (i & 3) * 1024, t0 + TSTR + (i >> 2) * 4096); }
        }
        STAMP(11);
        {
            const u32x4 ksd = mk_srd((const char*)P.k + tb, trange), vsd = mk_srd((const char*)P.v + tb, trange);
            const u32 lb = (u32)__builtin_amdgcn_readfirstlane((u32)(uintptr_t)(__attribute__((address_space(3))) char*)pf_base);
#pragma unroll
            for (int t = 0; t < 2; ++t) {
#pragma unroll
                for (int i = 0; i < 8; ++i) { if (i % PB == 0) LPACE(); dma16(lb + t * 16384 + i * 1024, koff, ksd, t0 + (2 + t) * TSTR + kimm(i)); }
#pragma unroll
                for (int i = 0; i < 8; ++i) { if (i % PB == 0) LPACE(); dma16(lb + t * 16384 + 8192 + i * 1024, voff, vsd, t0 + (2 + t) * TSTR + i * 1024); }
            }
        }
        STAMP(1);
#pragma unroll 1
        for (u32 spin = 0; spin < SPIN_CAP; ++spin) {
            asm volatile("" ::: "memory");
            if (lds_ld(lds + L_QF) != 0u) break;
            __builtin_amdgcn_s_sleep(2);
        }
        asm volatile("" ::: "memory");
        STAMP(2);
        u32x4 qf[4];
#pragma unroll
        for (int st = 0; st < 4; ++st) qf[st] = *(const u32x4*)(lds + L_Q + (n16 & 3) * 256 + st * 64 + g * 16);
        const u32 T = *(const u32*)(lds + L_T);
#pragma unroll
        for (int d = 0; d < 8; ++d) S.acc[d] = f32x4{0.f, 0.f, 0.f, 0.f};
        S.m = -INFINITY; S.l = 0.f;
        proc_tile(A, S, qf, ks, vs, koff, voff, t0 + 4 * TSTR);
        asm volatile("s_waitcnt vmcnt(32)" ::: "memory");
        proc_lds(pf_base + lane * 16, S, qf);
        proc_tile(B, S, qf, ks, vs, koff, voff, OOB);
        DBG(STAMP(6);)
        asm volatile("s_waitcnt vmcnt(32)" ::: "memory");
        proc_lds(pf_base + 16384 + lane * 16, S, qf);
        DBG(STAMP(7);)
        int i = 4;
#pragma unroll 1
        for (; i < nt; ++i) {
            proc_tile(A, S, qf, ks, vs, koff, voff, i + 1 < nt ? t0 + (u32)(i + 1) * TSTR : OOB);
            DBG(if (i == 5) STAMP(8); if (i == 9) STAMP(9); if (i == 13) STAMP(10);)
        }
        i = nt;
#pragma unroll 1
        for (; i + 1 < nt; i += 2) {
            const u32 ta = i + 2 < nt ? t0 + (u32)(i + 2) * TSTR : OOB, tb2 = i + 3 < nt ? t0 + (u32)(i + 3) * TSTR : OOB;
            proc_tile(A, S, qf, ks, vs, koff, voff, ta);
            proc_tile(B, S, qf, ks, vs, koff, voff, tb2);
            DBG(if (i == 4) STAMP(8); if (i == 8) STAMP(9); if (i == 12) STAMP(10);)
        }
        if (i < nt) proc_tile(A, S, qf, ks, vs, koff, voff, OOB);
        STAMP(3);
        const float lw = xrow_sum(S.l);
        if (n16 < 4) {
            float* mo = (float*)pf_base + n16 * 144 + g * 36;
#pragma unroll
            for (int r = 0; r < 4; ++r) {
                *(f32x4*)(mo + 8 * r) = f32x4{S.acc[0][r], S.acc[1][r], S.acc[2][r], S.acc[3][r]};
                *(f32x4*)(mo + 8 * r + 4) = f32x4{S.acc[4][r], S.acc[5][r], S.acc[6][r], S.acc[7][r]};
            }
            if (g == 0) *(f32x2*)(lds + L_ML + (wave * 4 + n16) * 8) = f32x2{S.m, lw};
        }
        wg_barrier();
        {
            f32x2 ml[4], ov[4];
#pragma unroll
            for (int w = 0; w < 4; ++w) {
                ml[w] = *(const f32x2*)(lds + L_ML + (w * 4 + wave) * 8);
                ov[w] = *(const f32x2*)((const float*)(lds + L_PF + w * 32768) + wave * 144 + (lane >> 4) * 36 + 2 * (lane & 15));
            }
            const float M = fmaxf(fmaxf(ml[0].x, ml[1].x), fmaxf(ml[2].x, ml[3].x));
            float L = 0.f, o0 = 0.f, o1 = 0.f;
#pragma unroll
            for (int w = 0; w < 4; ++w) {
                const float a = __builtin_amdgcn_exp2f(ml[w].x - M);
                L += a * ml[w].y; o0 += a * ov[w].x; o1 += a * ov[w].y;
            }
            const float inv = 1.0f / L;
            const u32 blk = (u32)((b * 32 + ahkv * 4 + wave) * 8 + split);
            st_unit(rs, WS_PO + blk * 1024 + lane * 16, mk_unit(__float_as_uint(o0 * inv), __float_as_uint(o1 * inv), T));
            if (lane == 0) {
                const float lse = M + __builtin_amdgcn_logf(L);
                st_unit(rs, WS_PL + blk * 16, mk_unit(__float_as_uint(lse), 0u, T));
            }
        }
        STAMP(4);
        {
            const int half = lane >> 5;
            const u32 blk0 = (u32)((wave * 32 + oh) * 8);
            const u32 ooff = WS_PO + (blk0 + half) * 1024 + (u32)((32 * okh + (lane & 31)) * 16);
            const u32 loff = lane < 8 ? WS_PL + (blk0 + lane) * 16 : OOB;
            u32x4 uo[4], ul;
            u32 want = 0, have = 0;
            u32 lseen = 0;
#pragma unroll 1
            for (u32 spin = 0; spin < SPIN_CAP; ++spin) {
                asm volatile("" ::: "memory");
                u32x4 t[4];
                ul = ld_unit(rs, loff);
#pragma unroll
                for (int r = 0; r < 4; ++r) t[r] = ld_unit(rs, ((want >> r) & 1u) ? ooff + r * 2048 : OOB);
#pragma unroll
                for (int r = 0; r < 4; ++r)
                    if (((want >> r) & 1u) && unit_ok(t[r], T)) { uo[r] = t[r]; have |= 1u << r; }
                lseen = (u32)__builtin_amdgcn_ballot_w64(unit_ok(ul, T)) & 0xffu;
                const bool done = lseen == 0xffu && __builtin_amdgcn_ballot_w64(have != 0xfu) == 0ull;
                if (done) break;
                const u32 gate = (__builtin_popcount(lseen) >= 6) ? 0xffu : lseen;
                const u32 gl = gate >> half;
                want = ((gl & 1u) | ((gl >> 1) & 2u) | ((gl >> 2) & 4u) | ((gl >> 3) & 8u)) & ~have;
                if (__builtin_amdgcn_ballot_w64(want != 0u) == 0ull || lseen != 0xffu) __builtin_amdgcn_s_sleep(2);
            }
            const float lse = __uint_as_float(ul.x);
            float M = fmaxf(lse, dppf<XOR1>(lse)); M = fmaxf(M, dppf<XOR2>(M)); M = fmaxf(M, dppf<HMIR>(M));
            const float al = __builtin_amdgcn_exp2f(lse - M);
            float W = al + dppf<XOR1>(al); W += dppf<XOR2>(W); W += dppf<HMIR>(W);
            const float inv = 1.0f / W;
            const u32 aw = __float_as_uint(al * inv);
#define WSEL(r) __uint_as_float(half ? __builtin_amdgcn_readlane(aw, 2 * (r) + 1) : __builtin_amdgcn_readlane(aw, 2 * (r)))
            const float a0 = WSEL(0), a1 = WSEL(1), a2 = WSEL(2), a3 = WSEL(3);
#undef WSEL
            float o0 = (a0 * __uint_as_float(uo[0].x) + a1 * __uint_as_float(uo[1].x)) + (a2 * __uint_as_float(uo[2].x) + a3 * __uint_as_float(uo[3].x));
            float o1 = (a0 * __uint_as_float(uo[0].z) + a1 * __uint_as_float(uo[1].z)) + (a2 * __uint_as_float(uo[2].z) + a3 * __uint_as_float(uo[3].z));
            o0 = x32_sum(o0); o1 = x32_sum(o1);
            if (lane < 32) *(u32*)(lds + L_O + wave * 128 + lane * 4) = pack2(o0, o1);
        }
        STAMP(5);
        wg_barrier();
        FLUSH_STAMPS();
    } else {
        STAMP(0);
        __builtin_amdgcn_s_setprio(SVCPRIO);
        u32x4 xr[4], wr[16];
        const u32 epoch = *(const GAS u32*)((gcchar*)P.ws + WS_EPOCH);
#pragma unroll
        for (int i = 0; i < 4; ++i) xr[i] = *(gcu32x4*)((gcchar*)P.x + g * 1024 + i * 256 + n16 * 16);
        {
            gcchar* wqb = (gcchar*)P.wq + (hkv * 512 + 16 * pidx) * 2;
            const u32 wqoff = (u32)((32 * (n16 >> 1) + 8 * g) * 8192 + (n16 & 1) * 16);
#pragma unroll
            for (int i = 0; i < 16; ++i) wr[i] = ldnt(wqb + wqoff + ((i >> 3) * 256 + (i & 7)) * 8192);
        }
        SB();
        if (lane == 0) lds_st(lds + L_QF, 0u);
        wg_barrier();
        SB();
        DBG(asm volatile("s_waitcnt vmcnt(16)" ::: "memory"); STAMP(8);)
        u32 T = epoch + 1u; if (T == 0u) T = 1u;
        T = __builtin_amdgcn_readfirstlane(T);
        {
            float s = 0.f;
#pragma unroll
            for (int i = 0; i < 4; ++i)
#pragma unroll
                for (int w = 0; w < 4; ++w) s = dot2(xr[i][w], 0x3f803f80u, s);
            s += dppf<XOR1>(s); s += dppf<XOR2>(s); s += dppf<ROR4>(s); s += dppf<ROR8>(s);
            const float mean = s * (1.0f / 512.0f);
            f32x2 q2 = {0.f, 0.f};
#pragma unroll
            for (int i = 0; i < 4; ++i)
#pragma unroll
                for (int w = 0; w < 4; ++w) {
                    const f32x2 d = f32x2{bflo(xr[i][w]), bfhi(xr[i][w])} - mean;
                    q2 += d * d;
                }
            float q = q2.x + q2.y;
            q += dppf<XOR1>(q); q += dppf<XOR2>(q); q += dppf<ROR4>(q); q += dppf<ROR8>(q);
            const float var = bfr(q * (1.0f / 512.0f));
            const float mb = bfr(mean);
            const float rden = 1.0f / bfr(__builtin_sqrtf(bfr(var + 1e-5f)));
#pragma unroll
            for (int i = 0; i < 4; ++i) {
                u32x4 o;
#pragma unroll
                for (int w = 0; w < 4; ++w) {
                    const f32x2 d = f32x2{bflo(xr[i][w]), bfhi(xr[i][w])} - mb;
                    const u32 c = pack2(d.x, d.y);
                    const f32x2 e = f32x2{bflo(c), bfhi(c)} * rden;
                    o[w] = pack2(e.x, e.y);
                }
                *(u32x4*)(lds + L_XN + g * 1024 + i * 256 + n16 * 16) = o;
            }
        }
        if (lane == 0) *(u32*)(lds + L_T) = T;
        STAMP(1);
        DBG(asm volatile("s_waitcnt vmcnt(0)" ::: "memory"); STAMP(9);)
        {
            f32x4 acc[2][8];
#pragma unroll
            for (int v = 0; v < 2; ++v)
#pragma unroll
                for (int d = 0; d < 8; ++d) acc[v][d] = f32x4{0.f, 0.f, 0.f, 0.f};
            asm volatile("s_waitcnt lgkmcnt(0)" ::: "memory");
#pragma unroll
            for (int tau = 0; tau < 2; ++tau) {
                u32x4 xf[2];
#pragma unroll
                for (int v = 0; v < 2; ++v)
                    xf[v] = *(const u32x4*)(lds + L_XN + (2 * v + (n16 >> 3)) * 1024 + (256 * tau + 32 * (n16 & 7) + 8 * g) * 2);
#define QSTEP(d) { const u32x4 a = tfrag<d>(&wr[8 * tau]); acc[0][d] = mfma(a, xf[0], acc[0][d]); acc[1][d] = mfma(a, xf[1], acc[1][d]); }
                QSTEP(0) QSTEP(1) QSTEP(2) QSTEP(3) QSTEP(4) QSTEP(5) QSTEP(6) QSTEP(7)
#undef QSTEP
            }
            if (g == ((n16 & 7) >> 1)) {
                const bool hi = n16 & 1;
                float* qs = (float*)(lds + L_QS) + ((n16 & 7) * 4 + (n16 >> 3)) * 16;
#pragma unroll
                for (int v = 0; v < 2; ++v)
#pragma unroll
                    for (int c = 0; c < 2; ++c) {
                        float t[8];
#pragma unroll
                        for (int d = 0; d < 8; ++d) t[d] = hi ? acc[v][d][2 + c] : acc[v][d][c];
                        *(f32x4*)(qs + v * 32 + c * 8) = f32x4{t[0], t[1], t[2], t[3]};
                        *(f32x4*)(qs + v * 32 + c * 8 + 4) = f32x4{t[4], t[5], t[6], t[7]};
                    }
            }
            asm volatile("s_waitcnt lgkmcnt(0)" ::: "memory");
            const float* qs = (const float*)(lds + L_QS) + lane;
            float y = ((qs[0] + qs[64]) + (qs[128] + qs[192])) + ((qs[256] + qs[320]) + (qs[384] + qs[448]));
            const u32 w01 = pack2(y, dppf<XOR1>(y));
            const u32 w23 = dppu<XOR2>(w01);
            if ((n16 & 3) == 0)
                st_unit(rs, WS_Q + (u32)((g * 8 + hkv) * 2048 + (4 * pidx + (n16 >> 2)) * 16), mk_unit(w01, w23, T));
        }
        STAMP(2);
        if (is_mlp) { wg_barrier(); wg_barrier(); wg_barrier(); }
        {
            u32x4 u0, u1;
            const u32 off = WS_Q + (u32)(grp * 2048 + lane * 16);
            if (QPRE > 0) __builtin_amdgcn_s_sleep(QPRE);
#pragma unroll 1
            for (u32 spin = 0; spin < SPIN_CAP; ++spin) {
                asm volatile("" ::: "memory");
                u0 = ld_unit(rs, off); u1 = ld_unit(rs, off + 1024);
                DBG(if (spin == 0) { asm volatile("s_waitcnt vmcnt(0)" ::: "memory"); STAMP(10); })
                if (__builtin_amdgcn_ballot_w64(!(unit_ok(u0, T) & unit_ok(u1, T))) == 0ull) { STAMPV(11, 1000000ull + spin); break; }
                __builtin_amdgcn_s_sleep(QINT);
            }
            *(u32x2*)(lds + L_Q + lane * 8) = u32x2{u0.x, u0.z};
            *(u32x2*)(lds + L_Q + 512 + lane * 8) = u32x2{u1.x, u1.z};
        }
        STAMP(3);
        asm volatile("" ::: "memory");
        if (lane == 0) lds_st(lds + L_QF, 1u);
        asm volatile("" ::: "memory");
        u32x4 wa[2][8];
        {
            u32x4 wo[16];
            gcchar* wob = (gcchar*)P.wo + (size_t)(oh * 128 + 64 * okh) * 1024 + 256 * ons;
            const u32 wooff = (u32)(8 * g * 1024 + n16 * 16);
#pragma unroll
            for (int i = 0; i < 16; ++i) wo[i] = ldnt(wob + wooff + ((i >> 3) * 32 + (i & 7)) * 1024);
#pragma unroll
            for (int tau = 0; tau < 2; ++tau) {
                wa[tau][0] = tfrag<0>(&wo[8 * tau]); wa[tau][1] = tfrag<1>(&wo[8 * tau]); wa[tau][2] = tfrag<2>(&wo[8 * tau]); wa[tau][3] = tfrag<3>(&wo[8 * tau]);
                wa[tau][4] = tfrag<4>(&wo[8 * tau]); wa[tau][5] = tfrag<5>(&wo[8 * tau]); wa[tau][6] = tfrag<6>(&wo[8 * tau]); wa[tau][7] = tfrag<7>(&wo[8 * tau]);
            }
        }
        const int fb = j >> 6, fng = j & 63;
        const int fu = lane & 3, fi = lane >> 2;
        const u32 xw = *(const GAS u32*)((gcchar*)P.x + fb * 1024 + fng * 16 + fu * 4);
        const u32 b2w = *(const GAS u32*)((gcchar*)P.b2 + fng * 16 + fu * 4);
        float g0, g1;
        {
            const u32 off = WS_MP + (u32)(((fb * 64 + fng) * 32 + fi) * 64 + fu * 16);
            const bool has1 = fi + 16 < NMLP;
            u32x4 u0 = {0, 0, 0, 0}, u1 = {0, 0, 0, 0};
            constexpr u32 GCAP = SPIN_CAP;
#pragma unroll 1
            for (u32 spin = 0; spin < GCAP; ++spin) {
                __builtin_amdgcn_s_sleep(127); __builtin_amdgcn_s_sleep(127);
                asm volatile("" ::: "memory");
                u0 = ld_unit(rs, off); u1 = ld_unit(rs, off + 1024);
                if (__builtin_amdgcn_ballot_w64(!(unit_ok(u0, T) & (unit_ok(u1, T) | !has1))) == 0ull) break;
            }
            float a0 = __uint_as_float(u0.x) + (has1 ? __uint_as_float(u1.x) : 0.f);
            float a1 = __uint_as_float(u0.z) + (has1 ? __uint_as_float(u1.z) : 0.f);
            a0 += dppf<ROR4>(a0); a0 += dppf<ROR8>(a0); a0 = xrow_sum(a0);
            a1 += dppf<ROR4>(a1); a1 += dppf<ROR8>(a1); a1 = xrow_sum(a1);
            const float y0 = bfr(bfr(a0) + bflo(b2w)), y1 = bfr(bfr(a1) + bfhi(b2w));
            g0 = bfr(y0 / (1.0f + __expf(-y0)));
            g1 = bfr(y1 / (1.0f + __expf(-y1)));
        }
        STAMP(4);
        wg_barrier();
        wg_barrier();
        STAMP(5);
        {
            f32x4 acc[8];
#pragma unroll
            for (int d = 0; d < 8; ++d) acc[d] = f32x4{0.f, 0.f, 0.f, 0.f};
#pragma unroll
            for (int tau = 0; tau < 2; ++tau) {
                const u32x4 of = *(const u32x4*)(lds + L_O + (n16 >> 2) * 128 + (32 * tau + 8 * g) * 2);
#pragma unroll
                for (int d = 0; d < 8; ++d) acc[d] = mfma(wa[tau][d], of, acc[d]);
            }
            publish_cols<64>(rs, WS_SP, acc, oh * 2 + okh, 16 * ons, lane, T);
        }
        STAMP(6);
        {
            const u32 off = WS_SP + (u32)(((fb * 64 + fng) * 64 + fi) * 64 + fu * 16);
            u32x4 u[4];
            u32 have = 0;
#pragma unroll 1
            for (u32 spin = 0; spin < SPIN_CAP; ++spin) {
                asm volatile("" ::: "memory");
                u32x4 t[4];
#pragma unroll
                for (int r = 0; r < 4; ++r) t[r] = ld_unit(rs, ((have >> r) & 1u) ? OOB : off + r * 1024);
#pragma unroll
                for (int r = 0; r < 4; ++r)
                    if (!((have >> r) & 1u) && unit_ok(t[r], T)) { u[r] = t[r]; have |= 1u << r; }
                if (__builtin_amdgcn_ballot_w64(have != 0xfu) == 0ull) break;
                __builtin_amdgcn_s_sleep(1);
            }
            float a0 = (__uint_as_float(u[0].x) + __uint_as_float(u[1].x)) + (__uint_as_float(u[2].x) + __uint_as_float(u[3].x));
            float a1 = (__uint_as_float(u[0].z) + __uint_as_float(u[1].z)) + (__uint_as_float(u[2].z) + __uint_as_float(u[3].z));
            a0 += dppf<ROR4>(a0); a0 += dppf<ROR8>(a0); a0 = xrow_sum(a0);
            a1 += dppf<ROR4>(a1); a1 += dppf<ROR8>(a1); a1 = xrow_sum(a1);
            const float r0 = bflo(xw) + bfr(bfr(a0) * g0), r1 = bfhi(xw) + bfr(bfr(a1) * g1);
            if (lane < 4) *(GAS u32*)((GAS char*)P.out + fb * 1024 + fng * 16 + fu * 4) = pack2(r0, r1);
            if (j == 0 && lane == 0) *(GAS u32*)((GAS char*)P.ws + WS_EPOCH) = T;
        }
        STAMP(7);
        FLUSH_STAMPS();
    }
}

extern "C" void kernel_launch(void* const* d_in, const int* in_sizes, int n_in,
                              void* d_out, int out_size, void* d_ws, size_t ws_size,
                              hipStream_t stream) {
    Params P;
    P.x = d_in[0]; P.k = d_in[1]; P.v = d_in[2]; P.wq = d_in[3]; P.wo = d_in[4];
    P.w1 = d_in[6]; P.b1 = d_in[7]; P.w2 = d_in[8]; P.b2 = d_in[9];
    P.out = d_out; P.ws = d_ws;
    fused_decode_kernel<<<256, 320, 0, stream>>>(P);
}
```

```cpp
#include <hip/hip_runtime.h>
#include <hip/hip_bf16.h>
#include <stdint.h>

typedef unsigned int u32;
typedef u32 u32x4 __attribute__((ext_vector_type(4)));
typedef u32 u32x2 __attribute__((ext_vector_type(2)));
typedef float f32x4 __attribute__((ext_vector_type(4)));
typedef float f32x2 __attribute__((ext_vector_type(2)));
typedef __bf16 bf16x8 __attribute__((ext_vector_type(8)));
typedef __bf16 bf16x2 __attribute__((ext_vector_type(2)));
#define GAS __attribute__((address_space(1)))
typedef GAS const u32x4 gcu32x4;
typedef GAS const char gcchar;
#define LAS __attribute__((address_space(3)))
__device__ __forceinline__ unsigned int lds_ld(const char* p) { return *(volatile LAS const unsigned int*)(LAS const char*)p; }
__device__ __forceinline__ u32x4 lds_ld4(const char* p) { return *(volatile LAS const u32x4*)(LAS const char*)p; }
__device__ __forceinline__ void lds_st(char* p, unsigned int v) { *(volatile LAS unsigned int*)(LAS char*)p = v; }

constexpr int NB = 4, DM = 512, HKV = 8, SK = 16384, DH = 128, FF = 1792;
constexpr int NMLP = 28;
constexpr u32 WS_EPOCH = 0;
constexpr u32 WS_Q  = 0x1000;
constexpr u32 WS_CNT = 0x18000;
constexpr u32 WS_PL = 0x20000;
constexpr u32 WS_MP = 0x30000;
constexpr u32 WS_PO = 0x100000;
constexpr u32 WS_SP = 0x200000;
constexpr u32 WS_END = 0x400000;
constexpr u32 SPIN_CAP = 1u << 17;
constexpr int L_Q   = 0;
constexpr int L_T   = 1024;
constexpr int L_QF  = 1040;
constexpr int L_XF  = 1044;
constexpr int L_BC  = 1048;
constexpr int L_XN  = 2048;
constexpr int L_HP  = 6144;
constexpr int L_H   = 14336;
constexpr int L_ML  = 14848;
constexpr int L_O   = 15104;
constexpr int L_PF  = 18432;
constexpr int LDS_BYTES = L_PF + 4 * 32768;

struct Params {
    const void* x; const void* k; const void* v; const void* wq; const void* wo;
    const void* w1; const void* b1; const void* w2; const void* b2;
    void* out; void* ws;
};

__device__ __forceinline__ u32x4 ldnt(gcchar* p) { return __builtin_nontemporal_load((gcu32x4*)p); }
__device__ __forceinline__ float bflo(u32 w) { return __uint_as_float(w << 16); }
__device__ __forceinline__ float bfhi(u32 w) { return __uint_as_float(w & 0xffff0000u); }
__device__ __forceinline__ u32 pack2(float lo, float hi) {
    f32x2 v = {lo, hi};
    return __builtin_bit_cast(u32, __builtin_convertvector(v, bf16x2));
}
__device__ __forceinline__ float bfr(float x) { return bflo(pack2(x, 0.f)); }
__device__ __forceinline__ f32x4 mfma(u32x4 a, u32x4 b, f32x4 c) {
    return __builtin_amdgcn_mfma_f32_16x16x32_bf16(__builtin_bit_cast(bf16x8, a), __builtin_bit_cast(bf16x8, b), c, 0, 0, 0);
}
__device__ __forceinline__ float dot2(u32 a, u32 b, float c) {
    return __builtin_amdgcn_fdot2_f32_bf16(__builtin_bit_cast(bf16x2, a), __builtin_bit_cast(bf16x2, b), c, false);
}
template <int CTRL> __device__ __forceinline__ u32 dppu(u32 x) { return (u32)__builtin_amdgcn_mov_dpp((int)x, CTRL, 0xf, 0xf, true); }
template <int CTRL> __device__ __forceinline__ float dppf(float x) { return __uint_as_float(dppu<CTRL>(__float_as_uint(x))); }
constexpr int XOR1 = 0xB1, XOR2 = 0x4E, HMIR = 0x141, ROR4 = 0x124, ROR8 = 0x128, EVEN = 0xA0, ODD = 0xF5;
__device__ __forceinline__ float xrow_max(float x) {
    auto s = __builtin_amdgcn_permlane16_swap(__float_as_uint(x), __float_as_uint(x), false, false);
    x = fmaxf(__uint_as_float(s[0]), __uint_as_float(s[1]));
    auto t = __builtin_amdgcn_permlane32_swap(__float_as_uint(x), __float_as_uint(x), false, false);
    return fmaxf(__uint_as_float(t[0]), __uint_as_float(t[1]));
}
__device__ __forceinline__ float xrow_sum(float x) {
    auto s = __builtin_amdgcn_permlane16_swap(__float_as_uint(x), __float_as_uint(x), false, false);
    x = __uint_as_float(s[0]) + __uint_as_float(s[1]);
    auto t = __builtin_amdgcn_permlane32_swap(__float_as_uint(x), __float_as_uint(x), false, false);
    return __uint_as_float(t[0]) + __uint_as_float(t[1]);
}
__device__ __forceinline__ float x32_max(float x) {
    auto t = __builtin_amdgcn_permlane32_swap(__float_as_uint(x), __float_as_uint(x), false, false);
    return fmaxf(__uint_as_float(t[0]), __uint_as_float(t[1]));
}
__device__ __forceinline__ float x32_sum(float x) {
    auto t = __builtin_amdgcn_permlane32_swap(__float_as_uint(x), __float_as_uint(x), false, false);
    return __uint_as_float(t[0]) + __uint_as_float(t[1]);
}
__device__ __forceinline__ float wave_sum(float x) {
    x += dppf<XOR1>(x); x += dppf<XOR2>(x); x += dppf<ROR4>(x); x += dppf<ROR8>(x);
    return xrow_sum(x);
}
__device__ __forceinline__ void wg_barrier() { asm volatile("s_waitcnt lgkmcnt(0)\n\ts_barrier" ::: "memory"); }
__device__ __forceinline__ void raw_barrier() { asm volatile("s_barrier" ::: "memory"); }
__device__ __forceinline__ void quad_barrier(char* cnt, u32 target, int lane) {
    asm volatile("" ::: "memory");
    if (lane == 0) __hip_atomic_fetch_add((LAS unsigned int*)(LAS char*)cnt, 1u, __ATOMIC_RELAXED, __HIP_MEMORY_SCOPE_WORKGROUP);
#pragma unroll 1
    for (u32 spin = 0; spin < SPIN_CAP; ++spin) {
        asm volatile("" ::: "memory");
        if (lds_ld(cnt) >= target) break;
    }
    asm volatile("" ::: "memory");
}
__device__ __forceinline__ void wait_flag(const char* f) {
#pragma unroll 1
    for (u32 spin = 0; spin < SPIN_CAP; ++spin) {
        asm volatile("" ::: "memory");
        if (lds_ld(f) != 0u) break;
        __builtin_amdgcn_s_sleep(1);
    }
    asm volatile("" ::: "memory");
}
constexpr u32 OOB = 0x40000000u;

__device__ __forceinline__ u32x4 mk_unit(u32 v0, u32 v1, u32 T) { u32x4 u = {v0, T ^ v1, v1, T ^ v0}; return u; }
__device__ __forceinline__ bool unit_ok(u32x4 u, u32 T) { return ((u.y ^ u.z) == T) & ((u.w ^ u.x) == T); }
__device__ __forceinline__ void st_unit(__amdgpu_buffer_rsrc_t rs, u32 off, u32x4 u) { __builtin_amdgcn_raw_buffer_store_b128(u, rs, off, 0, 16); }
__device__ __forceinline__ u32x4 ld_unit(__amdgpu_buffer_rsrc_t rs, u32 off) { return __builtin_amdgcn_raw_buffer_load_b128(rs, off, 0, 16); }

template <int DD> __device__ __forceinline__ u32x4 tfrag(const u32x4* v) {
    constexpr u32 sel = (DD & 1) ? 0x07060302u : 0x05040100u;
    u32x4 a;
    a.x = __builtin_amdgcn_perm(v[1][DD >> 1], v[0][DD >> 1], sel);
    a.y = __builtin_amdgcn_perm(v[3][DD >> 1], v[2][DD >> 1], sel);
    a.z = __builtin_amdgcn_perm(v[5][DD >> 1], v[4][DD >> 1], sel);
    a.w = __builtin_amdgcn_perm(v[7][DD >> 1], v[6][DD >> 1], sel);
    return a;
}
__device__ __forceinline__ void pv_tile(const u32x4* v, u32x4 p, f32x4 (&acc)[8]) {
    acc[0] = mfma(tfrag<0>(v), p, acc[0]); acc[1] = mfma(tfrag<1>(v), p, acc[1]);
    acc[2] = mfma(tfrag<2>(v), p, acc[2]); acc[3] = mfma(tfrag<3>(v), p, acc[3]);
    acc[4] = mfma(tfrag<4>(v), p, acc[4]); acc[5] = mfma(tfrag<5>(v), p, acc[5]);
    acc[6] = mfma(tfrag<6>(v), p, acc[6]); acc[7] = mfma(tfrag<7>(v), p, acc[7]);
}
#define MPAUX 16
template <int NSLOT, int AUX = 16>
__device__ __forceinline__ void publish_cols(__amdgpu_buffer_rsrc_t rs, u32 base, const f32x4 (&acc)[8], int slot, int ng0, int lane, u32 T) {
    const int g = lane >> 4, b = (lane >> 2) & 3, rho = lane & 3;
    const u32 off0 = base + (u32)(((b * 64 + ng0 + 4 * g) * NSLOT + slot) * 64 + rho * 16);
#pragma unroll
    for (int r = 0; r < 4; ++r) {
        float a0 = (rho & 2) ? acc[4][r] : acc[0][r], a1 = (rho & 2) ? acc[6][r] : acc[2][r];
        float c0 = (rho & 2) ? acc[5][r] : acc[1][r], c1 = (rho & 2) ? acc[7][r] : acc[3][r];
        float v0 = (rho & 1) ? a1 : a0, v1 = (rho & 1) ? c1 : c0;
        __builtin_amdgcn_raw_buffer_store_b128(mk_unit(__float_as_uint(v0), __float_as_uint(v1), T), rs, off0 + (u32)(r * NSLOT * 64), 0, AUX);
    }
}

#define STAMP(i) do { } while (0)
#define STAMPV(i, v) do { } while (0)
#define FLUSH_STAMPS() do { } while (0)
#define DBG(x)

struct Ring { u32x4 r[16]; };
typedef __amdgpu_buffer_rsrc_t rsrc_t;
#define DMAMOD "nt"
constexpr int NT = 2;
__device__ __forceinline__ u32x4 ldb_nt(rsrc_t rs, u32 voff, u32 soff) { return __builtin_amdgcn_raw_buffer_load_b128(rs, voff, soff, NT); }
constexpr u32 TSTR = 4 * 8192;
__device__ __forceinline__ constexpr u32 kimm(int i) { return (u32)((i >> 2) * 4096 + (i & 3) * 64); }
__device__ __forceinline__ void issue_k(Ring& R, rsrc_t ks, u32 koff, u32 toff, u32 toff2) {
#pragma unroll
    for (int i = 0; i < 8; ++i) R.r[i] = ldb_nt(ks, koff + (i & 3) * 64, i < 4 ? toff : toff2);
}
__device__ __forceinline__ void issue_v(Ring& R, rsrc_t vs, u32 voff, u32 toff, u32 toff2) {
#pragma unroll
    for (int i = 0; i < 8; ++i) R.r[8 + i] = ldb_nt(vs, voff + (i & 3) * 1024, i < 4 ? toff : toff2);
}
__device__ __forceinline__ void issue_k(Ring& R, rsrc_t ks, u32 koff, u32 toff) { issue_k(R, ks, koff, toff, toff + 4096); }
__device__ __forceinline__ void issue_v(Ring& R, rsrc_t vs, u32 voff, u32 toff) { issue_v(R, vs, voff, toff, toff + 4096); }
__device__ __forceinline__ void dma16(u32 ldst, u32 voff, u32x4 srd, u32 soff) {
    u32 keep;
    asm volatile("s_mov_b32 %0, m0\n\ts_mov_b32 m0, %1\n\ts_nop 0\n\tbuffer_load_dwordx4 %2, %3, %4 offen " DMAMOD " lds\n\ts_mov_b32 m0, %0"
                 : "=&s"(keep) : "s"(ldst), "v"(voff), "s"(srd), "s"(soff) : "memory");
}
__device__ __forceinline__ u32x4 mk_srd(const void* base, u32 bytes) {
    const uint64_t a = (uint64_t)base;
    u32x4 d = {(u32)__builtin_amdgcn_readfirstlane((u32)a), (u32)__builtin_amdgcn_readfirstlane((u32)(a >> 32) & 0xffffu), bytes, 0x00020000u};
    return d;
}
#define SB() __builtin_amdgcn_sched_barrier(0)
#define QPRE 48
#define QINT 16
#define PACE_N 7
#define STR2(x) #x
#define STR(x) STR2(x)
#define PACE() asm volatile("s_waitcnt vmcnt(" STR(PACE_N) ")" ::: "memory")
#define LPACE_N 7
#define LPACE() asm volatile("s_waitcnt vmcnt(" STR(LPACE_N) ")" ::: "memory")
#define MPACE_N 15
#define PACEC(flag) asm volatile("s_cmp_lg_u32 %0, 0\n\ts_cbranch_scc1 1f\n\ts_waitcnt vmcnt(" STR(PACE_N) ")\n1:\n\ts_waitcnt vmcnt(" STR(MPACE_N) ")" :: "s"(flag) : "scc", "memory")
#define PB 1
#define SVCPRIO 3

#define GAUX 16
#define DQ 32
#define NP 528
#define QR0 2
#define QR1 5
#define NL 1
#define QPRE_P 100
#define QPRE_X 60
#define XAUX 0
#define QVP 63
#define TOPN 4
constexpr int NP2 = ((4096 + 2 * DQ) / 8 + 3) / 4 * 4;
static_assert(NP % 4 == 0 && DQ % 4 == 0 && 4096 - 7 * NP + 2 * DQ > 160, "key split");
struct AttnState { f32x4 acc[8]; float m, l; };

__device__ __forceinline__ void qk_tile(const u32x4* k, const u32x4 (&qf)[4], f32x4& s0, f32x4& s1) {
    s0 = f32x4{0.f, 0.f, 0.f, 0.f}; s1 = f32x4{0.f, 0.f, 0.f, 0.f};
#pragma unroll
    for (int st = 0; st < 4; ++st) { s0 = mfma(k[st], qf[st], s0); s1 = mfma(k[4 + st], qf[st], s1); }
}
template <bool MASKED = false>
__device__ __forceinline__ u32x4 softmax_step(const f32x4 s0, const f32x4 s1, AttnState& S, int nj = 8) {
    constexpr float SCL = 0.08838834764831845f, LOG2E = 1.4426950408889634f;
    float z[8];
#pragma unroll
    for (int j = 0; j < 2; ++j) {
        const u32 a = pack2(s0[2 * j], s0[2 * j + 1]), c = pack2(s1[2 * j], s1[2 * j + 1]);
        const u32 a2 = pack2(bflo(a) * SCL, bfhi(a) * SCL), c2 = pack2(bflo(c) * SCL, bfhi(c) * SCL);
        z[2 * j] = bflo(a2); z[2 * j + 1] = bfhi(a2); z[4 + 2 * j] = bflo(c2); z[4 + 2 * j + 1] = bfhi(c2);
    }
    if (MASKED) {
#pragma unroll
        for (int j = 0; j < 8; ++j) z[j] = (j < nj) ? z[j] : -INFINITY;
    }
    float mx = fmaxf(fmaxf(fmaxf(z[0], z[1]), fmaxf(z[2], z[3])), fmaxf(fmaxf(z[4], z[5]), fmaxf(z[6], z[7])));
    mx = xrow_max(mx) * LOG2E;
    if (__builtin_amdgcn_ballot_w64(mx > S.m) != 0ull) {
        const float mn = fmaxf(S.m, mx);
        const float al = __builtin_amdgcn_exp2f(S.m - mn);
        S.m = mn; S.l *= al;
#pragma unroll
        for (int d = 0; d < 8; ++d) S.acc[d] *= al;
    }
    float p[8];
#pragma unroll
    for (int j = 0; j < 8; ++j) p[j] = __builtin_amdgcn_exp2f(__builtin_fmaf(z[j], LOG2E, -S.m));
    S.l += ((p[0] + p[1]) + (p[2] + p[3])) + ((p[4] + p[5]) + (p[6] + p[7]));
    u32x4 pf = {pack2(p[0], p[1]), pack2(p[2], p[3]), pack2(p[4], p[5]), pack2(p[6], p[7])};
    return pf;
}
__device__ __forceinline__ void proc_tile(Ring& R, AttnState& S, const u32x4 (&qf)[4], rsrc_t ks, rsrc_t vs, u32 koff, u32 voff, u32 toff) {
    f32x4 s0, s1;
    qk_tile(&R.r[0], qf, s0, s1);
    SB();
    issue_k(R, ks, koff, toff);
    SB();
    const u32x4 pf = softmax_step(s0, s1, S);
    pv_tile(&R.r[8], pf, S.acc);
    SB();
    issue_v(R, vs, voff, toff);
    SB();
}
__device__ __forceinline__ void proc_last(Ring& R, AttnState& S, const u32x4 (&qf)[4]) {
    f32x4 s0, s1;
    qk_tile(&R.r[0], qf, s0, s1);
    const u32x4 pf = softmax_step(s0, s1, S);
    pv_tile(&R.r[8], pf, S.acc);
}
__device__ __forceinline__ void proc_part(Ring& R, AttnState& S, const u32x4 (&qf)[4], int nj) {
    f32x4 s0, s1;
    qk_tile(&R.r[0], qf, s0, s1);
    const u32x4 pf = softmax_step<true>(s0, s1, S, nj);
    pv_tile(&R.r[8], pf, S.acc);
}
__device__ __forceinline__ void proc_lds(const char* sl, AttnState& S, const u32x4 (&qf)[4]) {
    u32x4 c[8];
#pragma unroll
    for (int i = 0; i < 8; ++i) c[i] = *(const u32x4*)(sl + i * 1024);
    f32x4 s0, s1;
    qk_tile(c, qf, s0, s1);
    SB();
#pragma unroll
    for (int i = 0; i < 8; ++i) c[i] = *(const u32x4*)(sl + 8192 + i * 1024);
    const u32x4 pf = softmax_step(s0, s1, S);
    pv_tile(c, pf, S.acc);
}

__global__ void __launch_bounds__(320, 2) fused_decode_kernel(Params P) {
    __shared__ __attribute__((aligned(16))) char lds[LDS_BYTES];
    const int tid = threadIdx.x, lane = tid & 63;
    const int wave = __builtin_amdgcn_readfirstlane(tid >> 6);
    const int j = blockIdx.x;
    DBG(unsigned long long ts[NSTAMP]; for (int z = 0; z < NSTAMP; ++z) ts[z] = 0;)
    STAMP(0);
    const int grp = j >> 3, split = j & 7;
    const int b = grp >> 3, ahkv = grp & 7;
    const int rank = (split - ahkv - 1) & 7;
    const bool grp_mlp = grp < NMLP;
    int rk = rank;
    asm volatile("" : "+s"(rk));
    int f_q = ((1 << QR0 | 1 << QR1) >> rk) & 1;
    int f_m = (((grp - NMLP) >> 24 & 0x80) >> rk) & 1;
    int f_q1 = (rk >> 2) & f_q;
    static_assert(QR0 < 4 && QR1 >= 4, "rank bit 2 tells the two q slices apart");
    asm volatile("" : "+s"(f_q), "+s"(f_m), "+s"(f_q1));
    int f_pre = f_q | f_m, f_svc = wave >> 2;
    asm volatile("" : "+s"(f_pre), "+s"(f_svc));
    const bool is_q = f_q != 0, is_mlp = f_m != 0, pre = f_pre != 0;
    const int g = lane >> 4, n16 = lane & 15;
    const int n_plain = grp_mlp ? NP : NP2;
    const int nkeys = rank == 7 ? 4096 + 2 * DQ - 7 * n_plain : n_plain - f_q * DQ;
    const int nfull = (nkeys - 1) >> 5, nj = (nkeys - 32 * nfull) >> 2;
    const u32 w_start = (u32)(rank * n_plain - (rank > QR0 ? DQ : 0) - (rank > QR1 ? DQ : 0)) * 1024u;
    const size_t tb = (size_t)grp * (SK * DH * 2);
    constexpr u32 trange = SK * DH * 2;
    const u32 t0 = w_start + (u32)wave * 8192;
    const u32 koff = (u32)((4 * (n16 & 3) + (n16 >> 2)) * 256 + g * 16);
    const u32 voff = (u32)(lane * 16);
    Ring A;
    u32 epoch = 0;
    {
        const u32 mq = 0u - (u32)f_q, mp = 0u - (u32)f_pre, ms = 0u - (u32)f_svc;
        auto sel = [](u32 m, u32 x, u32 y) { return y ^ ((x ^ y) & m); };
        const bool svc = f_svc != 0;
        const u32 wstr = sel(mq, 8192u, (u32)(FF * 2));
        const u32 wcol = sel(mq, (u32)(ahkv * 1024 + (2 * b + f_q1) * 128), (u32)(grp * 128));
        const u32 wb = (u32)(128 * wave) * wstr + wcol;
        const u32 nb = sel(mp, 512u * wstr, trange), na = sel(ms, 4096u, nb);
        const u32 wv = (u32)(32 * (n16 >> 3) + 8 * g) * wstr + (u32)((n16 & 7) * 16);
        const u32 va = sel(ms, (u32)(g * 1024 + n16 * 16), sel(mp, wv, koff));
        const u32 vb = sel(mp, wv, voff);
        const u32 sa = sel(ms, sel(mp, 0u, OOB), sel(mp, wb, t0)), sb = sel(mp, wb + 64u * wstr, t0);
        const u32 a1 = sel(ms, 256u, sel(mp, wstr, 64u)), a2 = sel(mp, 4u * wstr, 4096u);
        const u32 b1 = sel(mp, wstr, 1024u);
        SB();
        const uint64_t Mq = (uint64_t)(int64_t)(int)mq, Mp = (uint64_t)(int64_t)(int)mp, Ms = (uint64_t)(int64_t)(int)ms;
        auto sel64 = [](uint64_t m, uint64_t x, uint64_t y) { return y ^ ((x ^ y) & m); };
        const uint64_t wp = sel64(Mq, (uint64_t)P.wq, (uint64_t)P.w1);
        const uint64_t pa = sel64(Ms, (uint64_t)P.x, sel64(Mp, wp, (uint64_t)P.k + tb));
        const uint64_t pb = sel64(Mp, wp, (uint64_t)P.v + tb);
        const rsrc_t ra = __builtin_amdgcn_make_buffer_rsrc((void*)pa, 0, na, 0x00020000);
        const rsrc_t rb = __builtin_amdgcn_make_buffer_rsrc((void*)pb, 0, nb, 0x00020000);
        if (lane < 3) lds_st(lds + L_QF + lane * 4, 0u);
#pragma unroll
        for (int i = 0; i < 4; ++i) A.r[i] = __builtin_amdgcn_raw_buffer_load_b128(ra, va, sa + (u32)i * a1, XAUX);
        epoch = __builtin_amdgcn_raw_buffer_load_b32(__builtin_amdgcn_make_buffer_rsrc(P.ws, 0, 16, 0x00020000), 0, ~ms & OOB, 0);
        SB(); wg_barrier(); SB();
        if (!svc) {
#pragma unroll
            for (int i = 4; i < 16; ++i)
                A.r[i] = i < 8 ? ldb_nt(ra, va, sa + (u32)(i & 3) * a1 + a2) : ldb_nt(rb, vb, sb + (u32)(i & 3) * b1 + (u32)((i - 8) >> 2) * a2);
        }
        SB();
    }
    STAMP(15);
    int wave2 = wave;
    asm volatile("" : "+s"(wave2));
    const int hkv = j & 7, pidx = j >> 3;
    const __amdgpu_buffer_rsrc_t rs = __builtin_amdgcn_make_buffer_rsrc(P.ws, 0, WS_END, 0x00020000);
    const int oh = hkv * 4 + (pidx & 3), okh = (pidx >> 2) & 1, ons = pidx >> 3;

    if (wave2 < 4) {
        auto tof = [&](u32 i) { return t0 + i * TSTR; };
        const u32 m_off = w_start + (u32)nfull * TSTR + (u32)(wave * nj) * 1024u, m_end = m_off + (u32)nj * 1024u;
        const rsrc_t ks = __builtin_amdgcn_make_buffer_rsrc((char*)P.k + tb, 0, trange, 0x00020000);
        const rsrc_t vs = __builtin_amdgcn_make_buffer_rsrc((char*)P.v + tb, 0, trange, 0x00020000);
        char* const pf_base = lds + L_PF + wave * 32768;
        const u32x4 ksd = mk_srd((const char*)P.k + tb, trange), vsd = mk_srd((const char*)P.v + tb, trange);
        const u32 lb = (u32)__builtin_amdgcn_readfirstlane((u32)(uintptr_t)(__attribute__((address_space(3))) char*)pf_base);
        Ring B;
        AttnState S;
        const u32 tB = is_q ? tof(0) : tof(1);
        if (is_mlp) {
            const rsrc_t w2s = __builtin_amdgcn_make_buffer_rsrc((void*)P.w2, 0, FF * DM * 2, 0x00020000);
            const u32 w2b = (u32)(grp * 64 * 1024 + 256 * wave);
            const u32 w2off = (u32)(8 * g * 1024 + n16 * 16);
#pragma unroll
            for (int i = 0; i < 16; ++i) B.r[i] = ldb_nt(w2s, w2off, w2b + ((i >> 3) * 32 + (i & 7)) * 1024);
        } else {
            if (is_q) asm volatile("s_waitcnt vmcnt(" STR(QVP) ")" ::: "memory");
#pragma unroll
            for (int i = 0; i < 8; ++i) { if (!is_q && i % PB == 0) PACE(); B.r[8 + i] = ldb_nt(vs, voff + (i & 3) * 1024, tB + (i >> 2) * 4096); }
        }
        if (pre) {
            const float b1v = is_mlp ? bflo((u32)((const GAS unsigned short*)P.b1)[grp * 64 + lane]) : 0.f;
            wait_flag(lds + L_XF);
#pragma unroll
            for (int d = 0; d < 8; ++d) S.acc[d] = f32x4{0.f, 0.f, 0.f, 0.f};
#pragma unroll
            for (int tau = 0; tau < 2; ++tau) {
                const u32x4 xf = *(const u32x4*)(lds + L_XN + (n16 & 3) * 1024 + (128 * wave + 64 * tau + 32 * ((n16 >> 2) & 1) + 8 * g) * 2);
                pv_tile(&A.r[8 * tau], xf, S.acc);
            }
            if (n16 < 8 && (g >> 1) == (n16 >> 2)) {
                float* hp = (float*)(lds + L_HP) + ((wave * 2 + (n16 >> 2)) * 4 + (n16 & 3)) * 64 + 32 * (g & 1);
#pragma unroll
                for (int r = 0; r < 4; ++r) {
                    *(f32x4*)(hp + 8 * r) = f32x4{S.acc[0][r], S.acc[1][r], S.acc[2][r], S.acc[3][r]};
                    *(f32x4*)(hp + 8 * r + 4) = f32x4{S.acc[4][r], S.acc[5][r], S.acc[6][r], S.acc[7][r]};
                }
            }
            DBG(STAMP(6);)
            quad_barrier(lds + L_BC, 4u, lane);
            float sum = 0.f;
            {
                const float* hp = (const float*)(lds + L_HP) + wave * 64 + lane;
#pragma unroll
                for (int i = 0; i < 8; ++i) sum += hp[i * 256];
            }
            const u32 T = *(const u32*)(lds + L_T);
            if (is_q) {
                const u32 w01 = pack2(sum, dppf<XOR1>(sum));
                const u32 w23 = dppu<XOR2>(w01);
                if ((lane & 3) == 0)
                    st_unit(rs, WS_Q + (u32)((wave * 8 + ahkv) * 2048 + (16 * (2 * b + f_q1) + (lane >> 2)) * 16), mk_unit(w01, w23, T));
                DBG(STAMP(7);)
            } else {
                float h = bfr(bfr(sum) + b1v);
                h = h > 0.f ? h : 0.f;
                ((unsigned short*)(lds + L_H))[wave * 64 + lane] = (unsigned short)pack2(h * h, 0.f);
            }
            SB();
            const u32 tA = is_q ? tof(1) : tof(0);
            issue_k(A, ks, koff, tA);
            issue_v(A, vs, voff, tA);
            SB();
            if (is_mlp) {
                quad_barrier(lds + L_BC, 8u, lane);
#pragma unroll
                for (int d = 0; d < 8; ++d) S.acc[d] = f32x4{0.f, 0.f, 0.f, 0.f};
#pragma unroll
                for (int tau = 0; tau < 2; ++tau) {
                    const u32x4 hf = *(const u32x4*)(lds + L_H + (n16 >> 2) * 128 + (32 * tau + 8 * g) * 2);
                    pv_tile(&B.r[8 * tau], hf, S.acc);
                }
                publish_cols<32, MPAUX>(rs, WS_MP, S.acc, grp, 16 * wave, lane, T);
                DBG(STAMP(7);)
                SB();
                issue_v(B, vs, voff, tB);
            }
        }
        SB();
#pragma unroll
        for (int i = 0; i < 8; ++i) { if (i % PB == 0) PACE(); B.r[i] = ldb_nt(ks, koff + (i & 3) * 64, tB + (i >> 2) * 4096); }
        STAMP(11);
#pragma unroll
        for (int t = 0; t < NL; ++t) {
#pragma unroll
            for (int i = 0; i < 8; ++i) { if (i % PB == 0) LPACE(); dma16(lb + t * 16384 + i * 1024, koff, ksd, tof(2 + t) + kimm(i)); }
#pragma unroll
            for (int i = 0; i < 8; ++i) { if (i % PB == 0) LPACE(); dma16(lb + t * 16384 + 8192 + i * 1024, voff, vsd, tof(2 + t) + i * 1024); }
        }
        STAMP(1);
        wait_flag(lds + L_QF);
        STAMP(2);
        u32x4 qf[4];
#pragma unroll
        for (int st = 0; st < 4; ++st) qf[st] = *(const u32x4*)(lds + L_Q + (n16 & 3) * 256 + st * 64 + g * 16);
        const u32 T = *(const u32*)(lds + L_T);
#pragma unroll
        for (int d = 0; d < 8; ++d) S.acc[d] = f32x4{0.f, 0.f, 0.f, 0.f};
        S.m = -INFINITY; S.l = 0.f;
        proc_tile(A, S, qf, ks, vs, koff, voff, tof(2 + NL));
        DBG(STAMP(12);)
        proc_last(B, S, qf);
#pragma unroll
        for (int t = 0; t < NL; ++t) {
            asm volatile("s_waitcnt vmcnt(16)" ::: "memory");
            proc_lds(pf_base + t * 16384 + lane * 16, S, qf);
        }
        DBG(STAMP(13);)
#pragma unroll 1
        for (int i = 2 + NL; i < nfull; ++i) {
            const bool last = i + 1 == nfull;
            const u32 nrec = last ? m_end : trange;
            const rsrc_t kc = __builtin_amdgcn_make_buffer_rsrc((char*)P.k + tb, 0, nrec, 0x00020000);
            const rsrc_t vc = __builtin_amdgcn_make_buffer_rsrc((char*)P.v + tb, 0, nrec, 0x00020000);
            proc_tile(A, S, qf, kc, vc, koff, voff, last ? m_off : tof((u32)(i + 1)));
            DBG(if (i == 5) STAMP(8); if (i == 9) STAMP(9); if (i == 13) STAMP(10); )
        }
        proc_part(A, S, qf, nj);
        STAMP(3);
        const float lw = xrow_sum(S.l);
        if (n16 < 4) {
            float* mo = (float*)pf_base + n16 * 144 + g * 36;
#pragma unroll
            for (int r = 0; r < 4; ++r) {
                *(f32x4*)(mo + 8 * r) = f32x4{S.acc[0][r], S.acc[1][r], S.acc[2][r], S.acc[3][r]};
                *(f32x4*)(mo + 8 * r + 4) = f32x4{S.acc[4][r], S.acc[5][r], S.acc[6][r], S.acc[7][r]};
            }
            if (g == 0) *(f32x2*)(lds + L_ML + (wave * 4 + n16) * 8) = f32x2{S.m, lw};
        }
        wg_barrier();
        {
            f32x2 ml[4], ov[4];
#pragma unroll
            for (int w = 0; w < 4; ++w) {
                ml[w] = *(const f32x2*)(lds + L_ML + (w * 4 + wave) * 8);
                ov[w] = *(const f32x2*)((const float*)(lds + L_PF + w * 32768) + wave * 144 + (lane >> 4) * 36 + 2 * (lane & 15));
            }
            const float M = fmaxf(fmaxf(ml[0].x, ml[1].x), fmaxf(ml[2].x, ml[3].x));
            float L = 0.f, o0 = 0.f, o1 = 0.f;
#pragma unroll
            for (int w = 0; w < 4; ++w) {
                const float a = __builtin_amdgcn_exp2f(ml[w].x - M);
                L += a * ml[w].y; o0 += a * ov[w].x; o1 += a * ov[w].y;
            }
            const float inv = 1.0f / L;
            const u32 blk = (u32)((b * 32 + ahkv * 4 + wave) * 8 + split);
            st_unit(rs, WS_PO + blk * 1024 + lane * 16, mk_unit(__float_as_uint(o0 * inv), __float_as_uint(o1 * inv), T));
            if (lane == 0) {
                const float lse = M + __builtin_amdgcn_logf(L);
                st_unit(rs, WS_PL + blk * 16, mk_unit(__float_as_uint(lse), 0u, T));
            }
        }
        STAMP(4);
        {
            const int half = lane >> 5;
            const u32 blk0 = (u32)((wave * 32 + oh) * 8);
            const u32 ooff = WS_PO + (blk0 + half) * 1024 + (u32)((32 * okh + (lane & 31)) * 16);
            const u32 loff = lane < 8 ? WS_PL + (blk0 + lane) * 16 : OOB;
            u32x4 uo[4], ul;
            u32 want = 0, have = 0;
            u32 lseen = 0;
#pragma unroll 1
            for (u32 spin = 0; spin < SPIN_CAP; ++spin) {
                asm volatile("" ::: "memory");
                u32x4 t[4];
                ul = ld_unit(rs, loff);
#pragma unroll
                for (int r = 0; r < 4; ++r) t[r] = ld_unit(rs, ((want >> r) & 1u) ? ooff + r * 2048 : OOB);
#pragma unroll
                for (int r = 0; r < 4; ++r)
                    if (((want >> r) & 1u) && unit_ok(t[r], T)) { uo[r] = t[r]; have |= 1u << r; }
                lseen = (u32)__builtin_amdgcn_ballot_w64(unit_ok(ul, T)) & 0xffu;
                const bool done = lseen == 0xffu && __builtin_amdgcn_ballot_w64(have != 0xfu) == 0ull;
                if (done) break;
                const u32 gate = (__builtin_popcount(lseen) >= 6) ? 0xffu : lseen;
                const u32 gl = gate >> half;
                want = ((gl & 1u) | ((gl >> 1) & 2u) | ((gl >> 2) & 4u) | ((gl >> 3) & 8u)) & ~have;
                if (__builtin_amdgcn_ballot_w64(want != 0u) == 0ull || lseen != 0xffu) __builtin_amdgcn_s_sleep(2);
            }
            const float lse = __uint_as_float(ul.x);
            float M = fmaxf(lse, dppf<XOR1>(lse)); M = fmaxf(M, dppf<XOR2>(M)); M = fmaxf(M, dppf<HMIR>(M));
            const float al = __builtin_amdgcn_exp2f(lse - M);
            float W = al + dppf<XOR1>(al); W += dppf<XOR2>(W); W += dppf<HMIR>(W);
            const float inv = 1.0f / W;
            const u32 aw = __float_as_uint(al * inv);
#define WSEL(r) __uint_as_float(half ? __builtin_amdgcn_readlane(aw, 2 * (r) + 1) : __builtin_amdgcn_readlane(aw, 2 * (r)))
            const float a0 = WSEL(0), a1 = WSEL(1), a2 = WSEL(2), a3 = WSEL(3);
#undef WSEL
            float o0 = (a0 * __uint_as_float(uo[0].x) + a1 * __uint_as_float(uo[1].x)) + (a2 * __uint_as_float(uo[2].x) + a3 * __uint_as_float(uo[3].x));
            float o1 = (a0 * __uint_as_float(uo[0].z) + a1 * __uint_as_float(uo[1].z)) + (a2 * __uint_as_float(uo[2].z) + a3 * __uint_as_float(uo[3].z));
            o0 = x32_sum(o0); o1 = x32_sum(o1);
            if (lane < 32) *(u32*)(lds + L_O + wave * 128 + lane * 4) = pack2(o0, o1);
        }
        STAMP(5);
        wg_barrier();
        FLUSH_STAMPS();
    } else {
        __builtin_amdgcn_s_setprio(SVCPRIO);
        u32 T = epoch + 1u; if (T == 0u) T = 1u;
        T = __builtin_amdgcn_readfirstlane(T);
        if (lane == 0) *(u32*)(lds + L_T) = T;
        if (pre) {
        DBG(STAMP(8);)
        {
            float s = 0.f;
#pragma unroll
            for (int i = 0; i < 4; ++i)
#pragma unroll
                for (int w = 0; w < 4; ++w) s = dot2(A.r[i][w], 0x3f803f80u, s);
            s += dppf<XOR1>(s); s += dppf<XOR2>(s); s += dppf<ROR4>(s); s += dppf<ROR8>(s);
            const float mean = s * (1.0f / 512.0f);
            f32x2 q2 = {0.f, 0.f};
#pragma unroll
            for (int i = 0; i < 4; ++i)
#pragma unroll
                for (int w = 0; w < 4; ++w) {
                    const f32x2 d = f32x2{bflo(A.r[i][w]), bfhi(A.r[i][w])} - mean;
                    q2 += d * d;
                }
            float q = q2.x + q2.y;
            q += dppf<XOR1>(q); q += dppf<XOR2>(q); q += dppf<ROR4>(q); q += dppf<ROR8>(q);
            const float var = bfr(q * (1.0f / 512.0f));
            const float mb = bfr(mean);
            const float rden = 1.0f / bfr(__builtin_sqrtf(bfr(var + 1e-5f)));
#pragma unroll
            for (int i = 0; i < 4; ++i) {
                u32x4 o;
#pragma unroll
                for (int w = 0; w < 4; ++w) {
                    const f32x2 d = f32x2{bflo(A.r[i][w]), bfhi(A.r[i][w])} - mb;
                    const u32 c = pack2(d.x, d.y);
                    const f32x2 e = f32x2{bflo(c), bfhi(c)} * rden;
                    o[w] = pack2(e.x, e.y);
                }
                *(u32x4*)(lds + L_XN + g * 1024 + i * 256 + n16 * 16) = o;
            }
        }
        asm volatile("" ::: "memory");
        if (lane == 0) lds_st(lds + L_XF, 1u);
        asm volatile("" ::: "memory");
        }
        STAMP(1);
        {
            u32x4 u0, u1;
            const u32 off = WS_Q + (u32)(grp * 2048 + lane * 16);
            if (pre) __builtin_amdgcn_s_sleep(QPRE_X); else __builtin_amdgcn_s_sleep(QPRE_P);
#pragma unroll 1
            for (u32 spin = 0; spin < SPIN_CAP; ++spin) {
                asm volatile("" ::: "memory");
                u0 = ld_unit(rs, off); u1 = ld_unit(rs, off + 1024);
                DBG(if (spin == 0) { asm volatile("s_waitcnt vmcnt(0)" ::: "memory"); STAMP(10); })
                if (__builtin_amdgcn_ballot_w64(!(unit_ok(u0, T) & unit_ok(u1, T))) == 0ull) { STAMPV(11, 1000000ull + spin); break; }
                __builtin_amdgcn_s_sleep(QINT);
            }
            *(u32x2*)(lds + L_Q + lane * 8) = u32x2{u0.x, u0.z};
            *(u32x2*)(lds + L_Q + 512 + lane * 8) = u32x2{u1.x, u1.z};
        }
        STAMP(3);
        asm volatile("" ::: "memory");
        if (lane == 0) lds_st(lds + L_QF, 1u);
        asm volatile("" ::: "memory");
        u32x4 wa[2][8];
        {
            u32x4 wo[16];
            gcchar* wob = (gcchar*)P.wo + (size_t)(oh * 128 + 64 * okh) * 1024 + 256 * ons;
            const u32 wooff = (u32)(8 * g * 1024 + n16 * 16);
#pragma unroll
            for (int i = 0; i < 16; ++i) wo[i] = ldnt(wob + wooff + ((i >> 3) * 32 + (i & 7)) * 1024);
#pragma unroll
            for (int tau = 0; tau < 2; ++tau) {
                wa[tau][0] = tfrag<0>(&wo[8 * tau]); wa[tau][1] = tfrag<1>(&wo[8 * tau]); wa[tau][2] = tfrag<2>(&wo[8 * tau]); wa[tau][3] = tfrag<3>(&wo[8 * tau]);
                wa[tau][4] = tfrag<4>(&wo[8 * tau]); wa[tau][5] = tfrag<5>(&wo[8 * tau]); wa[tau][6] = tfrag<6>(&wo[8 * tau]); wa[tau][7] = tfrag<7>(&wo[8 * tau]);
            }
        }
        const int fb = j >> 6, fng = j & 63;
        const int fu = lane & 3, fi = lane >> 2;
        const u32 xw = *(const GAS u32*)((gcchar*)P.x + fb * 1024 + fng * 16 + fu * 4);
        const u32 b2w = *(const GAS u32*)((gcchar*)P.b2 + fng * 16 + fu * 4);
        float g0, g1;
        {
            const u32 off = WS_MP + (u32)(((fb * 64 + fng) * 32 + fi) * 64 + fu * 16);
            const bool has1 = fi + 16 < NMLP;
            u32x4 u0 = {0, 0, 0, 0}, u1 = {0, 0, 0, 0};
            constexpr u32 GCAP = SPIN_CAP;
#pragma unroll 1
            for (u32 spin = 0; spin < GCAP; ++spin) {
                __builtin_amdgcn_s_sleep(127); __builtin_amdgcn_s_sleep(127);
                asm volatile("" ::: "memory");
                u0 = __builtin_amdgcn_raw_buffer_load_b128(rs, off, 0, GAUX); u1 = __builtin_amdgcn_raw_buffer_load_b128(rs, off + 1024, 0, GAUX);
                if (__builtin_amdgcn_ballot_w64(!(unit_ok(u0, T) & (unit_ok(u1, T) | !has1))) == 0ull) break;
            }
            float a0 = __uint_as_float(u0.x) + (has1 ? __uint_as_float(u1.x) : 0.f);
            float a1 = __uint_as_float(u0.z) + (has1 ? __uint_as_float(u1.z) : 0.f);
            a0 += dppf<ROR4>(a0); a0 += dppf<ROR8>(a0); a0 = xrow_sum(a0);
            a1 += dppf<ROR4>(a1); a1 += dppf<ROR8>(a1); a1 = xrow_sum(a1);
            const float y0 = bfr(bfr(a0) + bflo(b2w)), y1 = bfr(bfr(a1) + bfhi(b2w));
            g0 = bfr(y0 / (1.0f + __expf(-y0)));
            g1 = bfr(y1 / (1.0f + __expf(-y1)));
        }
        STAMP(4);
        wg_barrier();
        wg_barrier();
        STAMP(5);
        {
            f32x4 acc[8];
#pragma unroll
            for (int d = 0; d < 8; ++d) acc[d] = f32x4{0.f, 0.f, 0.f, 0.f};
#pragma unroll
            for (int tau = 0; tau < 2; ++tau) {
                const u32x4 of = *(const u32x4*)(lds + L_O + (n16 >> 2) * 128 + (32 * tau + 8 * g) * 2);
#pragma unroll
                for (int d = 0; d < 8; ++d) acc[d] = mfma(wa[tau][d], of, acc[d]);
            }
            publish_cols<64>(rs, WS_SP, acc, oh * 2 + okh, 16 * ons, lane, T);
        }
        STAMP(6);
        {
            const u32 off = WS_SP + (u32)(((fb * 64 + fng) * 64 + fi) * 64 + fu * 16);
            u32x4 u[4];
            u32 have = 0;
#pragma unroll 1
            for (u32 spin = 0; spin < SPIN_CAP; ++spin) {
                asm volatile("" ::: "memory");
                u32x4 t[4];
#pragma unroll
                for (int r = 0; r < 4; ++r) t[r] = ld_unit(rs, ((have >> r) & 1u) ? OOB : off + r * 1024);
#pragma unroll
                for (int r = 0; r < 4; ++r)
                    if (!((have >> r) & 1u) && unit_ok(t[r], T)) { u[r] = t[r]; have |= 1u << r; }
                if (__builtin_amdgcn_ballot_w64(have != 0xfu) == 0ull) break;
                __builtin_amdgcn_s_sleep(1);
            }
            float a0 = (__uint_as_float(u[0].x) + __uint_as_float(u[1].x)) + (__uint_as_float(u[2].x) + __uint_as_float(u[3].x));
            float a1 = (__uint_as_float(u[0].z) + __uint_as_float(u[1].z)) + (__uint_as_float(u[2].z) + __uint_as_float(u[3].z));
            a0 += dppf<ROR4>(a0); a0 += dppf<ROR8>(a0); a0 = xrow_sum(a0);
            a1 += dppf<ROR4>(a1); a1 += dppf<ROR8>(a1); a1 = xrow_sum(a1);
            const float r0 = bflo(xw) + bfr(bfr(a0) * g0), r1 = bfhi(xw) + bfr(bfr(a1) * g1);
            if (lane < 4) *(GAS u32*)((GAS char*)P.out + fb * 1024 + fng * 16 + fu * 4) = pack2(r0, r1);
            if (j == 0 && lane == 0) *(GAS u32*)((GAS char*)P.ws + WS_EPOCH) = T;
        }
        STAMP(7);
        FLUSH_STAMPS();
    }
}

extern "C" void kernel_launch(void* const* d_in, const int* in_sizes, int n_in,
                              void* d_out, int out_size, void* d_ws, size_t ws_size,
                              hipStream_t stream) {
    Params P;
    P.x = d_in[0]; P.k = d_in[1]; P.v = d_in[2]; P.wq = d_in[3]; P.wo = d_in[4];
    P.w1 = d_in[6]; P.b1 = d_in[7]; P.w2 = d_in[8]; P.b2 = d_in[9];
    P.out = d_out; P.ws = d_ws;
    fused_decode_kernel<<<256, 320, 0, stream>>>(P);
}
```

```cpp
#include <hip/hip_runtime.h>
#include <hip/hip_bf16.h>
#include <stdint.h>

typedef unsigned int u32;
typedef u32 u32x4 __attribute__((ext_vector_type(4)));
typedef u32 u32x2 __attribute__((ext_vector_type(2)));
typedef float f32x4 __attribute__((ext_vector_type(4)));
typedef float f32x2 __attribute__((ext_vector_type(2)));
typedef __bf16 bf16x8 __attribute__((ext_vector_type(8)));
typedef __bf16 bf16x2 __attribute__((ext_vector_type(2)));
#define GAS __attribute__((address_space(1)))
typedef GAS const u32x4 gcu32x4;
typedef GAS const char gcchar;
#define LAS __attribute__((address_space(3)))
__device__ __forceinline__ unsigned int lds_ld(const char* p) { return *(volatile LAS const unsigned int*)(LAS const char*)p; }
__device__ __forceinline__ u32x4 lds_ld4(const char* p) { return *(volatile LAS const u32x4*)(LAS const char*)p; }
__device__ __forceinline__ void lds_st(char* p, unsigned int v) { *(volatile LAS unsigned int*)(LAS char*)p = v; }

constexpr int NB = 4, DM = 512, HKV = 8, SK = 16384, DH = 128, FF = 1792;
constexpr int NMLP = 28;
constexpr u32 WS_EPOCH = 0;
constexpr u32 WS_Q  = 0x1000;
constexpr u32 WS_CNT = 0x18000;
constexpr u32 WS_PL = 0x20000;
constexpr u32 WS_MP = 0x30000;
constexpr u32 WS_PO = 0x100000;
constexpr u32 WS_SP = 0x200000;
constexpr u32 WS_END = 0x400000;
constexpr u32 SPIN_CAP = 1u << 17;
constexpr int L_Q   = 0;
constexpr int L_T   = 1024;
constexpr int L_QF  = 1040;
constexpr int L_XF  = 1044;
constexpr int L_BC  = 1048;
constexpr int L_FIN = 1088;
constexpr int L_XN  = 2048;
constexpr int L_HP  = 6144;
constexpr int L_H   = 14336;
constexpr int L_ML  = 14848;
constexpr int L_O   = 15104;
constexpr int L_PF  = 18432;
constexpr int LDS_BYTES = L_PF + 4 * 32768;

struct Params {
    const void* x; const void* k; const void* v; const void* wq; const void* wo;
    const void* w1; const void* b1; const void* w2; const void* b2;
    void* out; void* ws;
};

__device__ __forceinline__ u32x4 ldnt(gcchar* p) { return __builtin_nontemporal_load((gcu32x4*)p); }
__device__ __forceinline__ float bflo(u32 w) { return __uint_as_float(w << 16); }
__device__ __forceinline__ float bfhi(u32 w) { return __uint_as_float(w & 0xffff0000u); }
__device__ __forceinline__ u32 pack2(float lo, float hi) {
    f32x2 v = {lo, hi};
    return __builtin_bit_cast(u32, __builtin_convertvector(v, bf16x2));
}
__device__ __forceinline__ float bfr(float x) { return bflo(pack2(x, 0.f)); }
__device__ __forceinline__ f32x4 mfma(u32x4 a, u32x4 b, f32x4 c) {
    return __builtin_amdgcn_mfma_f32_16x16x32_bf16(__builtin_bit_cast(bf16x8, a), __builtin_bit_cast(bf16x8, b), c, 0, 0, 0);
}
__device__ __forceinline__ float dot2(u32 a, u32 b, float c) {
    return __builtin_amdgcn_fdot2_f32_bf16(__builtin_bit_cast(bf16x2, a), __builtin_bit_cast(bf16x2, b), c, false);
}
template <int CTRL> __device__ __forceinline__ u32 dppu(u32 x) { return (u32)__builtin_amdgcn_mov_dpp((int)x, CTRL, 0xf, 0xf, true); }
template <int CTRL> __device__ __forceinline__ float dppf(float x) { return __uint_as_float(dppu<CTRL>(__float_as_uint(x))); }
constexpr int XOR1 = 0xB1, XOR2 = 0x4E, HMIR = 0x141, ROR4 = 0x124, ROR8 = 0x128, EVEN = 0xA0, ODD = 0xF5;
__device__ __forceinline__ float xrow_max(float x) {
    auto s = __builtin_amdgcn_permlane16_swap(__float_as_uint(x), __float_as_uint(x), false, false);
    x = fmaxf(__uint_as_float(s[0]), __uint_as_float(s[1]));
    auto t = __builtin_amdgcn_permlane32_swap(__float_as_uint(x), __float_as_uint(x), false, false);
    return fmaxf(__uint_as_float(t[0]), __uint_as_float(t[1]));
}
__device__ __forceinline__ float xrow_sum(float x) {
    auto s = __builtin_amdgcn_permlane16_swap(__float_as_uint(x), __float_as_uint(x), false, false);
    x = __uint_as_float(s[0]) + __uint_as_float(s[1]);
    auto t = __builtin_amdgcn_permlane32_swap(__float_as_uint(x), __float_as_uint(x), false, false);
    return __uint_as_float(t[0]) + __uint_as_float(t[1]);
}
__device__ __forceinline__ float x32_max(float x) {
    auto t = __builtin_amdgcn_permlane32_swap(__float_as_uint(x), __float_as_uint(x), false, false);
    return fmaxf(__uint_as_float(t[0]), __uint_as_float(t[1]));
}
__device__ __forceinline__ float x32_sum(float x) {
    auto t = __builtin_amdgcn_permlane32_swap(__float_as_uint(x), __float_as_uint(x), false, false);
    return __uint_as_float(t[0]) + __uint_as_float(t[1]);
}
__device__ __forceinline__ float wave_sum(float x) {
    x += dppf<XOR1>(x); x += dppf<XOR2>(x); x += dppf<ROR4>(x); x += dppf<ROR8>(x);
    return xrow_sum(x);
}
__device__ __forceinline__ void wg_barrier() { asm volatile("s_waitcnt lgkmcnt(0)\n\ts_barrier" ::: "memory"); }
__device__ __forceinline__ void raw_barrier() { asm volatile("s_barrier" ::: "memory"); }
__device__ __forceinline__ void quad_barrier(char* cnt, u32 target, int lane) {
    asm volatile("" ::: "memory");
    if (lane == 0) __hip_atomic_fetch_add((LAS unsigned int*)(LAS char*)cnt, 1u, __ATOMIC_RELAXED, __HIP_MEMORY_SCOPE_WORKGROUP);
#pragma unroll 1
    for (u32 spin = 0; spin < SPIN_CAP; ++spin) {
        asm volatile("" ::: "memory");
        if (lds_ld(cnt) >= target) break;
    }
    asm volatile("" ::: "memory");
}
__device__ __forceinline__ void wait_flag(const char* f) {
#pragma unroll 1
    for (u32 spin = 0; spin < SPIN_CAP; ++spin) {
        asm volatile("" ::: "memory");
        if (lds_ld(f) != 0u) break;
        __builtin_amdgcn_s_sleep(1);
    }
    asm volatile("" ::: "memory");
}
constexpr u32 OOB = 0x40000000u;

__device__ __forceinline__ u32x4 mk_unit(u32 v0, u32 v1, u32 T) { u32x4 u = {v0, T ^ v1, v1, T ^ v0}; return u; }
__device__ __forceinline__ bool unit_ok(u32x4 u, u32 T) { return ((u.y ^ u.z) == T) & ((u.w ^ u.x) == T); }
__device__ __forceinline__ void st_unit(__amdgpu_buffer_rsrc_t rs, u32 off, u32x4 u) { __builtin_amdgcn_raw_buffer_store_b128(u, rs, off, 0, 16); }
__device__ __forceinline__ u32x4 ld_unit(__amdgpu_buffer_rsrc_t rs, u32 off) { return __builtin_amdgcn_raw_buffer_load_b128(rs, off, 0, 16); }

template <int DD> __device__ __forceinline__ u32x4 tfrag(const u32x4* v) {
    constexpr u32 sel = (DD & 1) ? 0x07060302u : 0x05040100u;
    u32x4 a;
    a.x = __builtin_amdgcn_perm(v[1][DD >> 1], v[0][DD >> 1], sel);
    a.y = __builtin_amdgcn_perm(v[3][DD >> 1], v[2][DD >> 1], sel);
    a.z = __builtin_amdgcn_perm(v[5][DD >> 1], v[4][DD >> 1], sel);
    a.w = __builtin_amdgcn_perm(v[7][DD >> 1], v[6][DD >> 1], sel);
    return a;
}
__device__ __forceinline__ void pv_tile(const u32x4* v, u32x4 p, f32x4 (&acc)[8]) {
    acc[0] = mfma(tfrag<0>(v), p, acc[0]); acc[1] = mfma(tfrag<1>(v), p, acc[1]);
    acc[2] = mfma(tfrag<2>(v), p, acc[2]); acc[3] = mfma(tfrag<3>(v), p, acc[3]);
    acc[4] = mfma(tfrag<4>(v), p, acc[4]); acc[5] = mfma(tfrag<5>(v), p, acc[5]);
    acc[6] = mfma(tfrag<6>(v), p, acc[6]); acc[7] = mfma(tfrag<7>(v), p, acc[7]);
}
#define MPAUX 16
template <int NSLOT, int AUX = 16>
__device__ __forceinline__ void publish_cols(__amdgpu_buffer_rsrc_t rs, u32 base, const f32x4 (&acc)[8], int slot, int ng0, int lane, u32 T) {
    const int g = lane >> 4, b = (lane >> 2) & 3, rho = lane & 3;
    const u32 off0 = base + (u32)(((b * 64 + ng0 + 4 * g) * NSLOT + slot) * 64 + rho * 16);
#pragma unroll
    for (int r = 0; r < 4; ++r) {
        float a0 = (rho & 2) ? acc[4][r] : acc[0][r], a1 = (rho & 2) ? acc[6][r] : acc[2][r];
        float c0 = (rho & 2) ? acc[5][r] : acc[1][r], c1 = (rho & 2) ? acc[7][r] : acc[3][r];
        float v0 = (rho & 1) ? a1 : a0, v1 = (rho & 1) ? c1 : c0;
        __builtin_amdgcn_raw_buffer_store_b128(mk_unit(__float_as_uint(v0), __float_as_uint(v1), T), rs, off0 + (u32)(r * NSLOT * 64), 0, AUX);
    }
}

#define STAMP(i) do { } while (0)
#define STAMPV(i, v) do { } while (0)
#define FLUSH_STAMPS() do { } while (0)
#define DBG(x)

struct Ring { u32x4 r[16]; };
typedef __amdgpu_buffer_rsrc_t rsrc_t;
#define DMAMOD "nt"
constexpr int NT = 2;
__device__ __forceinline__ u32x4 ldb_nt(rsrc_t rs, u32 voff, u32 soff) { return __builtin_amdgcn_raw_buffer_load_b128(rs, voff, soff, NT); }
constexpr u32 TSTR = 4 * 8192;
__device__ __forceinline__ constexpr u32 kimm(int i) { return (u32)((i >> 2) * 4096 + (i & 3) * 64); }
__device__ __forceinline__ void issue_k(Ring& R, rsrc_t ks, u32 koff, u32 toff, u32 toff2) {
#pragma unroll
    for (int i = 0; i < 8; ++i) R.r[i] = ldb_nt(ks, koff + (i & 3) * 64, i < 4 ? toff : toff2);
}
__device__ __forceinline__ void issue_v(Ring& R, rsrc_t vs, u32 voff, u32 toff, u32 toff2) {
#pragma unroll
    for (int i = 0; i < 8; ++i) R.r[8 + i] = ldb_nt(vs, voff + (i & 3) * 1024, i < 4 ? toff : toff2);
}
__device__ __forceinline__ void issue_k(Ring& R, rsrc_t ks, u32 koff, u32 toff) { issue_k(R, ks, koff, toff, toff + 4096); }
__device__ __forceinline__ void issue_v(Ring& R, rsrc_t vs, u32 voff, u32 toff) { issue_v(R, vs, voff, toff, toff + 4096); }
__device__ __forceinline__ void dma16(u32 ldst, u32 voff, u32x4 srd, u32 soff) {
    u32 keep;
    asm volatile("s_mov_b32 %0, m0\n\ts_mov_b32 m0, %1\n\ts_nop 0\n\tbuffer_load_dwordx4 %2, %3, %4 offen " DMAMOD " lds\n\ts_mov_b32 m0, %0"
                 : "=&s"(keep) : "s"(ldst), "v"(voff), "s"(srd), "s"(soff) : "memory");
}
__device__ __forceinline__ u32x4 mk_srd(const void* base, u32 bytes) {
    const uint64_t a = (uint64_t)base;
    u32x4 d = {(u32)__builtin_amdgcn_readfirstlane((u32)a), (u32)__builtin_amdgcn_readfirstlane((u32)(a >> 32) & 0xffffu), bytes, 0x00020000u};
    return d;
}
#define SB() __builtin_amdgcn_sched_barrier(0)
#define QPRE 48
#define QINT 16
#define PACE_N 7
#define STR2(x) #x
#define STR(x) STR2(x)
#define PACE() asm volatile("s_waitcnt vmcnt(" STR(PACE_N) ")" ::: "memory")
#define LPACE_N 7
#define LPACE() asm volatile("s_waitcnt vmcnt(" STR(LPACE_N) ")" ::: "memory")
#define MPACE_N 15
#define PACEC(flag) asm volatile("s_cmp_lg_u32 %0, 0\n\ts_cbranch_scc1 1f\n\ts_waitcnt vmcnt(" STR(PACE_N) ")\n1:\n\ts_waitcnt vmcnt(" STR(MPACE_N) ")" :: "s"(flag) : "scc", "memory")
#define PB 1
#define SVCPRIO 3

#define GAUX 16
#define DQ 32
#define NP 528
#define QR0 2
#define QR1 5
#define NL 1
#define QPRE_P 100
#define QPRE_X 60
#define XAUX 0
#define QVP 63
#define TOPN 4
#define EARLYP 1
#define GD 1
#define GK 1
#define GATE_N 6
#define PD 1
#define PK 1
#define FINW 0
#define GLATE 1
#define XEARLY 1
#define TOUCH 14
#define TOUCHQ 0
#define TOUCHP 2
#define ORD2 6
#define WLATE 1
#define WPIN 1
#define CBX 0
#define DYN 1
#define SP 15
#define DQT 1
#define DMT 2
#define PTOUCH 0
constexpr int NP2 = ((4096 + 2 * DQ) / 8 + 3) / 4 * 4;
static_assert(NP % 4 == 0 && DQ % 4 == 0 && 4096 - 7 * NP + 2 * DQ > 160, "key split");
struct AttnState { f32x4 acc[8]; float m, l; };

__device__ __forceinline__ void qk_tile(const u32x4* k, const u32x4 (&qf)[4], f32x4& s0, f32x4& s1) {
    s0 = f32x4{0.f, 0.f, 0.f, 0.f}; s1 = f32x4{0.f, 0.f, 0.f, 0.f};
#pragma unroll
    for (int st = 0; st < 4; ++st) { s0 = mfma(k[st], qf[st], s0); s1 = mfma(k[4 + st], qf[st], s1); }
}
template <bool MASKED = false>
__device__ __forceinline__ u32x4 softmax_step(const f32x4 s0, const f32x4 s1, AttnState& S, int nj = 8) {
    constexpr float SCL = 0.08838834764831845f, LOG2E = 1.4426950408889634f;
    float z[8];
#pragma unroll
    for (int j = 0; j < 2; ++j) {
        const u32 a = pack2(s0[2 * j], s0[2 * j + 1]), c = pack2(s1[2 * j], s1[2 * j + 1]);
        const u32 a2 = pack2(bflo(a) * SCL, bfhi(a) * SCL), c2 = pack2(bflo(c) * SCL, bfhi(c) * SCL);
        z[2 * j] = bflo(a2); z[2 * j + 1] = bfhi(a2); z[4 + 2 * j] = bflo(c2); z[4 + 2 * j + 1] = bfhi(c2);
    }
    if (MASKED) {
#pragma unroll
        for (int j = 0; j < 8; ++j) z[j] = (j < nj) ? z[j] : -INFINITY;
    }
    float mx = fmaxf(fmaxf(fmaxf(z[0], z[1]), fmaxf(z[2], z[3])), fmaxf(fmaxf(z[4], z[5]), fmaxf(z[6], z[7])));
    mx = xrow_max(mx) * LOG2E;
    if (__builtin_amdgcn_ballot_w64(mx > S.m) != 0ull) {
        const float mn = fmaxf(S.m, mx);
        const float al = __builtin_amdgcn_exp2f(S.m - mn);
        S.m = mn; S.l *= al;
        f32x2 al2 = {al, al};
        asm volatile("" : "+v"(al2));
        const f32x4 al4 = {al2.x, al2.y, al2.x, al2.y};
#pragma unroll
        for (int d = 0; d < 8; ++d) S.acc[d] *= al4;
    }
    float p[8];
#pragma unroll
    for (int j = 0; j < 8; ++j) p[j] = __builtin_amdgcn_exp2f(__builtin_fmaf(z[j], LOG2E, -S.m));
    S.l += ((p[0] + p[1]) + (p[2] + p[3])) + ((p[4] + p[5]) + (p[6] + p[7]));
    u32x4 pf = {pack2(p[0], p[1]), pack2(p[2], p[3]), pack2(p[4], p[5]), pack2(p[6], p[7])};
    return pf;
}
__device__ __forceinline__ void proc_tile(Ring& R, AttnState& S, const u32x4 (&qf)[4], rsrc_t ks, rsrc_t vs, u32 koff, u32 voff, u32 toff) {
    f32x4 s0, s1;
    qk_tile(&R.r[0], qf, s0, s1);
    SB();
    issue_k(R, ks, koff, toff);
    SB();
    const u32x4 pf = softmax_step(s0, s1, S);
    pv_tile(&R.r[8], pf, S.acc);
    SB();
    issue_v(R, vs, voff, toff);
    SB();
}
__device__ __forceinline__ u32 proc_tile_c(Ring& R, AttnState& S, const u32x4 (&qf)[4], rsrc_t ks, rsrc_t vs, u32 koff, u32 voff, u32 toff, rsrc_t cs, bool dyn) {
    f32x4 s0, s1;
    qk_tile(&R.r[0], qf, s0, s1);
    SB();
    issue_k(R, ks, koff, toff);
    SB();
    const u32x4 pf = softmax_step(s0, s1, S);
    asm volatile("" : "+v"(S.l));
    pv_tile(&R.r[8], pf, S.acc);
    SB();
    u32 claim = 0;
    if (dyn) claim = (u32)__builtin_amdgcn_raw_ptr_buffer_atomic_add_i32(1, cs, (int)voff, 0, 16);
    SB();
    issue_v(R, vs, voff, toff);
    SB();
    return claim;
}
__device__ __forceinline__ void proc_last(Ring& R, AttnState& S, const u32x4 (&qf)[4]) {
    f32x4 s0, s1;
    qk_tile(&R.r[0], qf, s0, s1);
    const u32x4 pf = softmax_step(s0, s1, S);
    pv_tile(&R.r[8], pf, S.acc);
}
__device__ __forceinline__ void proc_part(Ring& R, AttnState& S, const u32x4 (&qf)[4], int nj) {
    f32x4 s0, s1;
    qk_tile(&R.r[0], qf, s0, s1);
    const u32x4 pf = softmax_step<true>(s0, s1, S, nj);
    pv_tile(&R.r[8], pf, S.acc);
}
__device__ __forceinline__ void proc_lds(const char* sl, AttnState& S, const u32x4 (&qf)[4]) {
    u32x4 c[8];
#pragma unroll
    for (int i = 0; i < 8; ++i) c[i] = *(const u32x4*)(sl + i * 1024);
    f32x4 s0, s1;
    qk_tile(c, qf, s0, s1);
    SB();
#pragma unroll
    for (int i = 0; i < 8; ++i) c[i] = *(const u32x4*)(sl + 8192 + i * 1024);
    const u32x4 pf = softmax_step(s0, s1, S);
    pv_tile(c, pf, S.acc);
}

__global__ void __launch_bounds__(320, 2) fused_decode_kernel(Params P) {
    __shared__ __attribute__((aligned(16))) char lds[LDS_BYTES];
    const int tid = threadIdx.x, lane = tid & 63;
    const int wave = __builtin_amdgcn_readfirstlane(tid >> 6);
    const int j = blockIdx.x;
    DBG(unsigned long long ts[NSTAMP]; for (int z = 0; z < NSTAMP; ++z) ts[z] = 0;)
    STAMP(0);
    const int grp = j >> 3, split = j & 7;
    const int b = grp >> 3, ahkv = grp & 7;
    const int rank = (split - ahkv - 1) & 7;
    const bool grp_mlp = grp < NMLP;
    int rk = rank;
    asm volatile("" : "+s"(rk));
    int f_q = ((1 << QR0 | 1 << QR1) >> rk) & 1;
    int f_m = (((grp - NMLP) >> 24 & 0x80) >> rk) & 1;
    int f_q1 = (rk >> 2) & f_q;
    static_assert(QR0 < 4 && QR1 >= 4, "rank bit 2 tells the two q slices apart");
    asm volatile("" : "+s"(f_q), "+s"(f_m), "+s"(f_q1));
    int f_pre = f_q | f_m, f_svc = wave >> 2;
    asm volatile("" : "+s"(f_pre), "+s"(f_svc));
    const bool is_q = f_q != 0, is_mlp = f_m != 0, pre = f_pre != 0;
    const int g = lane >> 4, n16 = lane & 15;
    const int ns = SP - f_q * DQT - f_m * DMT;
    const u32 w_start = (u32)(rank * SP - (rank > QR0 ? DQT : 0) - (rank > QR1 ? DQT : 0)) * 32768u;
    const u32 nstat = (u32)(8 * SP - 2 * DQT - (grp_mlp ? DMT : 0));
    const u32 pool0 = nstat * 32768u, npool = 512u - 4u * nstat;
    const size_t tb = (size_t)grp * (SK * DH * 2);
    constexpr u32 trange = SK * DH * 2;
    const u32 t0 = w_start + (u32)wave * 8192;
    const u32 koff = (u32)((4 * (n16 & 3) + (n16 >> 2)) * 256 + g * 16);
    const u32 voff = (u32)(lane * 16);
    Ring A;
    u32 epoch = 0;
    {
        const u32 mq = 0u - (u32)f_q, mp = 0u - (u32)f_pre, ms = 0u - (u32)f_svc;
        auto sel = [](u32 m, u32 x, u32 y) { return y ^ ((x ^ y) & m); };
        const bool svc = f_svc != 0;
        const u32 wstr = sel(mq, 8192u, (u32)(FF * 2));
        const u32 wcol = sel(mq, (u32)(ahkv * 1024 + ((2 * b + f_q1) ^ CBX) * 128), (u32)(grp * 128));
        const u32 wb = (u32)(128 * wave) * wstr + wcol;
        const u32 nb = sel(mp, 512u * wstr, trange), na = sel(ms, 4096u, nb);
        const u32 wv = (u32)(32 * (n16 >> 3) + 8 * g) * wstr + (u32)((n16 & 7) * 16);
        const u32 va = sel(ms, (u32)(g * 1024 + n16 * 16), sel(mp, wv, koff));
        const u32 vb = sel(mp, wv, voff);
        const u32 sa = sel(ms, sel(mp, 0u, OOB), sel(mp, wb, t0)), sb = sel(mp, wb + 64u * wstr, t0);
        const u32 a1 = sel(ms, 256u, sel(mp, wstr, 64u)), a2 = sel(mp, 4u * wstr, 4096u);
        const u32 b1 = sel(mp, wstr, 1024u);
        SB();
        const uint64_t Mq = (uint64_t)(int64_t)(int)mq, Mp = (uint64_t)(int64_t)(int)mp, Ms = (uint64_t)(int64_t)(int)ms;
        auto sel64 = [](uint64_t m, uint64_t x, uint64_t y) { return y ^ ((x ^ y) & m); };
        const uint64_t wp = sel64(Mq, (uint64_t)P.wq, (uint64_t)P.w1);
        const uint64_t pa = sel64(Ms, (uint64_t)P.x, sel64(Mp, wp, (uint64_t)P.k + tb));
        const uint64_t pb = sel64(Mp, wp, (uint64_t)P.v + tb);
        const rsrc_t ra = __builtin_amdgcn_make_buffer_rsrc((void*)pa, 0, na, 0x00020000);
        const rsrc_t rb = __builtin_amdgcn_make_buffer_rsrc((void*)pb, 0, nb, 0x00020000);
        if (lane < 3) lds_st(lds + L_QF + lane * 4, 0u);
#pragma unroll
        for (int i = 0; i < 4; ++i) A.r[i] = __builtin_amdgcn_raw_buffer_load_b128(ra, va, sa + (u32)i * a1, XAUX);
        epoch = __builtin_amdgcn_raw_buffer_load_b32(__builtin_amdgcn_make_buffer_rsrc(P.ws, 0, 16, 0x00020000), 0, ~ms & OOB, 0);
        SB(); wg_barrier(); SB();
        if (!svc) {
#pragma unroll
            for (int i = 4; i < 16; ++i)
                A.r[i] = i < 8 ? ldb_nt(ra, va, sa + (u32)(i & 3) * a1 + a2) : ldb_nt(rb, vb, sb + (u32)(i & 3) * b1 + (u32)((i - 8) >> 2) * a2);
        }
        SB();
    }
    STAMP(15);
    int wave2 = wave;
    asm volatile("" : "+s"(wave2));
    const int hkv = j & 7, pidx = j >> 3;
    const __amdgpu_buffer_rsrc_t rs = __builtin_amdgcn_make_buffer_rsrc(P.ws, 0, WS_END, 0x00020000);
    const int oh = hkv * 4 + (pidx & 3), okh = (pidx >> 2) & 1, ons = pidx >> 3;

    if (wave2 < 4) {
        auto tof = [&](u32 i) { return t0 + i * TSTR; };
        const rsrc_t ks = __builtin_amdgcn_make_buffer_rsrc((char*)P.k + tb, 0, trange, 0x00020000);
        const rsrc_t vs = __builtin_amdgcn_make_buffer_rsrc((char*)P.v + tb, 0, trange, 0x00020000);
        char* const pf_base = lds + L_PF + wave * 32768;
        const u32x4 ksd = mk_srd((const char*)P.k + tb, trange), vsd = mk_srd((const char*)P.v + tb, trange);
        const u32 lb = (u32)__builtin_amdgcn_readfirstlane((u32)(uintptr_t)(__attribute__((address_space(3))) char*)pf_base);
        Ring B;
        AttnState S;
        const u32 tB = is_q ? tof(0) : tof(1);
        if (is_mlp) {
            const rsrc_t w2s = __builtin_amdgcn_make_buffer_rsrc((void*)P.w2, 0, FF * DM * 2, 0x00020000);
            const u32 w2b = (u32)(grp * 64 * 1024 + 256 * wave);
            const u32 w2off = (u32)(8 * g * 1024 + n16 * 16);
#pragma unroll
            for (int i = 0; i < 16; ++i) B.r[i] = ldb_nt(w2s, w2off, w2b + ((i >> 3) * 32 + (i & 7)) * 1024);
        } else {
            if (is_q) asm volatile("s_waitcnt vmcnt(" STR(QVP) ")" ::: "memory");
#pragma unroll
            for (int i = 0; i < 8; ++i) { if (!is_q && i % PB == 0) PACE(); B.r[8 + i] = ldb_nt(vs, voff + (i & 3) * 1024, tB + (i >> 2) * 4096); }
        }
        if (pre) {
            const float b1v = is_mlp ? bflo((u32)((const GAS unsigned short*)P.b1)[grp * 64 + lane]) : 0.f;
            wait_flag(lds + L_XF);
#pragma unroll
            for (int d = 0; d < 8; ++d) S.acc[d] = f32x4{0.f, 0.f, 0.f, 0.f};
#pragma unroll
            for (int tau = 0; tau < 2; ++tau) {
                const u32x4 xf = *(const u32x4*)(lds + L_XN + (n16 & 3) * 1024 + (128 * wave + 64 * tau + 32 * ((n16 >> 2) & 1) + 8 * g) * 2);
                pv_tile(&A.r[8 * tau], xf, S.acc);
            }
            if (n16 < 8 && (g >> 1) == (n16 >> 2)) {
                float* hp = (float*)(lds + L_HP) + ((wave * 2 + (n16 >> 2)) * 4 + (n16 & 3)) * 64 + 32 * (g & 1);
#pragma unroll
                for (int r = 0; r < 4; ++r) {
                    *(f32x4*)(hp + 8 * r) = f32x4{S.acc[0][r], S.acc[1][r], S.acc[2][r], S.acc[3][r]};
                    *(f32x4*)(hp + 8 * r + 4) = f32x4{S.acc[4][r], S.acc[5][r], S.acc[6][r], S.acc[7][r]};
                }
            }
            DBG(STAMP(6);)
            quad_barrier(lds + L_BC, 4u, lane);
            float sum = 0.f;
            {
                const float* hp = (const float*)(lds + L_HP) + wave * 64 + lane;
#pragma unroll
                for (int i = 0; i < 8; ++i) sum += hp[i * 256];
            }
            const u32 T = *(const u32*)(lds + L_T);
            if (is_q) {
                const u32 w01 = pack2(sum, dppf<XOR1>(sum));
                const u32 w23 = dppu<XOR2>(w01);
                if ((lane & 3) == 0)
                    st_unit(rs, WS_Q + (u32)((wave * 8 + ahkv) * 2048 + (16 * ((2 * b + f_q1) ^ CBX) + (lane >> 2)) * 16), mk_unit(w01, w23, T));
                DBG(STAMP(7);)
            } else {
                float h = bfr(bfr(sum) + b1v);
                h = h > 0.f ? h : 0.f;
                ((unsigned short*)(lds + L_H))[wave * 64 + lane] = (unsigned short)pack2(h * h, 0.f);
            }
            SB();
            const u32 tA = is_q ? tof(1) : tof(0);
            issue_k(A, ks, koff, tA);
            issue_v(A, vs, voff, tA);
            SB();
            if (is_mlp) {
                quad_barrier(lds + L_BC, 8u, lane);
#pragma unroll
                for (int d = 0; d < 8; ++d) S.acc[d] = f32x4{0.f, 0.f, 0.f, 0.f};
#pragma unroll
                for (int tau = 0; tau < 2; ++tau) {
                    const u32x4 hf = *(const u32x4*)(lds + L_H + (n16 >> 2) * 128 + (32 * tau + 8 * g) * 2);
                    pv_tile(&B.r[8 * tau], hf, S.acc);
                }
                publish_cols<32, MPAUX>(rs, WS_MP, S.acc, grp, 16 * wave, lane, T);
                DBG(STAMP(7);)
                SB();
                issue_v(B, vs, voff, tB);
            }
        }
        SB();
#pragma unroll
        for (int i = 0; i < 8; ++i) { if (i % PB == 0) PACE(); B.r[i] = ldb_nt(ks, koff + (i & 3) * 64, tB + (i >> 2) * 4096); }
        STAMP(11);
#pragma unroll
        for (int t = 0; t < NL; ++t) {
#pragma unroll
            for (int i = 0; i < 8; ++i) { if (i % PB == 0) LPACE(); dma16(lb + t * 16384 + i * 1024, koff, ksd, tof(2 + t) + kimm(i)); }
#pragma unroll
            for (int i = 0; i < 8; ++i) { if (i % PB == 0) LPACE(); dma16(lb + t * 16384 + 8192 + i * 1024, voff, vsd, tof(2 + t) + i * 1024); }
        }
        STAMP(1);
        wait_flag(lds + L_QF);
        STAMP(2);
        u32x4 qf[4];
#pragma unroll
        for (int st = 0; st < 4; ++st) qf[st] = *(const u32x4*)(lds + L_Q + (n16 & 3) * 256 + st * 64 + g * 16);
        const u32 T = *(const u32*)(lds + L_T);
#pragma unroll
        for (int d = 0; d < 8; ++d) S.acc[d] = f32x4{0.f, 0.f, 0.f, 0.f};
        S.m = -INFINITY; S.l = 0.f;
        proc_tile(A, S, qf, ks, vs, koff, voff, tof(2 + NL));
        DBG(STAMP(12);)
        proc_last(B, S, qf);
#pragma unroll
        for (int t = 0; t < NL; ++t) {
            asm volatile("s_waitcnt vmcnt(16)" ::: "memory");
            proc_lds(pf_base + t * 16384 + lane * 16, S, qf);
        }
        DBG(STAMP(13);)
        {
            const rsrc_t cs = __builtin_amdgcn_make_buffer_rsrc((char*)P.ws + WS_CNT + grp * 256, 0, 4, 0x00020000);
            u32 nxt = tof(3 + NL);
#pragma unroll 1
            for (int i = 2 + NL;; ++i) {
                const bool dyn = i + 2 >= ns;
                const u32 claim = proc_tile_c(A, S, qf, ks, vs, koff, voff, nxt, cs, dyn);
                DBG(if (i == 5) STAMP(8); if (i == 9) STAMP(9); if (i == ns - 2) STAMP(10); )
                if (dyn) {
                    const u32 c = __builtin_amdgcn_readfirstlane(claim);
                    if (c >= npool) { DBG(STAMPV(6, 1000000ull + i);) break; }
                    nxt = pool0 + c * 8192u;
                } else nxt = tof((u32)(i + 2));
            }
            proc_last(A, S, qf);
        }
        STAMP(3);
        const int half = lane >> 5;
        const u32 blk0 = (u32)((wave * 32 + oh) * 8);
        const u32 ooff = WS_PO + (blk0 + half) * 1024 + (u32)((32 * okh + (lane & 31)) * 16);
        const u32 loff = lane < 8 ? WS_PL + (blk0 + lane) * 16 : OOB;
        struct Pass { u32x4 l, t[4]; };
        Pass p0, p1;
        u32 want = 0xfu, have = 0;
        u32 lseen = 0;
        u32 lsev = 0;
        f32x2 uo[4];
        auto g_issue = [&](Pass& p) {
            SB();
            p.l = ld_unit(rs, loff);
#pragma unroll
            for (int r = 0; r < 4; ++r) p.t[r] = ld_unit(rs, ((want >> r) & 1u) ? ooff + r * 2048 : OOB);
            SB();
        };
        auto g_eval = [&](const Pass& p) -> bool {
            SB();
            const bool lok = unit_ok(p.l, T);
            lsev = lok ? p.l.x : lsev;
            lseen |= (u32)__builtin_amdgcn_ballot_w64(lok) & 0xffu;
#pragma unroll
            for (int r = 0; r < 4; ++r) {
                const bool ok = unit_ok(p.t[r], T);
                uo[r].x = ok ? __uint_as_float(p.t[r].x) : uo[r].x; uo[r].y = ok ? __uint_as_float(p.t[r].z) : uo[r].y;
                have |= ok ? 1u << r : 0u;
            }
            const u32 gate = (__builtin_popcount(lseen) >= GATE_N) ? 0xffu : lseen;
            const u32 gl = gate >> half;
            want = ((gl & 1u) | ((gl >> 1) & 2u) | ((gl >> 2) & 4u) | ((gl >> 3) & 8u)) & ~have;
            asm volatile("" : "+v"(uo[0].x), "+v"(uo[0].y), "+v"(uo[1].x), "+v"(uo[1].y), "+v"(uo[2].x), "+v"(uo[2].y), "+v"(uo[3].x), "+v"(uo[3].y),
                              "+v"(lsev), "+v"(have), "+v"(want));
            return lseen == 0xffu && __builtin_amdgcn_ballot_w64(have != 0xfu) == 0ull;
        };
#pragma unroll
        for (int r = 0; r < 4; ++r) uo[r] = f32x2{0.f, 0.f};
        const float lw = xrow_sum(S.l);
        if (n16 < 4) {
            float* mo = (float*)pf_base + n16 * 144 + g * 36;
#pragma unroll
            for (int d = 0; d < 8; ++d) *(f32x4*)(mo + 4 * d) = S.acc[d];
            if (g == 0) *(f32x2*)(lds + L_ML + (wave * 4 + n16) * 8) = f32x2{S.m, lw};
        }
        SB();
        g_issue(p0);
        SB();
        wg_barrier();
        {
            f32x2 ml[4], ov[4];
#pragma unroll
            for (int w = 0; w < 4; ++w) {
                ml[w] = *(const f32x2*)(lds + L_ML + (w * 4 + wave) * 8);
                const float* q = (const float*)(lds + L_PF + w * 32768) + wave * 144 + (lane >> 4) * 36 + 8 * (lane & 3) + ((lane & 15) >> 2);
                ov[w] = f32x2{q[0], q[4]};
            }
            const float M = fmaxf(fmaxf(ml[0].x, ml[1].x), fmaxf(ml[2].x, ml[3].x));
            float L = 0.f, o0 = 0.f, o1 = 0.f;
#pragma unroll
            for (int w = 0; w < 4; ++w) {
                const float a = __builtin_amdgcn_exp2f(ml[w].x - M);
                L += a * ml[w].y; o0 += a * ov[w].x; o1 += a * ov[w].y;
            }
            const float inv = __builtin_amdgcn_rcpf(L);
            const u32 blk = (u32)((b * 32 + ahkv * 4 + wave) * 8 + split);
            st_unit(rs, WS_PO + blk * 1024 + lane * 16, mk_unit(__float_as_uint(o0 * inv), __float_as_uint(o1 * inv), T));
            if (lane == 0) {
                const float lse = M + __builtin_amdgcn_logf(L);
                st_unit(rs, WS_PL + blk * 16, mk_unit(__float_as_uint(lse), 0u, T));
            }
        }
        STAMP(4);
        {
            SB();
#pragma unroll 1
            for (u32 spin = 0; spin < SPIN_CAP; ++spin) {
                asm volatile("" ::: "memory");
                if (g_eval(p0)) { STAMPV(14, 1000000ull + spin); break; }
                __builtin_amdgcn_s_sleep(GK);
                g_issue(p0);
            }
            const float lse = __uint_as_float(lsev);
            float M = fmaxf(lse, dppf<XOR1>(lse)); M = fmaxf(M, dppf<XOR2>(M)); M = fmaxf(M, dppf<HMIR>(M));
            const float al = __builtin_amdgcn_exp2f(lse - M);
            float W = al + dppf<XOR1>(al); W += dppf<XOR2>(W); W += dppf<HMIR>(W);
            const float inv = __builtin_amdgcn_rcpf(W);
            const u32 aw = __float_as_uint(al * inv);
            float aa[4];
#pragma unroll
            for (int r = 0; r < 4; ++r) {
                const u32 e = __builtin_amdgcn_readlane(aw, 2 * r), o = __builtin_amdgcn_readlane(aw, 2 * r + 1);
                aa[r] = __uint_as_float(half ? o : e);
            }
            const float a0 = aa[0], a1 = aa[1], a2 = aa[2], a3 = aa[3];
            float o0 = (a0 * uo[0].x + a1 * uo[1].x) + (a2 * uo[2].x + a3 * uo[3].x);
            float o1 = (a0 * uo[0].y + a1 * uo[1].y) + (a2 * uo[2].y + a3 * uo[3].y);
            o0 = x32_sum(o0); o1 = x32_sum(o1);
            if (lane < 32) *(u32*)(lds + L_O + wave * 128 + lane * 4) = pack2(o0, o1);
        }
        STAMP(5);
        wg_barrier();
        if (wave == FINW) {
            const int fb = j >> 6, fng = j & 63;
            const int fu = lane & 3, fi = lane >> 2;
            const u32 off = WS_SP + (u32)(((fb * 64 + fng) * 64 + fi) * 64 + fu * 16);
            f32x2 u[4];
#pragma unroll
            for (int r = 0; r < 4; ++r) u[r] = f32x2{0.f, 0.f};
            u32 hv = 0;
            auto f_issue = [&](u32x4 (&t)[4]) {
                SB();
#pragma unroll
                for (int r = 0; r < 4; ++r) t[r] = ld_unit(rs, ((hv >> r) & 1u) ? OOB : off + r * 1024);
                SB();
            };
            auto f_eval = [&](const u32x4 (&t)[4]) -> bool {
                SB();
#pragma unroll
                for (int r = 0; r < 4; ++r) {
                    const bool ok = unit_ok(t[r], T);
                    u[r].x = ok ? __uint_as_float(t[r].x) : u[r].x; u[r].y = ok ? __uint_as_float(t[r].z) : u[r].y;
                    hv |= ok ? 1u << r : 0u;
                }
                asm volatile("" : "+v"(u[0].x), "+v"(u[0].y), "+v"(u[1].x), "+v"(u[1].y), "+v"(u[2].x), "+v"(u[2].y), "+v"(u[3].x), "+v"(u[3].y), "+v"(hv));
                return __builtin_amdgcn_ballot_w64(hv != 0xfu) == 0ull;
            };
            const u32x4 fin = *(const u32x4*)(lds + L_FIN + fu * 16);
            u32x4 t0[4], t1[4], t2[4];
            f_issue(t0);
#pragma unroll 1
            for (u32 spin = 0; spin < SPIN_CAP; ++spin) {
                asm volatile("" ::: "memory");
                if (f_eval(t0)) break;
                f_issue(t0);
                __builtin_amdgcn_s_sleep(PK);
            }
            float a0 = (u[0].x + u[1].x) + (u[2].x + u[3].x);
            float a1 = (u[0].y + u[1].y) + (u[2].y + u[3].y);
            a0 += dppf<ROR4>(a0); a0 += dppf<ROR8>(a0); a0 = xrow_sum(a0);
            a1 += dppf<ROR4>(a1); a1 += dppf<ROR8>(a1); a1 = xrow_sum(a1);
            const float r0 = bflo(fin.z) + bfr(bfr(a0) * __uint_as_float(fin.x)), r1 = bfhi(fin.z) + bfr(bfr(a1) * __uint_as_float(fin.y));
            if (lane < 4) *(GAS u32*)((GAS char*)P.out + fb * 1024 + fng * 16 + fu * 4) = pack2(r0, r1);
            if (j == 0 && lane == 0) *(GAS u32*)((GAS char*)P.ws + WS_EPOCH) = T;
            DBG(STAMP(14);)
        }
        FLUSH_STAMPS();
    } else {
        __builtin_amdgcn_s_setprio(SVCPRIO);
        SB();
        if (is_q) __builtin_amdgcn_raw_buffer_store_b32(0u, rs, lane == 0 ? WS_CNT + (u32)grp * 256u : OOB, 0, 16);
        SB();
        u32 tch[4] = {0, 0, 0, 0};
        auto touch = [&]() {
            const int fb = j >> 6, fng = j & 63;
            SB();
            if (TOUCH & 1) tch[0] = *(const GAS u32*)((gcchar*)P.wo + (size_t)(oh * 128 + 64 * okh + lane) * 1024 + 256 * ons);
            if (TOUCH & 2) tch[1] = __builtin_amdgcn_raw_buffer_load_b32(rs, WS_SP + (u32)(((fb * 64 + fng) * 64 + lane) * 64), 0, 0);
            if (TOUCH & 4) tch[2] = __builtin_amdgcn_raw_buffer_load_b32(rs, WS_SP + (u32)((((lane >> 4) * 64 + 16 * ons + (lane & 15)) * 64 + oh * 2 + okh) * 64), 0, 0);
            if (TOUCH & 8) tch[3] = *(const GAS u32*)((gcchar*)P.out + fb * 1024 + fng * 16);
            SB();
        };
        u32 T = epoch + 1u; if (T == 0u) T = 1u;
        T = __builtin_amdgcn_readfirstlane(T);
        if (lane == 0) *(u32*)(lds + L_T) = T;
        if (pre) {
        DBG(STAMP(8);)

        {
            float s = 0.f;
#pragma unroll
            for (int i = 0; i < 4; ++i)
#pragma unroll
                for (int w = 0; w < 4; ++w) s = dot2(A.r[i][w], 0x3f803f80u, s);
            s += dppf<XOR1>(s); s += dppf<XOR2>(s); s += dppf<ROR4>(s); s += dppf<ROR8>(s);
            const float mean = s * (1.0f / 512.0f);
            f32x2 q2 = {0.f, 0.f};
#pragma unroll
            for (int i = 0; i < 4; ++i)
#pragma unroll
                for (int w = 0; w < 4; ++w) {
                    const f32x2 d = f32x2{bflo(A.r[i][w]), bfhi(A.r[i][w])} - mean;
                    q2 += d * d;
                }
            float q = q2.x + q2.y;
            q += dppf<XOR1>(q); q += dppf<XOR2>(q); q += dppf<ROR4>(q); q += dppf<ROR8>(q);
            const float var = bfr(q * (1.0f / 512.0f));
            const float mb = bfr(mean);
            const float rden = 1.0f / bfr(__builtin_sqrtf(bfr(var + 1e-5f)));
#pragma unroll
            for (int i = 0; i < 4; ++i) {
                u32x4 o;
#pragma unroll
                for (int w = 0; w < 4; ++w) {
                    const f32x2 d = f32x2{bflo(A.r[i][w]), bfhi(A.r[i][w])} - mb;
                    const u32 c = pack2(d.x, d.y);
                    const f32x2 e = f32x2{bflo(c), bfhi(c)} * rden;
                    o[w] = pack2(e.x, e.y);
                }
                *(u32x4*)(lds + L_XN + g * 1024 + i * 256 + n16 * 16) = o;
            }
        }
        asm volatile("s_waitcnt vmcnt(0)" ::: "memory");
        asm volatile("" ::: "memory");
        if (lane == 0) lds_st(lds + L_XF, 1u);
        asm volatile("" ::: "memory");
        }
        u32x4 wa[2][8];
        u32x4 wo[16];
        auto issue_wo = [&]() {
            gcchar* wob = (gcchar*)P.wo + (size_t)(oh * 128 + 64 * okh) * 1024 + 256 * ons;
            const u32 wooff = (u32)(8 * g * 1024 + n16 * 16);
#pragma unroll
            for (int i = 0; i < 16; ++i) wo[i] = ldnt(wob + wooff + ((i >> 3) * 32 + (i & 7)) * 1024);
        };
        auto perm_wo = [&]() {
#pragma unroll
            for (int tau = 0; tau < 2; ++tau) {
                wa[tau][0] = tfrag<0>(&wo[8 * tau]); wa[tau][1] = tfrag<1>(&wo[8 * tau]); wa[tau][2] = tfrag<2>(&wo[8 * tau]); wa[tau][3] = tfrag<3>(&wo[8 * tau]);
                wa[tau][4] = tfrag<4>(&wo[8 * tau]); wa[tau][5] = tfrag<5>(&wo[8 * tau]); wa[tau][6] = tfrag<6>(&wo[8 * tau]); wa[tau][7] = tfrag<7>(&wo[8 * tau]);
            }
        };
        const int fb = j >> 6, fng = j & 63;
        const int fu = lane & 3, fi = lane >> 2;
        const u32 xw = *(const GAS u32*)((gcchar*)P.x + fb * 1024 + fng * 16 + fu * 4);
        const u32 b2w = *(const GAS u32*)((gcchar*)P.b2 + fng * 16 + fu * 4);
        STAMP(1);
        {
            u32x4 u0, u1;
            const u32 off = WS_Q + (u32)(grp * 2048 + lane * 16);
            if (pre) __builtin_amdgcn_s_sleep(QPRE_X); else __builtin_amdgcn_s_sleep(QPRE_P);
#pragma unroll 1
            for (u32 spin = 0; spin < SPIN_CAP; ++spin) {
                asm volatile("" ::: "memory");
                u0 = ld_unit(rs, off); u1 = ld_unit(rs, off + 1024);
                DBG(if (spin == 0) { asm volatile("s_waitcnt vmcnt(0)" ::: "memory"); STAMP(10); })
                if (__builtin_amdgcn_ballot_w64(!(unit_ok(u0, T) & unit_ok(u1, T))) == 0ull) { STAMPV(11, 1000000ull + spin); break; }
                __builtin_amdgcn_s_sleep(QINT);
            }
            *(u32x2*)(lds + L_Q + lane * 8) = u32x2{u0.x, u0.z};
            *(u32x2*)(lds + L_Q + 512 + lane * 8) = u32x2{u1.x, u1.z};
        }
        STAMP(3);
        asm volatile("" ::: "memory");
        if (lane == 0) lds_st(lds + L_QF, 1u);
        asm volatile("" ::: "memory");
        auto pin_wa = [&]() {
#pragma unroll
            for (int tau = 0; tau < 2; ++tau)
#pragma unroll
                for (int d = 0; d < 8; ++d) asm volatile("" : "+v"(wa[tau][d]));
        };
        auto gate = [&]() {
            const u32 off = WS_MP + (u32)(((fb * 64 + fng) * 32 + fi) * 64 + fu * 16);
            const bool has1 = fi + 16 < NMLP;
            u32x4 u0 = {0, 0, 0, 0}, u1 = {0, 0, 0, 0};
#pragma unroll 1
            for (u32 spin = 0; spin < SPIN_CAP; ++spin) {
                asm volatile("" ::: "memory");
                u0 = __builtin_amdgcn_raw_buffer_load_b128(rs, off, 0, GAUX); u1 = __builtin_amdgcn_raw_buffer_load_b128(rs, off + 1024, 0, GAUX);
                if (__builtin_amdgcn_ballot_w64(!(unit_ok(u0, T) & (unit_ok(u1, T) | !has1))) == 0ull) break;
                __builtin_amdgcn_s_sleep(2);
            }
            float a0 = __uint_as_float(u0.x) + (has1 ? __uint_as_float(u1.x) : 0.f);
            float a1 = __uint_as_float(u0.z) + (has1 ? __uint_as_float(u1.z) : 0.f);
            a0 += dppf<ROR4>(a0); a0 += dppf<ROR8>(a0); a0 = xrow_sum(a0);
            a1 += dppf<ROR4>(a1); a1 += dppf<ROR8>(a1); a1 = xrow_sum(a1);
            const float y0 = bfr(bfr(a0) + bflo(b2w)), y1 = bfr(bfr(a1) + bfhi(b2w));
            const float g0 = bfr(y0 / (1.0f + __expf(-y0)));
            const float g1 = bfr(y1 / (1.0f + __expf(-y1)));
            if (lane < 4) *(u32x4*)(lds + L_FIN + lane * 16) = u32x4{__float_as_uint(g0), __float_as_uint(g1), xw, 0u};
        };
        STAMP(4);
        wg_barrier();
        {
            __builtin_amdgcn_s_sleep(ORD2);
            asm volatile("" ::: "memory");
            const u32 goff = WS_MP + (u32)(((fb * 64 + fng) * 32 + fi) * 64 + fu * 16);
            const bool has1 = fi + 16 < NMLP;
            SB();
            u32x4 m0 = ld_unit(rs, goff), m1 = ld_unit(rs, goff + 1024);
            SB();
            issue_wo();
            SB();
            touch();
            SB();
            if (__builtin_amdgcn_ballot_w64(!(unit_ok(m0, T) & (unit_ok(m1, T) | !has1))) != 0ull) {
#pragma unroll 1
                for (u32 spin = 0; spin < SPIN_CAP; ++spin) {
                    __builtin_amdgcn_s_sleep(2);
                    asm volatile("" ::: "memory");
                    m0 = ld_unit(rs, goff); m1 = ld_unit(rs, goff + 1024);
                    if (__builtin_amdgcn_ballot_w64(!(unit_ok(m0, T) & (unit_ok(m1, T) | !has1))) == 0ull) break;
                }
            }
            float a0 = __uint_as_float(m0.x) + (has1 ? __uint_as_float(m1.x) : 0.f);
            float a1 = __uint_as_float(m0.z) + (has1 ? __uint_as_float(m1.z) : 0.f);
            a0 += dppf<ROR4>(a0); a0 += dppf<ROR8>(a0); a0 = xrow_sum(a0);
            a1 += dppf<ROR4>(a1); a1 += dppf<ROR8>(a1); a1 = xrow_sum(a1);
            const float y0 = bfr(bfr(a0) + bflo(b2w)), y1 = bfr(bfr(a1) + bfhi(b2w));
            const float g0 = bfr(y0 / (1.0f + __expf(-y0)));
            const float g1 = bfr(y1 / (1.0f + __expf(-y1)));
            if (lane < 4) *(u32x4*)(lds + L_FIN + lane * 16) = u32x4{__float_as_uint(g0), __float_as_uint(g1), xw, 0u};
            DBG(STAMP(7);)
            SB();
            perm_wo();
            pin_wa();
        }
        DBG(STAMP(9);)
        wg_barrier();
        STAMP(5);
        {
            f32x4 acc[8];
#pragma unroll
            for (int d = 0; d < 8; ++d) acc[d] = f32x4{0.f, 0.f, 0.f, 0.f};
#pragma unroll
            for (int tau = 0; tau < 2; ++tau) {
                const u32x4 of = *(const u32x4*)(lds + L_O + (n16 >> 2) * 128 + (32 * tau + 8 * g) * 2);
#pragma unroll
                for (int d = 0; d < 8; ++d) acc[d] = mfma(wa[tau][d], of, acc[d]);
            }
            publish_cols<64>(rs, WS_SP, acc, oh * 2 + okh, 16 * ons, lane, T);
        }
        asm volatile("" :: "v"(tch[0]), "v"(tch[1]), "v"(tch[2]), "v"(tch[3]));
        STAMP(6);
        FLUSH_STAMPS();
    }
}

extern "C" void kernel_launch(void* const* d_in, const int* in_sizes, int n_in,
                              void* d_out, int out_size, void* d_ws, size_t ws_size,
                              hipStream_t stream) {
    Params P;
    P.x = d_in[0]; P.k = d_in[1]; P.v = d_in[2]; P.wq = d_in[3]; P.wo = d_in[4];
    P.w1 = d_in[6]; P.b1 = d_in[7]; P.w2 = d_in[8]; P.b2 = d_in[9];
    P.out = d_out; P.ws = d_ws;
    fused_decode_kernel<<<256, 320, 0, stream>>>(P);
}
```

```cpp
#include <hip/hip_runtime.h>
#include <hip/hip_bf16.h>
#include <stdint.h>

typedef unsigned int u32;
typedef u32 u32x4 __attribute__((ext_vector_type(4)));
typedef u32 u32x2 __attribute__((ext_vector_type(2)));
typedef float f32x4 __attribute__((ext_vector_type(4)));
typedef float f32x2 __attribute__((ext_vector_type(2)));
typedef __bf16 bf16x8 __attribute__((ext_vector_type(8)));
typedef __bf16 bf16x2 __attribute__((ext_vector_type(2)));
#define GAS __attribute__((address_space(1)))
typedef GAS const u32x4 gcu32x4;
typedef GAS const char gcchar;
#define LAS __attribute__((address_space(3)))
__device__ __forceinline__ unsigned int lds_ld(const char* p) { return *(volatile LAS const unsigned int*)(LAS const char*)p; }
__device__ __forceinline__ u32x4 lds_ld4(const char* p) { return *(volatile LAS const u32x4*)(LAS const char*)p; }
__device__ __forceinline__ void lds_st(char* p, unsigned int v) { *(volatile LAS unsigned int*)(LAS char*)p = v; }

constexpr int NB = 4, DM = 512, HKV = 8, SK = 16384, DH = 128, FF = 1792;
constexpr int NMLP = 28;
constexpr u32 WS_EPOCH = 0;
constexpr u32 WS_Q  = 0x1000;
constexpr u32 WS_CNT = 0x18000;
constexpr u32 WS_PL = 0x20000;
constexpr u32 WS_MP = 0x30000;
constexpr u32 WS_PO = 0x100000;
constexpr u32 WS_SP = 0x200000;
constexpr u32 WS_END = 0x400000;
constexpr u32 SPIN_CAP = 1u << 17;
constexpr int L_Q   = 0;
constexpr int L_T   = 1024;
constexpr int L_QF  = 1040;
constexpr int L_XF  = 1044;
constexpr int L_BC  = 1048;
constexpr int L_FIN = 1088;
constexpr int L_XN  = 2048;
constexpr int L_HP  = 6144;
constexpr int L_H   = 14336;
constexpr int L_ML  = 14848;
constexpr int L_O   = 15104;
constexpr int L_PF  = 18432;
constexpr int LDS_BYTES = L_PF + 4 * 32768;

struct Params {
    const void* x; const void* k; const void* v; const void* wq; const void* wo;
    const void* w1; const void* b1; const void* w2; const void* b2;
    void* out; void* ws;
};

__device__ __forceinline__ u32x4 ldnt(gcchar* p) { return __builtin_nontemporal_load((gcu32x4*)p); }
__device__ __forceinline__ float bflo(u32 w) { return __uint_as_float(w << 16); }
__device__ __forceinline__ float bfhi(u32 w) { return __uint_as_float(w & 0xffff0000u); }
__device__ __forceinline__ u32 pack2(float lo, float hi) {
    f32x2 v = {lo, hi};
    return __builtin_bit_cast(u32, __builtin_convertvector(v, bf16x2));
}
__device__ __forceinline__ float bfr(float x) { return bflo(pack2(x, 0.f)); }
__device__ __forceinline__ f32x4 mfma(u32x4 a, u32x4 b, f32x4 c) {
    return __builtin_amdgcn_mfma_f32_16x16x32_bf16(__builtin_bit_cast(bf16x8, a), __builtin_bit_cast(bf16x8, b), c, 0, 0, 0);
}
__device__ __forceinline__ float dot2(u32 a, u32 b, float c) {
    return __builtin_amdgcn_fdot2_f32_bf16(__builtin_bit_cast(bf16x2, a), __builtin_bit_cast(bf16x2, b), c, false);
}
template <int CTRL> __device__ __forceinline__ u32 dppu(u32 x) { return (u32)__builtin_amdgcn_mov_dpp((int)x, CTRL, 0xf, 0xf, true); }
template <int CTRL> __device__ __forceinline__ float dppf(float x) { return __uint_as_float(dppu<CTRL>(__float_as_uint(x))); }
constexpr int XOR1 = 0xB1, XOR2 = 0x4E, HMIR = 0x141, ROR4 = 0x124, ROR8 = 0x128, EVEN = 0xA0, ODD = 0xF5;
__device__ __forceinline__ float xrow_max(float x) {
    auto s = __builtin_amdgcn_permlane16_swap(__float_as_uint(x), __float_as_uint(x), false, false);
    x = fmaxf(__uint_as_float(s[0]), __uint_as_float(s[1]));
    auto t = __builtin_amdgcn_permlane32_swap(__float_as_uint(x), __float_as_uint(x), false, false);
    return fmaxf(__uint_as_float(t[0]), __uint_as_float(t[1]));
}
__device__ __forceinline__ float xrow_sum(float x) {
    auto s = __builtin_amdgcn_permlane16_swap(__float_as_uint(x), __float_as_uint(x), false, false);
    x = __uint_as_float(s[0]) + __uint_as_float(s[1]);
    auto t = __builtin_amdgcn_permlane32_swap(__float_as_uint(x), __float_as_uint(x), false, false);
    return __uint_as_float(t[0]) + __uint_as_float(t[1]);
}
__device__ __forceinline__ float x32_max(float x) {
    auto t = __builtin_amdgcn_permlane32_swap(__float_as_uint(x), __float_as_uint(x), false, false);
    return fmaxf(__uint_as_float(t[0]), __uint_as_float(t[1]));
}
__device__ __forceinline__ float x32_sum(float x) {
    auto t = __builtin_amdgcn_permlane32_swap(__float_as_uint(x), __float_as_uint(x), false, false);
    return __uint_as_float(t[0]) + __uint_as_float(t[1]);
}
__device__ __forceinline__ float wave_sum(float x) {
    x += dppf<XOR1>(x); x += dppf<XOR2>(x); x += dppf<ROR4>(x); x += dppf<ROR8>(x);
    return xrow_sum(x);
}
__device__ __forceinline__ void wg_barrier() { asm volatile("s_waitcnt lgkmcnt(0)\n\ts_barrier" ::: "memory"); }
__device__ __forceinline__ void raw_barrier() { asm volatile("s_barrier" ::: "memory"); }
__device__ __forceinline__ void quad_barrier(char* cnt, u32 target, int lane) {
    asm volatile("" ::: "memory");
    if (lane == 0) __hip_atomic_fetch_add((LAS unsigned int*)(LAS char*)cnt, 1u, __ATOMIC_RELAXED, __HIP_MEMORY_SCOPE_WORKGROUP);
#pragma unroll 1
    for (u32 spin = 0; spin < SPIN_CAP; ++spin) {
        asm volatile("" ::: "memory");
        if (lds_ld(cnt) >= target) break;
    }
    asm volatile("" ::: "memory");
}
__device__ __forceinline__ void wait_flag(const char* f) {
#pragma unroll 1
    for (u32 spin = 0; spin < SPIN_CAP; ++spin) {
        asm volatile("" ::: "memory");
        if (lds_ld(f) != 0u) break;
        __builtin_amdgcn_s_sleep(1);
    }
    asm volatile("" ::: "memory");
}
constexpr u32 OOB = 0x40000000u;

__device__ __forceinline__ u32x4 mk_unit(u32 v0, u32 v1, u32 T) { u32x4 u = {v0, T ^ v1, v1, T ^ v0}; return u; }
__device__ __forceinline__ bool unit_ok(u32x4 u, u32 T) { return ((u.y ^ u.z) == T) & ((u.w ^ u.x) == T); }
__device__ __forceinline__ void st_unit(__amdgpu_buffer_rsrc_t rs, u32 off, u32x4 u) { __builtin_amdgcn_raw_buffer_store_b128(u, rs, off, 0, 16); }
__device__ __forceinline__ u32x4 ld_unit(__amdgpu_buffer_rsrc_t rs, u32 off) { return __builtin_amdgcn_raw_buffer_load_b128(rs, off, 0, 16); }

template <int DD> __device__ __forceinline__ u32x4 tfrag(const u32x4* v) {
    constexpr u32 sel = (DD & 1) ? 0x07060302u : 0x05040100u;
    u32x4 a;
    a.x = __builtin_amdgcn_perm(v[1][DD >> 1], v[0][DD >> 1], sel);
    a.y = __builtin_amdgcn_perm(v[3][DD >> 1], v[2][DD >> 1], sel);
    a.z = __builtin_amdgcn_perm(v[5][DD >> 1], v[4][DD >> 1], sel);
    a.w = __builtin_amdgcn_perm(v[7][DD >> 1], v[6][DD >> 1], sel);
    return a;
}
__device__ __forceinline__ void pv_tile(const u32x4* v, u32x4 p, f32x4 (&acc)[8]) {
    acc[0] = mfma(tfrag<0>(v), p, acc[0]); acc[1] = mfma(tfrag<1>(v), p, acc[1]);
    acc[2] = mfma(tfrag<2>(v), p, acc[2]); acc[3] = mfma(tfrag<3>(v), p, acc[3]);
    acc[4] = mfma(tfrag<4>(v), p, acc[4]); acc[5] = mfma(tfrag<5>(v), p, acc[5]);
    acc[6] = mfma(tfrag<6>(v), p, acc[6]); acc[7] = mfma(tfrag<7>(v), p, acc[7]);
}
#define MPAUX 16
template <int NSLOT, int AUX = 16>
__device__ __forceinline__ void publish_cols(__amdgpu_buffer_rsrc_t rs, u32 base, const f32x4 (&acc)[8], int slot, int ng0, int lane, u32 T) {
    const int g = lane >> 4, b = (lane >> 2) & 3, rho = lane & 3;
    const u32 off0 = base + (u32)(((b * 64 + ng0 + 4 * g) * NSLOT + slot) * 64 + rho * 16);
#pragma unroll
    for (int r = 0; r < 4; ++r) {
        float a0 = (rho & 2) ? acc[4][r] : acc[0][r], a1 = (rho & 2) ? acc[6][r] : acc[2][r];
        float c0 = (rho & 2) ? acc[5][r] : acc[1][r], c1 = (rho & 2) ? acc[7][r] : acc[3][r];
        float v0 = (rho & 1) ? a1 : a0, v1 = (rho & 1) ? c1 : c0;
        __builtin_amdgcn_raw_buffer_store_b128(mk_unit(__float_as_uint(v0), __float_as_uint(v1), T), rs, off0 + (u32)(r * NSLOT * 64), 0, AUX);
    }
}

#define STAMP(i) do { } while (0)
#define STAMPV(i, v) do { } while (0)
#define FLUSH_STAMPS() do { } while (0)
#define DBG(x)

struct Ring { u32x4 r[16]; };
typedef __amdgpu_buffer_rsrc_t rsrc_t;
#define DMAMOD "nt"
constexpr int NT = 2;
__device__ __forceinline__ u32x4 ldb_nt(rsrc_t rs, u32 voff, u32 soff) { return __builtin_amdgcn_raw_buffer_load_b128(rs, voff, soff, NT); }
constexpr u32 TSTR = 4 * 8192;
__device__ __forceinline__ constexpr u32 kimm(int i) { return (u32)((i >> 2) * 4096 + (i & 3) * 64); }
__device__ __forceinline__ void issue_k(Ring& R, rsrc_t ks, u32 koff, u32 toff, u32 toff2) {
#pragma unroll
    for (int i = 0; i < 8; ++i) R.r[i] = ldb_nt(ks, koff + (i & 3) * 64, i < 4 ? toff : toff2);
}
__device__ __forceinline__ void issue_v(Ring& R, rsrc_t vs, u32 voff, u32 toff, u32 toff2) {
#pragma unroll
    for (int i = 0; i < 8; ++i) R.r[8 + i] = ldb_nt(vs, voff + (i & 3) * 1024, i < 4 ? toff : toff2);
}
__device__ __forceinline__ void issue_k(Ring& R, rsrc_t ks, u32 koff, u32 toff) { issue_k(R, ks, koff, toff, toff + 4096); }
__device__ __forceinline__ void issue_v(Ring& R, rsrc_t vs, u32 voff, u32 toff) { issue_v(R, vs, voff, toff, toff + 4096); }
__device__ __forceinline__ void dma16(u32 ldst, u32 voff, u32x4 srd, u32 soff) {
    u32 keep;
    asm volatile("s_mov_b32 %0, m0\n\ts_mov_b32 m0, %1\n\ts_nop 0\n\tbuffer_load_dwordx4 %2, %3, %4 offen " DMAMOD " lds\n\ts_mov_b32 m0, %0"
                 : "=&s"(keep) : "s"(ldst), "v"(voff), "s"(srd), "s"(soff) : "memory");
}
__device__ __forceinline__ u32x4 mk_srd(const void* base, u32 bytes) {
    const uint64_t a = (uint64_t)base;
    u32x4 d = {(u32)__builtin_amdgcn_readfirstlane((u32)a), (u32)__builtin_amdgcn_readfirstlane((u32)(a >> 32) & 0xffffu), bytes, 0x00020000u};
    return d;
}
#define SB() __builtin_amdgcn_sched_barrier(0)
#define QPRE 48
#define QINT 16
#define PACE_N 7
#define STR2(x) #x
#define STR(x) STR2(x)
#define PACE() asm volatile("s_waitcnt vmcnt(" STR(PACE_N) ")" ::: "memory")
#define LPACE_N 7
#define LPACE() asm volatile("s_waitcnt vmcnt(" STR(LPACE_N) ")" ::: "memory")
#define MPACE_N 15
#define PACEC(flag) asm volatile("s_cmp_lg_u32 %0, 0\n\ts_cbranch_scc1 1f\n\ts_waitcnt vmcnt(" STR(PACE_N) ")\n1:\n\ts_waitcnt vmcnt(" STR(MPACE_N) ")" :: "s"(flag) : "scc", "memory")
#define PB 1
#define SVCPRIO 3

#define GAUX 16
#define DQ 32
#define NP 528
#define QR0 2
#define QR1 5
#define NL 1
#define QPRE_P 100
#define QPRE_X 60
#define XAUX 0
#define QVP 63
#define TOPN 4
#define EARLYP 1
#define GD 1
#define GK 1
#define GATE_N 6
#define PD 1
#define PK 1
#define FINW 0
#define GLATE 1
#define XEARLY 1
#define TOUCH 14
#define TOUCHQ 0
#define TOUCHP 2
#define ORD2 6
#define WLATE 1
#define WPIN 1
#define CBX 0
#define DYN 0
#define SP 15
#define DQT 1
#define DMT 2
#define PTOUCH 1
constexpr int NP2 = ((4096 + 2 * DQ) / 8 + 3) / 4 * 4;
static_assert(NP % 4 == 0 && DQ % 4 == 0 && 4096 - 7 * NP + 2 * DQ > 160, "key split");
struct AttnState { f32x4 acc[8]; float m, l; };

__device__ __forceinline__ void qk_tile(const u32x4* k, const u32x4 (&qf)[4], f32x4& s0, f32x4& s1) {
    s0 = f32x4{0.f, 0.f, 0.f, 0.f}; s1 = f32x4{0.f, 0.f, 0.f, 0.f};
#pragma unroll
    for (int st = 0; st < 4; ++st) { s0 = mfma(k[st], qf[st], s0); s1 = mfma(k[4 + st], qf[st], s1); }
}
template <bool MASKED = false>
__device__ __forceinline__ u32x4 softmax_step(const f32x4 s0, const f32x4 s1, AttnState& S, int nj = 8) {
    constexpr float SCL = 0.08838834764831845f, LOG2E = 1.4426950408889634f;
    float z[8];
#pragma unroll
    for (int j = 0; j < 2; ++j) {
        const u32 a = pack2(s0[2 * j], s0[2 * j + 1]), c = pack2(s1[2 * j], s1[2 * j + 1]);
        const u32 a2 = pack2(bflo(a) * SCL, bfhi(a) * SCL), c2 = pack2(bflo(c) * SCL, bfhi(c) * SCL);
        z[2 * j] = bflo(a2); z[2 * j + 1] = bfhi(a2); z[4 + 2 * j] = bflo(c2); z[4 + 2 * j + 1] = bfhi(c2);
    }
    if (MASKED) {
#pragma unroll
        for (int j = 0; j < 8; ++j) z[j] = (j < nj) ? z[j] : -INFINITY;
    }
    float mx = fmaxf(fmaxf(fmaxf(z[0], z[1]), fmaxf(z[2], z[3])), fmaxf(fmaxf(z[4], z[5]), fmaxf(z[6], z[7])));
    mx = xrow_max(mx) * LOG2E;
    if (__builtin_amdgcn_ballot_w64(mx > S.m) != 0ull) {
        const float mn = fmaxf(S.m, mx);
        const float al = __builtin_amdgcn_exp2f(S.m - mn);
        S.m = mn; S.l *= al;
        f32x2 al2 = {al, al};
        asm volatile("" : "+v"(al2));
        const f32x4 al4 = {al2.x, al2.y, al2.x, al2.y};
#pragma unroll
        for (int d = 0; d < 8; ++d) S.acc[d] *= al4;
    }
    float p[8];
#pragma unroll
    for (int j = 0; j < 8; ++j) p[j] = __builtin_amdgcn_exp2f(__builtin_fmaf(z[j], LOG2E, -S.m));
    S.l += ((p[0] + p[1]) + (p[2] + p[3])) + ((p[4] + p[5]) + (p[6] + p[7]));
    u32x4 pf = {pack2(p[0], p[1]), pack2(p[2], p[3]), pack2(p[4], p[5]), pack2(p[6], p[7])};
    return pf;
}
__device__ __forceinline__ void proc_tile(Ring& R, AttnState& S, const u32x4 (&qf)[4], rsrc_t ks, rsrc_t vs, u32 koff, u32 voff, u32 toff) {
    f32x4 s0, s1;
    qk_tile(&R.r[0], qf, s0, s1);
    SB();
    issue_k(R, ks, koff, toff);
    SB();
    const u32x4 pf = softmax_step(s0, s1, S);
    pv_tile(&R.r[8], pf, S.acc);
    SB();
    issue_v(R, vs, voff, toff);
    SB();
}
__device__ __forceinline__ u32 proc_tile_c(Ring& R, AttnState& S, const u32x4 (&qf)[4], rsrc_t ks, rsrc_t vs, u32 koff, u32 voff, u32 toff, rsrc_t cs, bool dyn) {
    f32x4 s0, s1;
    qk_tile(&R.r[0], qf, s0, s1);
    SB();
    issue_k(R, ks, koff, toff);
    SB();
    const u32x4 pf = softmax_step(s0, s1, S);
    asm volatile("" : "+v"(S.l));
    pv_tile(&R.r[8], pf, S.acc);
    SB();
    u32 claim = 0;
    if (dyn) claim = (u32)__builtin_amdgcn_raw_ptr_buffer_atomic_add_i32(1, cs, (int)voff, 0, 16);
    SB();
    issue_v(R, vs, voff, toff);
    SB();
    return claim;
}
__device__ __forceinline__ void proc_last(Ring& R, AttnState& S, const u32x4 (&qf)[4]) {
    f32x4 s0, s1;
    qk_tile(&R.r[0], qf, s0, s1);
    const u32x4 pf = softmax_step(s0, s1, S);
    pv_tile(&R.r[8], pf, S.acc);
}
__device__ __forceinline__ void proc_part(Ring& R, AttnState& S, const u32x4 (&qf)[4], int nj) {
    f32x4 s0, s1;
    qk_tile(&R.r[0], qf, s0, s1);
    const u32x4 pf = softmax_step<true>(s0, s1, S, nj);
    pv_tile(&R.r[8], pf, S.acc);
}
__device__ __forceinline__ void proc_lds(const char* sl, AttnState& S, const u32x4 (&qf)[4]) {
    u32x4 c[8];
#pragma unroll
    for (int i = 0; i < 8; ++i) c[i] = *(const u32x4*)(sl + i * 1024);
    f32x4 s0, s1;
    qk_tile(c, qf, s0, s1);
    SB();
#pragma unroll
    for (int i = 0; i < 8; ++i) c[i] = *(const u32x4*)(sl + 8192 + i * 1024);
    const u32x4 pf = softmax_step(s0, s1, S);
    pv_tile(c, pf, S.acc);
}

__global__ void __launch_bounds__(320, 2) fused_decode_kernel(Params P) {
    __shared__ __attribute__((aligned(16))) char lds[LDS_BYTES];
    const int tid = threadIdx.x, lane = tid & 63;
    const int wave = __builtin_amdgcn_readfirstlane(tid >> 6);
    const int j = blockIdx.x;
    DBG(unsigned long long ts[NSTAMP]; for (int z = 0; z < NSTAMP; ++z) ts[z] = 0;)
    STAMP(0);
    const int grp = j >> 3, split = j & 7;
    const int b = grp >> 3, ahkv = grp & 7;
    const int rank = (split - ahkv - 1) & 7;
    const bool grp_mlp = grp < NMLP;
    int rk = rank;
    asm volatile("" : "+s"(rk));
    int f_q = ((1 << QR0 | 1 << QR1) >> rk) & 1;
    int f_m = (((grp - NMLP) >> 24 & 0x80) >> rk) & 1;
    int f_q1 = (rk >> 2) & f_q;
    static_assert(QR0 < 4 && QR1 >= 4, "rank bit 2 tells the two q slices apart");
    asm volatile("" : "+s"(f_q), "+s"(f_m), "+s"(f_q1));
    int f_pre = f_q | f_m, f_svc = wave >> 2;
    asm volatile("" : "+s"(f_pre), "+s"(f_svc));
    const bool is_q = f_q != 0, is_mlp = f_m != 0, pre = f_pre != 0;
    const int g = lane >> 4, n16 = lane & 15;
    const int n_plain = grp_mlp ? NP : NP2;
    const int nkeys = rank == 7 ? 4096 + 2 * DQ - 7 * n_plain : n_plain - f_q * DQ;
    const int nfull = (nkeys - 1) >> 5, nj = (nkeys - 32 * nfull) >> 2;
    const u32 w_start = (u32)(rank * n_plain - (rank > QR0 ? DQ : 0) - (rank > QR1 ? DQ : 0)) * 1024u;
    const size_t tb = (size_t)grp * (SK * DH * 2);
    constexpr u32 trange = SK * DH * 2;
    const u32 t0 = w_start + (u32)wave * 8192;
    const u32 koff = (u32)((4 * (n16 & 3) + (n16 >> 2)) * 256 + g * 16);
    const u32 voff = (u32)(lane * 16);
    Ring A;
    u32 epoch = 0;
    {
        const u32 mq = 0u - (u32)f_q, mp = 0u - (u32)f_pre, ms = 0u - (u32)f_svc;
        auto sel = [](u32 m, u32 x, u32 y) { return y ^ ((x ^ y) & m); };
        const bool svc = f_svc != 0;
        const u32 wstr = sel(mq, 8192u, (u32)(FF * 2));
        const u32 wcol = sel(mq, (u32)(ahkv * 1024 + ((2 * b + f_q1) ^ CBX) * 128), (u32)(grp * 128));
        const u32 wb = (u32)(128 * wave) * wstr + wcol;
        const u32 nb = sel(mp, 512u * wstr, trange), na = sel(ms, 4096u, nb);
        const u32 wv = (u32)(32 * (n16 >> 3) + 8 * g) * wstr + (u32)((n16 & 7) * 16);
        const u32 va = sel(ms, (u32)(g * 1024 + n16 * 16), sel(mp, wv, koff));
        const u32 vb = sel(mp, wv, voff);
        const u32 sa = sel(ms, sel(mp, 0u, OOB), sel(mp, wb, t0)), sb = sel(mp, wb + 64u * wstr, t0);
        const u32 a1 = sel(ms, 256u, sel(mp, wstr, 64u)), a2 = sel(mp, 4u * wstr, 4096u);
        const u32 b1 = sel(mp, wstr, 1024u);
        SB();
        const uint64_t Mq = (uint64_t)(int64_t)(int)mq, Mp = (uint64_t)(int64_t)(int)mp, Ms = (uint64_t)(int64_t)(int)ms;
        auto sel64 = [](uint64_t m, uint64_t x, uint64_t y) { return y ^ ((x ^ y) & m); };
        const uint64_t wp = sel64(Mq, (uint64_t)P.wq, (uint64_t)P.w1);
        const uint64_t pa = sel64(Ms, (uint64_t)P.x, sel64(Mp, wp, (uint64_t)P.k + tb));
        const uint64_t pb = sel64(Mp, wp, (uint64_t)P.v + tb);
        const rsrc_t ra = __builtin_amdgcn_make_buffer_rsrc((void*)pa, 0, na, 0x00020000);
        const rsrc_t rb = __builtin_amdgcn_make_buffer_rsrc((void*)pb, 0, nb, 0x00020000);
        if (lane < 3) lds_st(lds + L_QF + lane * 4, 0u);
#pragma unroll
        for (int i = 0; i < 4; ++i) A.r[i] = __builtin_amdgcn_raw_buffer_load_b128(ra, va, sa + (u32)i * a1, XAUX);
        epoch = __builtin_amdgcn_raw_buffer_load_b32(__builtin_amdgcn_make_buffer_rsrc(P.ws, 0, 16, 0x00020000), 0, ~ms & OOB, 0);
        SB(); wg_barrier(); SB();
        if (!svc) {
#pragma unroll
            for (int i = 4; i < 16; ++i)
                A.r[i] = i < 8 ? ldb_nt(ra, va, sa + (u32)(i & 3) * a1 + a2) : ldb_nt(rb, vb, sb + (u32)(i & 3) * b1 + (u32)((i - 8) >> 2) * a2);
        }
        SB();
    }
    STAMP(15);
    int wave2 = wave;
    asm volatile("" : "+s"(wave2));
    const int hkv = j & 7, pidx = j >> 3;
    const __amdgpu_buffer_rsrc_t rs = __builtin_amdgcn_make_buffer_rsrc(P.ws, 0, WS_END, 0x00020000);
    const int oh = hkv * 4 + (pidx & 3), okh = (pidx >> 2) & 1, ons = pidx >> 3;

    if (wave2 < 4) {
        auto tof = [&](u32 i) { return t0 + i * TSTR; };
        const u32 m_off = w_start + (u32)nfull * TSTR + (u32)(wave * nj) * 1024u, m_end = m_off + (u32)nj * 1024u;
        const rsrc_t ks = __builtin_amdgcn_make_buffer_rsrc((char*)P.k + tb, 0, trange, 0x00020000);
        const rsrc_t vs = __builtin_amdgcn_make_buffer_rsrc((char*)P.v + tb, 0, trange, 0x00020000);
        char* const pf_base = lds + L_PF + wave * 32768;
        const u32x4 ksd = mk_srd((const char*)P.k + tb, trange), vsd = mk_srd((const char*)P.v + tb, trange);
        const u32 lb = (u32)__builtin_amdgcn_readfirstlane((u32)(uintptr_t)(__attribute__((address_space(3))) char*)pf_base);
        Ring B;
        AttnState S;
        const u32 tB = is_q ? tof(0) : tof(1);
        if (is_mlp) {
            const rsrc_t w2s = __builtin_amdgcn_make_buffer_rsrc((void*)P.w2, 0, FF * DM * 2, 0x00020000);
            const u32 w2b = (u32)(grp * 64 * 1024 + 256 * wave);
            const u32 w2off = (u32)(8 * g * 1024 + n16 * 16);
#pragma unroll
            for (int i = 0; i < 16; ++i) B.r[i] = ldb_nt(w2s, w2off, w2b + ((i >> 3) * 32 + (i & 7)) * 1024);
        } else {
            if (is_q) asm volatile("s_waitcnt vmcnt(" STR(QVP) ")" ::: "memory");
#pragma unroll
            for (int i = 0; i < 8; ++i) { if (!is_q && i % PB == 0) PACE(); B.r[8 + i] = ldb_nt(vs, voff + (i & 3) * 1024, tB + (i >> 2) * 4096); }
        }
        if (pre) {
            const float b1v = is_mlp ? bflo((u32)((const GAS unsigned short*)P.b1)[grp * 64 + lane]) : 0.f;
            wait_flag(lds + L_XF);
#pragma unroll
            for (int d = 0; d < 8; ++d) S.acc[d] = f32x4{0.f, 0.f, 0.f, 0.f};
#pragma unroll
            for (int tau = 0; tau < 2; ++tau) {
                const u32x4 xf = *(const u32x4*)(lds + L_XN + (n16 & 3) * 1024 + (128 * wave + 64 * tau + 32 * ((n16 >> 2) & 1) + 8 * g) * 2);
                pv_tile(&A.r[8 * tau], xf, S.acc);
            }
            if (n16 < 8 && (g >> 1) == (n16 >> 2)) {
                float* hp = (float*)(lds + L_HP) + ((wave * 2 + (n16 >> 2)) * 4 + (n16 & 3)) * 64 + 32 * (g & 1);
#pragma unroll
                for (int r = 0; r < 4; ++r) {
                    *(f32x4*)(hp + 8 * r) = f32x4{S.acc[0][r], S.acc[1][r], S.acc[2][r], S.acc[3][r]};
                    *(f32x4*)(hp + 8 * r + 4) = f32x4{S.acc[4][r], S.acc[5][r], S.acc[6][r], S.acc[7][r]};
                }
            }
            DBG(STAMP(6);)
            quad_barrier(lds + L_BC, 4u, lane);
            float sum = 0.f;
            {
                const float* hp = (const float*)(lds + L_HP) + wave * 64 + lane;
#pragma unroll
                for (int i = 0; i < 8; ++i) sum += hp[i * 256];
            }
            const u32 T = *(const u32*)(lds + L_T);
            if (is_q) {
                const u32 w01 = pack2(sum, dppf<XOR1>(sum));
                const u32 w23 = dppu<XOR2>(w01);
                if ((lane & 3) == 0)
                    st_unit(rs, WS_Q + (u32)((wave * 8 + ahkv) * 2048 + (16 * ((2 * b + f_q1) ^ CBX) + (lane >> 2)) * 16), mk_unit(w01, w23, T));
                DBG(STAMP(7);)
            } else {
                float h = bfr(bfr(sum) + b1v);
                h = h > 0.f ? h : 0.f;
                ((unsigned short*)(lds + L_H))[wave * 64 + lane] = (unsigned short)pack2(h * h, 0.f);
            }
            SB();
            const u32 tA = is_q ? tof(1) : tof(0);
            issue_k(A, ks, koff, tA);
            issue_v(A, vs, voff, tA);
            SB();
            if (is_mlp) {
                quad_barrier(lds + L_BC, 8u, lane);
#pragma unroll
                for (int d = 0; d < 8; ++d) S.acc[d] = f32x4{0.f, 0.f, 0.f, 0.f};
#pragma unroll
                for (int tau = 0; tau < 2; ++tau) {
                    const u32x4 hf = *(const u32x4*)(lds + L_H + (n16 >> 2) * 128 + (32 * tau + 8 * g) * 2);
                    pv_tile(&B.r[8 * tau], hf, S.acc);
                }
                publish_cols<32, MPAUX>(rs, WS_MP, S.acc, grp, 16 * wave, lane, T);
                DBG(STAMP(7);)
                SB();
                issue_v(B, vs, voff, tB);
            }
        }
        SB();
#pragma unroll
        for (int i = 0; i < 8; ++i) { if (i % PB == 0) PACE(); B.r[i] = ldb_nt(ks, koff + (i & 3) * 64, tB + (i >> 2) * 4096); }
        STAMP(11);
#pragma unroll
        for (int t = 0; t < NL; ++t) {
#pragma unroll
            for (int i = 0; i < 8; ++i) { if (i % PB == 0) LPACE(); dma16(lb + t * 16384 + i * 1024, koff, ksd, tof(2 + t) + kimm(i)); }
#pragma unroll
            for (int i = 0; i < 8; ++i) { if (i % PB == 0) LPACE(); dma16(lb + t * 16384 + 8192 + i * 1024, voff, vsd, tof(2 + t) + i * 1024); }
        }
        STAMP(1);
        wait_flag(lds + L_QF);
        STAMP(2);
        u32x4 qf[4];
#pragma unroll
        for (int st = 0; st < 4; ++st) qf[st] = *(const u32x4*)(lds + L_Q + (n16 & 3) * 256 + st * 64 + g * 16);
        const u32 T = *(const u32*)(lds + L_T);
#pragma unroll
        for (int d = 0; d < 8; ++d) S.acc[d] = f32x4{0.f, 0.f, 0.f, 0.f};
        S.m = -INFINITY; S.l = 0.f;
        proc_tile(A, S, qf, ks, vs, koff, voff, tof(2 + NL));
        DBG(STAMP(12);)
        proc_last(B, S, qf);
#pragma unroll
        for (int t = 0; t < NL; ++t) {
            asm volatile("s_waitcnt vmcnt(16)" ::: "memory");
            proc_lds(pf_base + t * 16384 + lane * 16, S, qf);
        }
        DBG(STAMP(13);)
#pragma unroll 1
        for (int i = 2 + NL; i < nfull; ++i) {
            const bool last = i + 1 == nfull;
            const u32 nrec = last ? m_end : trange;
            const rsrc_t kc = __builtin_amdgcn_make_buffer_rsrc((char*)P.k + tb, 0, nrec, 0x00020000);
            const rsrc_t vc = __builtin_amdgcn_make_buffer_rsrc((char*)P.v + tb, 0, nrec, 0x00020000);
            proc_tile(A, S, qf, kc, vc, koff, voff, last ? m_off : tof((u32)(i + 1)));
            DBG(if (i == 5) STAMP(8); if (i == 9) STAMP(9); if (i == 13) STAMP(10); )
        }
        proc_part(A, S, qf, nj);
        STAMP(3);
        const int half = lane >> 5;
        const u32 blk0 = (u32)((wave * 32 + oh) * 8);
        const u32 ooff = WS_PO + (blk0 + half) * 1024 + (u32)((32 * okh + (lane & 31)) * 16);
        const u32 loff = lane < 8 ? WS_PL + (blk0 + lane) * 16 : OOB;
        struct Pass { u32x4 l, t[4]; };
        Pass p0, p1;
        u32 want = 0xfu, have = 0;
        u32 lseen = 0;
        u32 lsev = 0;
        f32x2 uo[4];
        auto g_issue = [&](Pass& p) {
            SB();
            p.l = ld_unit(rs, loff);
#pragma unroll
            for (int r = 0; r < 4; ++r) p.t[r] = ld_unit(rs, ((want >> r) & 1u) ? ooff + r * 2048 : OOB);
            SB();
        };
        auto g_eval = [&](const Pass& p) -> bool {
            SB();
            const bool lok = unit_ok(p.l, T);
            lsev = lok ? p.l.x : lsev;
            lseen |= (u32)__builtin_amdgcn_ballot_w64(lok) & 0xffu;
#pragma unroll
            for (int r = 0; r < 4; ++r) {
                const bool ok = unit_ok(p.t[r], T);
                uo[r].x = ok ? __uint_as_float(p.t[r].x) : uo[r].x; uo[r].y = ok ? __uint_as_float(p.t[r].z) : uo[r].y;
                have |= ok ? 1u << r : 0u;
            }
            const u32 gate = (__builtin_popcount(lseen) >= GATE_N) ? 0xffu : lseen;
            const u32 gl = gate >> half;
            want = ((gl & 1u) | ((gl >> 1) & 2u) | ((gl >> 2) & 4u) | ((gl >> 3) & 8u)) & ~have;
            asm volatile("" : "+v"(uo[0].x), "+v"(uo[0].y), "+v"(uo[1].x), "+v"(uo[1].y), "+v"(uo[2].x), "+v"(uo[2].y), "+v"(uo[3].x), "+v"(uo[3].y),
                              "+v"(lsev), "+v"(have), "+v"(want));
            return lseen == 0xffu && __builtin_amdgcn_ballot_w64(have != 0xfu) == 0ull;
        };
#pragma unroll
        for (int r = 0; r < 4; ++r) uo[r] = f32x2{0.f, 0.f};
        const float lw = xrow_sum(S.l);
        if (n16 < 4) {
            float* mo = (float*)pf_base + n16 * 144 + g * 36;
#pragma unroll
            for (int d = 0; d < 8; ++d) *(f32x4*)(mo + 4 * d) = S.acc[d];
            if (g == 0) *(f32x2*)(lds + L_ML + (wave * 4 + n16) * 8) = f32x2{S.m, lw};
        }
        SB();
        g_issue(p0);
        SB();
        wg_barrier();
        {
            f32x2 ml[4], ov[4];
#pragma unroll
            for (int w = 0; w < 4; ++w) {
                ml[w] = *(const f32x2*)(lds + L_ML + (w * 4 + wave) * 8);
                const float* q = (const float*)(lds + L_PF + w * 32768) + wave * 144 + (lane >> 4) * 36 + 8 * (lane & 3) + ((lane & 15) >> 2);
                ov[w] = f32x2{q[0], q[4]};
            }
            const float M = fmaxf(fmaxf(ml[0].x, ml[1].x), fmaxf(ml[2].x, ml[3].x));
            float L = 0.f, o0 = 0.f, o1 = 0.f;
#pragma unroll
            for (int w = 0; w < 4; ++w) {
                const float a = __builtin_amdgcn_exp2f(ml[w].x - M);
                L += a * ml[w].y; o0 += a * ov[w].x; o1 += a * ov[w].y;
            }
            const float inv = __builtin_amdgcn_rcpf(L);
            const u32 blk = (u32)((b * 32 + ahkv * 4 + wave) * 8 + split);
            st_unit(rs, WS_PO + blk * 1024 + lane * 16, mk_unit(__float_as_uint(o0 * inv), __float_as_uint(o1 * inv), T));
            if (lane == 0) {
                const float lse = M + __builtin_amdgcn_logf(L);
                st_unit(rs, WS_PL + blk * 16, mk_unit(__float_as_uint(lse), 0u, T));
            }
        }
        STAMP(4);
        {
            SB();
#pragma unroll 1
            for (u32 spin = 0; spin < SPIN_CAP; ++spin) {
                asm volatile("" ::: "memory");
                if (g_eval(p0)) { STAMPV(14, 1000000ull + spin); break; }
                __builtin_amdgcn_s_sleep(GK);
                g_issue(p0);
            }
            const float lse = __uint_as_float(lsev);
            float M = fmaxf(lse, dppf<XOR1>(lse)); M = fmaxf(M, dppf<XOR2>(M)); M = fmaxf(M, dppf<HMIR>(M));
            const float al = __builtin_amdgcn_exp2f(lse - M);
            float W = al + dppf<XOR1>(al); W += dppf<XOR2>(W); W += dppf<HMIR>(W);
            const float inv = __builtin_amdgcn_rcpf(W);
            const u32 aw = __float_as_uint(al * inv);
            float aa[4];
#pragma unroll
            for (int r = 0; r < 4; ++r) {
                const u32 e = __builtin_amdgcn_readlane(aw, 2 * r), o = __builtin_amdgcn_readlane(aw, 2 * r + 1);
                aa[r] = __uint_as_float(half ? o : e);
            }
            const float a0 = aa[0], a1 = aa[1], a2 = aa[2], a3 = aa[3];
            float o0 = (a0 * uo[0].x + a1 * uo[1].x) + (a2 * uo[2].x + a3 * uo[3].x);
            float o1 = (a0 * uo[0].y + a1 * uo[1].y) + (a2 * uo[2].y + a3 * uo[3].y);
            o0 = x32_sum(o0); o1 = x32_sum(o1);
            if (lane < 32) *(u32*)(lds + L_O + wave * 128 + lane * 4) = pack2(o0, o1);
        }
        STAMP(5);
        wg_barrier();
        if (wave == FINW) {
            const int fb = j >> 6, fng = j & 63;
            const int fu = lane & 3, fi = lane >> 2;
            const u32 off = WS_SP + (u32)(((fb * 64 + fng) * 64 + fi) * 64 + fu * 16);
            f32x2 u[4];
#pragma unroll
            for (int r = 0; r < 4; ++r) u[r] = f32x2{0.f, 0.f};
            u32 hv = 0;
            auto f_issue = [&](u32x4 (&t)[4]) {
                SB();
#pragma unroll
                for (int r = 0; r < 4; ++r) t[r] = ld_unit(rs, ((hv >> r) & 1u) ? OOB : off + r * 1024);
                SB();
            };
            auto f_eval = [&](const u32x4 (&t)[4]) -> bool {
                SB();
#pragma unroll
                for (int r = 0; r < 4; ++r) {
                    const bool ok = unit_ok(t[r], T);
                    u[r].x = ok ? __uint_as_float(t[r].x) : u[r].x; u[r].y = ok ? __uint_as_float(t[r].z) : u[r].y;
                    hv |= ok ? 1u << r : 0u;
                }
                asm volatile("" : "+v"(u[0].x), "+v"(u[0].y), "+v"(u[1].x), "+v"(u[1].y), "+v"(u[2].x), "+v"(u[2].y), "+v"(u[3].x), "+v"(u[3].y), "+v"(hv));
                return __builtin_amdgcn_ballot_w64(hv != 0xfu) == 0ull;
            };
            const u32x4 fin = *(const u32x4*)(lds + L_FIN + fu * 16);
            u32x4 t0[4], t1[4], t2[4];
            f_issue(t0);
#pragma unroll 1
            for (u32 spin = 0; spin < SPIN_CAP; ++spin) {
                asm volatile("" ::: "memory");
                if (f_eval(t0)) break;
                f_issue(t0);
                __builtin_amdgcn_s_sleep(PK);
            }
            float a0 = (u[0].x + u[1].x) + (u[2].x + u[3].x);
            float a1 = (u[0].y + u[1].y) + (u[2].y + u[3].y);
            a0 += dppf<ROR4>(a0); a0 += dppf<ROR8>(a0); a0 = xrow_sum(a0);
            a1 += dppf<ROR4>(a1); a1 += dppf<ROR8>(a1); a1 = xrow_sum(a1);
            const float r0 = bflo(fin.z) + bfr(bfr(a0) * __uint_as_float(fin.x)), r1 = bfhi(fin.z) + bfr(bfr(a1) * __uint_as_float(fin.y));
            if (lane < 4) *(GAS u32*)((GAS char*)P.out + fb * 1024 + fng * 16 + fu * 4) = pack2(r0, r1);
            if (j == 0 && lane == 0) *(GAS u32*)((GAS char*)P.ws + WS_EPOCH) = T;
            DBG(STAMP(14);)
        }
        FLUSH_STAMPS();
    } else {
        __builtin_amdgcn_s_setprio(SVCPRIO);
        u32 tch[4] = {0, 0, 0, 0};
        auto touch = [&]() {
            const int fb = j >> 6, fng = j & 63;
            SB();
            if (TOUCH & 1) tch[0] = *(const GAS u32*)((gcchar*)P.wo + (size_t)(oh * 128 + 64 * okh + lane) * 1024 + 256 * ons);
            if (TOUCH & 2) tch[1] = __builtin_amdgcn_raw_buffer_load_b32(rs, WS_SP + (u32)(((fb * 64 + fng) * 64 + lane) * 64), 0, 0);
            if (TOUCH & 4) tch[2] = __builtin_amdgcn_raw_buffer_load_b32(rs, WS_SP + (u32)((((lane >> 4) * 64 + 16 * ons + (lane & 15)) * 64 + oh * 2 + okh) * 64), 0, 0);
            if (TOUCH & 8) tch[3] = *(const GAS u32*)((gcchar*)P.out + fb * 1024 + fng * 16);
            SB();
        };
        u32 T = epoch + 1u; if (T == 0u) T = 1u;
        T = __builtin_amdgcn_readfirstlane(T);
        if (lane == 0) *(u32*)(lds + L_T) = T;
        if (pre) {
        DBG(STAMP(8);)

        {
            float s = 0.f;
#pragma unroll
            for (int i = 0; i < 4; ++i)
#pragma unroll
                for (int w = 0; w < 4; ++w) s = dot2(A.r[i][w], 0x3f803f80u, s);
            s += dppf<XOR1>(s); s += dppf<XOR2>(s); s += dppf<ROR4>(s); s += dppf<ROR8>(s);
            const float mean = s * (1.0f / 512.0f);
            f32x2 q2 = {0.f, 0.f};
#pragma unroll
            for (int i = 0; i < 4; ++i)
#pragma unroll
                for (int w = 0; w < 4; ++w) {
                    const f32x2 d = f32x2{bflo(A.r[i][w]), bfhi(A.r[i][w])} - mean;
                    q2 += d * d;
                }
            float q = q2.x + q2.y;
            q += dppf<XOR1>(q); q += dppf<XOR2>(q); q += dppf<ROR4>(q); q += dppf<ROR8>(q);
            const float var = bfr(q * (1.0f / 512.0f));
            const float mb = bfr(mean);
            const float rden = 1.0f / bfr(__builtin_sqrtf(bfr(var + 1e-5f)));
#pragma unroll
            for (int i = 0; i < 4; ++i) {
                u32x4 o;
#pragma unroll
                for (int w = 0; w < 4; ++w) {
                    const f32x2 d = f32x2{bflo(A.r[i][w]), bfhi(A.r[i][w])} - mb;
                    const u32 c = pack2(d.x, d.y);
                    const f32x2 e = f32x2{bflo(c), bfhi(c)} * rden;
                    o[w] = pack2(e.x, e.y);
                }
                *(u32x4*)(lds + L_XN + g * 1024 + i * 256 + n16 * 16) = o;
            }
        }
        asm volatile("" ::: "memory");
        if (lane == 0) lds_st(lds + L_XF, 1u);
        asm volatile("" ::: "memory");
        }
        u32x4 wa[2][8];
        u32x4 wo[16];
        auto issue_wo = [&]() {
            gcchar* wob = (gcchar*)P.wo + (size_t)(oh * 128 + 64 * okh) * 1024 + 256 * ons;
            const u32 wooff = (u32)(8 * g * 1024 + n16 * 16);
#pragma unroll
            for (int i = 0; i < 16; ++i) wo[i] = ldnt(wob + wooff + ((i >> 3) * 32 + (i & 7)) * 1024);
        };
        auto perm_wo = [&]() {
#pragma unroll
            for (int tau = 0; tau < 2; ++tau) {
                wa[tau][0] = tfrag<0>(&wo[8 * tau]); wa[tau][1] = tfrag<1>(&wo[8 * tau]); wa[tau][2] = tfrag<2>(&wo[8 * tau]); wa[tau][3] = tfrag<3>(&wo[8 * tau]);
                wa[tau][4] = tfrag<4>(&wo[8 * tau]); wa[tau][5] = tfrag<5>(&wo[8 * tau]); wa[tau][6] = tfrag<6>(&wo[8 * tau]); wa[tau][7] = tfrag<7>(&wo[8 * tau]);
            }
        };
        const int fb = j >> 6, fng = j & 63;
        const int fu = lane & 3, fi = lane >> 2;
        const u32 xw = *(const GAS u32*)((gcchar*)P.x + fb * 1024 + fng * 16 + fu * 4);
        const u32 b2w = *(const GAS u32*)((gcchar*)P.b2 + fng * 16 + fu * 4);
        STAMP(1);
        {
            u32x4 u0, u1;
            const u32 off = WS_Q + (u32)(grp * 2048 + lane * 16);
            if (pre) __builtin_amdgcn_s_sleep(QPRE_X); else __builtin_amdgcn_s_sleep(QPRE_P);
#pragma unroll 1
            for (u32 spin = 0; spin < SPIN_CAP; ++spin) {
                asm volatile("" ::: "memory");
                u0 = ld_unit(rs, off); u1 = ld_unit(rs, off + 1024);
                DBG(if (spin == 0) { asm volatile("s_waitcnt vmcnt(0)" ::: "memory"); STAMP(10); })
                if (__builtin_amdgcn_ballot_w64(!(unit_ok(u0, T) & unit_ok(u1, T))) == 0ull) { STAMPV(11, 1000000ull + spin); break; }
                __builtin_amdgcn_s_sleep(QINT);
            }
            *(u32x2*)(lds + L_Q + lane * 8) = u32x2{u0.x, u0.z};
            *(u32x2*)(lds + L_Q + 512 + lane * 8) = u32x2{u1.x, u1.z};
        }
        STAMP(3);
        asm volatile("" ::: "memory");
        if (lane == 0) lds_st(lds + L_QF, 1u);
        asm volatile("" ::: "memory");
        auto pin_wa = [&]() {
#pragma unroll
            for (int tau = 0; tau < 2; ++tau)
#pragma unroll
                for (int d = 0; d < 8; ++d) asm volatile("" : "+v"(wa[tau][d]));
        };
        auto gate = [&]() {
            const u32 off = WS_MP + (u32)(((fb * 64 + fng) * 32 + fi) * 64 + fu * 16);
            const bool has1 = fi + 16 < NMLP;
            u32x4 u0 = {0, 0, 0, 0}, u1 = {0, 0, 0, 0};
#pragma unroll 1
            for (u32 spin = 0; spin < SPIN_CAP; ++spin) {
                asm volatile("" ::: "memory");
                u0 = __builtin_amdgcn_raw_buffer_load_b128(rs, off, 0, GAUX); u1 = __builtin_amdgcn_raw_buffer_load_b128(rs, off + 1024, 0, GAUX);
                if (__builtin_amdgcn_ballot_w64(!(unit_ok(u0, T) & (unit_ok(u1, T) | !has1))) == 0ull) break;
                __builtin_amdgcn_s_sleep(2);
            }
            float a0 = __uint_as_float(u0.x) + (has1 ? __uint_as_float(u1.x) : 0.f);
            float a1 = __uint_as_float(u0.z) + (has1 ? __uint_as_float(u1.z) : 0.f);
            a0 += dppf<ROR4>(a0); a0 += dppf<ROR8>(a0); a0 = xrow_sum(a0);
            a1 += dppf<ROR4>(a1); a1 += dppf<ROR8>(a1); a1 = xrow_sum(a1);
            const float y0 = bfr(bfr(a0) + bflo(b2w)), y1 = bfr(bfr(a1) + bfhi(b2w));
            const float g0 = bfr(y0 / (1.0f + __expf(-y0)));
            const float g1 = bfr(y1 / (1.0f + __expf(-y1)));
            if (lane < 4) *(u32x4*)(lds + L_FIN + lane * 16) = u32x4{__float_as_uint(g0), __float_as_uint(g1), xw, 0u};
        };
        STAMP(4);
        wg_barrier();
        {
            __builtin_amdgcn_s_sleep(ORD2);
            asm volatile("" ::: "memory");
            const u32 goff = WS_MP + (u32)(((fb * 64 + fng) * 32 + fi) * 64 + fu * 16);
            const bool has1 = fi + 16 < NMLP;
            SB();
            u32x4 m0 = ld_unit(rs, goff), m1 = ld_unit(rs, goff + 1024);
            SB();
            issue_wo();
            SB();
            touch();
            SB();
            if (__builtin_amdgcn_ballot_w64(!(unit_ok(m0, T) & (unit_ok(m1, T) | !has1))) != 0ull) {
#pragma unroll 1
                for (u32 spin = 0; spin < SPIN_CAP; ++spin) {
                    __builtin_amdgcn_s_sleep(2);
                    asm volatile("" ::: "memory");
                    m0 = ld_unit(rs, goff); m1 = ld_unit(rs, goff + 1024);
                    if (__builtin_amdgcn_ballot_w64(!(unit_ok(m0, T) & (unit_ok(m1, T) | !has1))) == 0ull) break;
                }
            }
            float a0 = __uint_as_float(m0.x) + (has1 ? __uint_as_float(m1.x) : 0.f);
            float a1 = __uint_as_float(m0.z) + (has1 ? __uint_as_float(m1.z) : 0.f);
            a0 += dppf<ROR4>(a0); a0 += dppf<ROR8>(a0); a0 = xrow_sum(a0);
            a1 += dppf<ROR4>(a1); a1 += dppf<ROR8>(a1); a1 = xrow_sum(a1);
            const float y0 = bfr(bfr(a0) + bflo(b2w)), y1 = bfr(bfr(a1) + bfhi(b2w));
            const float g0 = bfr(y0 / (1.0f + __expf(-y0)));
            const float g1 = bfr(y1 / (1.0f + __expf(-y1)));
            if (lane < 4) *(u32x4*)(lds + L_FIN + lane * 16) = u32x4{__float_as_uint(g0), __float_as_uint(g1), xw, 0u};
            DBG(STAMP(7);)
            SB();
            perm_wo();
            pin_wa();
        }
        DBG(STAMP(9);)
        wg_barrier();
        STAMP(5);
        {
            f32x4 acc[8];
#pragma unroll
            for (int d = 0; d < 8; ++d) acc[d] = f32x4{0.f, 0.f, 0.f, 0.f};
#pragma unroll
            for (int tau = 0; tau < 2; ++tau) {
                const u32x4 of = *(const u32x4*)(lds + L_O + (n16 >> 2) * 128 + (32 * tau + 8 * g) * 2);
#pragma unroll
                for (int d = 0; d < 8; ++d) acc[d] = mfma(wa[tau][d], of, acc[d]);
            }
            publish_cols<64>(rs, WS_SP, acc, oh * 2 + okh, 16 * ons, lane, T);
        }
        asm volatile("" :: "v"(tch[0]), "v"(tch[1]), "v"(tch[2]), "v"(tch[3]));
        STAMP(6);
        FLUSH_STAMPS();
    }
}

extern "C" void kernel_launch(void* const* d_in, const int* in_sizes, int n_in,
                              void* d_out, int out_size, void* d_ws, size_t ws_size,
                              hipStream_t stream) {
    Params P;
    P.x = d_in[0]; P.k = d_in[1]; P.v = d_in[2]; P.wq = d_in[3]; P.wo = d_in[4];
    P.w1 = d_in[6]; P.b1 = d_in[7]; P.w2 = d_in[8]; P.b2 = d_in[9];
    P.out = d_out; P.ws = d_ws;
    fused_decode_kernel<<<256, 320, 0, stream>>>(P);
}
```

```cpp
#include <hip/hip_runtime.h>
#include <hip/hip_bf16.h>
#include <stdint.h>

typedef unsigned int u32;
typedef u32 u32x4 __attribute__((ext_vector_type(4)));
typedef u32 u32x2 __attribute__((ext_vector_type(2)));
typedef float f32x4 __attribute__((ext_vector_type(4)));
typedef float f32x2 __attribute__((ext_vector_type(2)));
typedef __bf16 bf16x8 __attribute__((ext_vector_type(8)));
typedef __bf16 bf16x2 __attribute__((ext_vector_type(2)));
#define GAS __attribute__((address_space(1)))
typedef GAS const u32x4 gcu32x4;
typedef GAS const char gcchar;
#define LAS __attribute__((address_space(3)))
__device__ __forceinline__ unsigned int lds_ld(const char* p) { return *(volatile LAS const unsigned int*)(LAS const char*)p; }
__device__ __forceinline__ u32x4 lds_ld4(const char* p) { return *(volatile LAS const u32x4*)(LAS const char*)p; }
__device__ __forceinline__ void lds_st(char* p, unsigned int v) { *(volatile LAS unsigned int*)(LAS char*)p = v; }

constexpr int NB = 4, DM = 512, HKV = 8, SK = 16384, DH = 128, FF = 1792;
constexpr int NMLP = 28;
constexpr u32 WS_EPOCH = 0;
constexpr u32 WS_Q  = 0x1000;
constexpr u32 WS_CNT = 0x18000;
constexpr u32 WS_PL = 0x20000;
constexpr u32 WS_MP = 0x30000;
constexpr u32 WS_PO = 0x100000;
constexpr u32 WS_SP = 0x200000;
constexpr u32 WS_END = 0x400000;
constexpr u32 SPIN_CAP = 1u << 17;
constexpr int L_Q   = 0;
constexpr int L_T   = 1024;
constexpr int L_QF  = 1040;
constexpr int L_XF  = 1044;
constexpr int L_BC  = 1048;
constexpr int L_FIN = 1088;
constexpr int L_XN  = 2048;
constexpr int L_HP  = 6144;
constexpr int L_H   = 14336;
constexpr int L_ML  = 14848;
constexpr int L_O   = 15104;
constexpr int L_PF  = 18432;
constexpr int LDS_BYTES = L_PF + 4 * 32768;

struct Params {
    const void* x; const void* k; const void* v; const void* wq; const void* wo;
    const void* w1; const void* b1; const void* w2; const void* b2;
    void* out; void* ws;
};

__device__ __forceinline__ u32x4 ldnt(gcchar* p) { return __builtin_nontemporal_load((gcu32x4*)p); }
__device__ __forceinline__ float bflo(u32 w) { return __uint_as_float(w << 16); }
__device__ __forceinline__ float bfhi(u32 w) { return __uint_as_float(w & 0xffff0000u); }
__device__ __forceinline__ u32 pack2(float lo, float hi) {
    f32x2 v = {lo, hi};
    return __builtin_bit_cast(u32, __builtin_convertvector(v, bf16x2));
}
__device__ __forceinline__ float bfr(float x) { return bflo(pack2(x, 0.f)); }
__device__ __forceinline__ f32x4 mfma(u32x4 a, u32x4 b, f32x4 c) {
    return __builtin_amdgcn_mfma_f32_16x16x32_bf16(__builtin_bit_cast(bf16x8, a), __builtin_bit_cast(bf16x8, b), c, 0, 0, 0);
}
__device__ __forceinline__ float dot2(u32 a, u32 b, float c) {
    return __builtin_amdgcn_fdot2_f32_bf16(__builtin_bit_cast(bf16x2, a), __builtin_bit_cast(bf16x2, b), c, false);
}
template <int CTRL> __device__ __forceinline__ u32 dppu(u32 x) { return (u32)__builtin_amdgcn_mov_dpp((int)x, CTRL, 0xf, 0xf, true); }
template <int CTRL> __device__ __forceinline__ float dppf(float x) { return __uint_as_float(dppu<CTRL>(__float_as_uint(x))); }
constexpr int XOR1 = 0xB1, XOR2 = 0x4E, HMIR = 0x141, ROR4 = 0x124, ROR8 = 0x128, EVEN = 0xA0, ODD = 0xF5;
__device__ __forceinline__ float xrow_max(float x) {
    auto s = __builtin_amdgcn_permlane16_swap(__float_as_uint(x), __float_as_uint(x), false, false);
    x = fmaxf(__uint_as_float(s[0]), __uint_as_float(s[1]));
    auto t = __builtin_amdgcn_permlane32_swap(__float_as_uint(x), __float_as_uint(x), false, false);
    return fmaxf(__uint_as_float(t[0]), __uint_as_float(t[1]));
}
__device__ __forceinline__ float xrow_sum(float x) {
    auto s = __builtin_amdgcn_permlane16_swap(__float_as_uint(x), __float_as_uint(x), false, false);
    x = __uint_as_float(s[0]) + __uint_as_float(s[1]);
    auto t = __builtin_amdgcn_permlane32_swap(__float_as_uint(x), __float_as_uint(x), false, false);
    return __uint_as_float(t[0]) + __uint_as_float(t[1]);
}
__device__ __forceinline__ float x32_max(float x) {
    auto t = __builtin_amdgcn_permlane32_swap(__float_as_uint(x), __float_as_uint(x), false, false);
    return fmaxf(__uint_as_float(t[0]), __uint_as_float(t[1]));
}
__device__ __forceinline__ float x32_sum(float x) {
    auto t = __builtin_amdgcn_permlane32_swap(__float_as_uint(x), __float_as_uint(x), false, false);
    return __uint_as_float(t[0]) + __uint_as_float(t[1]);
}
__device__ __forceinline__ float wave_sum(float x) {
    x += dppf<XOR1>(x); x += dppf<XOR2>(x); x += dppf<ROR4>(x); x += dppf<ROR8>(x);
    return xrow_sum(x);
}
__device__ __forceinline__ void wg_barrier() { asm volatile("s_waitcnt lgkmcnt(0)\n\ts_barrier" ::: "memory"); }
__device__ __forceinline__ void raw_barrier() { asm volatile("s_barrier" ::: "memory"); }
__device__ __forceinline__ void quad_barrier(char* cnt, u32 target, int lane) {
    asm volatile("" ::: "memory");
    if (lane == 0) __hip_atomic_fetch_add((LAS unsigned int*)(LAS char*)cnt, 1u, __ATOMIC_RELAXED, __HIP_MEMORY_SCOPE_WORKGROUP);
#pragma unroll 1
    for (u32 spin = 0; spin < SPIN_CAP; ++spin) {
        asm volatile("" ::: "memory");
        if (lds_ld(cnt) >= target) break;
    }
    asm volatile("" ::: "memory");
}
__device__ __forceinline__ void wait_flag(const char* f) {
#pragma unroll 1
    for (u32 spin = 0; spin < SPIN_CAP; ++spin) {
        asm volatile("" ::: "memory");
        if (lds_ld(f) != 0u) break;
        __builtin_amdgcn_s_sleep(1);
    }
    asm volatile("" ::: "memory");
}
constexpr u32 OOB = 0x40000000u;

__device__ __forceinline__ u32x4 mk_unit(u32 v0, u32 v1, u32 T) { u32x4 u = {v0, T ^ v1, v1, T ^ v0}; return u; }
__device__ __forceinline__ bool unit_ok(u32x4 u, u32 T) { return ((u.y ^ u.z) == T) & ((u.w ^ u.x) == T); }
__device__ __forceinline__ void st_unit(__amdgpu_buffer_rsrc_t rs, u32 off, u32x4 u) { __builtin_amdgcn_raw_buffer_store_b128(u, rs, off, 0, 16); }
__device__ __forceinline__ u32x4 ld_unit(__amdgpu_buffer_rsrc_t rs, u32 off) { return __builtin_amdgcn_raw_buffer_load_b128(rs, off, 0, 16); }

template <int DD> __device__ __forceinline__ u32x4 tfrag(const u32x4* v) {
    constexpr u32 sel = (DD & 1) ? 0x07060302u : 0x05040100u;
    u32x4 a;
    a.x = __builtin_amdgcn_perm(v[1][DD >> 1], v[0][DD >> 1], sel);
    a.y = __builtin_amdgcn_perm(v[3][DD >> 1], v[2][DD >> 1], sel);
    a.z = __builtin_amdgcn_perm(v[5][DD >> 1], v[4][DD >> 1], sel);
    a.w = __builtin_amdgcn_perm(v[7][DD >> 1], v[6][DD >> 1], sel);
    return a;
}
__device__ __forceinline__ void pv_tile(const u32x4* v, u32x4 p, f32x4 (&acc)[8]) {
    acc[0] = mfma(tfrag<0>(v), p, acc[0]); acc[1] = mfma(tfrag<1>(v), p, acc[1]);
    acc[2] = mfma(tfrag<2>(v), p, acc[2]); acc[3] = mfma(tfrag<3>(v), p, acc[3]);
    acc[4] = mfma(tfrag<4>(v), p, acc[4]); acc[5] = mfma(tfrag<5>(v), p, acc[5]);
    acc[6] = mfma(tfrag<6>(v), p, acc[6]); acc[7] = mfma(tfrag<7>(v), p, acc[7]);
}
#define MPAUX 16
template <int NSLOT, int AUX = 16>
__device__ __forceinline__ void publish_cols(__amdgpu_buffer_rsrc_t rs, u32 base, const f32x4 (&acc)[8], int slot, int ng0, int lane, u32 T) {
    const int g = lane >> 4, b = (lane >> 2) & 3, rho = lane & 3;
    const u32 off0 = base + (u32)(((b * 64 + ng0 + 4 * g) * NSLOT + slot) * 64 + rho * 16);
#pragma unroll
    for (int r = 0; r < 4; ++r) {
        float a0 = (rho & 2) ? acc[4][r] : acc[0][r], a1 = (rho & 2) ? acc[6][r] : acc[2][r];
        float c0 = (rho & 2) ? acc[5][r] : acc[1][r], c1 = (rho & 2) ? acc[7][r] : acc[3][r];
        float v0 = (rho & 1) ? a1 : a0, v1 = (rho & 1) ? c1 : c0;
        __builtin_amdgcn_raw_buffer_store_b128(mk_unit(__float_as_uint(v0), __float_as_uint(v1), T), rs, off0 + (u32)(r * NSLOT * 64), 0, AUX);
    }
}

#define STAMP(i) do { } while (0)
#define STAMPV(i, v) do { } while (0)
#define FLUSH_STAMPS() do { } while (0)
#define DBG(x)

struct Ring { u32x4 r[16]; };
typedef __amdgpu_buffer_rsrc_t rsrc_t;
#define DMAMOD "nt"
constexpr int NT = 2;
__device__ __forceinline__ u32x4 ldb_nt(rsrc_t rs, u32 voff, u32 soff) { return __builtin_amdgcn_raw_buffer_load_b128(rs, voff, soff, NT); }
constexpr u32 TSTR = 4 * 8192;
__device__ __forceinline__ constexpr u32 kimm(int i) { return (u32)((i >> 2) * 4096 + (i & 3) * 64); }
__device__ __forceinline__ void issue_k(Ring& R, rsrc_t ks, u32 koff, u32 toff, u32 toff2) {
#pragma unroll
    for (int i = 0; i < 8; ++i) R.r[i] = ldb_nt(ks, koff + (i & 3) * 64, i < 4 ? toff : toff2);
}
__device__ __forceinline__ void issue_v(Ring& R, rsrc_t vs, u32 voff, u32 toff, u32 toff2) {
#pragma unroll
    for (int i = 0; i < 8; ++i) R.r[8 + i] = ldb_nt(vs, voff + (i & 3) * 1024, i < 4 ? toff : toff2);
}
__device__ __forceinline__ void issue_k(Ring& R, rsrc_t ks, u32 koff, u32 toff) { issue_k(R, ks, koff, toff, toff + 4096); }
__device__ __forceinline__ void issue_v(Ring& R, rsrc_t vs, u32 voff, u32 toff) { issue_v(R, vs, voff, toff, toff + 4096); }
__device__ __forceinline__ void dma16(u32 ldst, u32 voff, u32x4 srd, u32 soff) {
    u32 keep;
    asm volatile("s_mov_b32 %0, m0\n\ts_mov_b32 m0, %1\n\ts_nop 0\n\tbuffer_load_dwordx4 %2, %3, %4 offen " DMAMOD " lds\n\ts_mov_b32 m0, %0"
                 : "=&s"(keep) : "s"(ldst), "v"(voff), "s"(srd), "s"(soff) : "memory");
}
__device__ __forceinline__ u32x4 mk_srd(const void* base, u32 bytes) {
    const uint64_t a = (uint64_t)base;
    u32x4 d = {(u32)__builtin_amdgcn_readfirstlane((u32)a), (u32)__builtin_amdgcn_readfirstlane((u32)(a >> 32) & 0xffffu), bytes, 0x00020000u};
    return d;
}
#define SB() __builtin_amdgcn_sched_barrier(0)
#define QPRE 48
#define QINT 16
#define PACE_N 7
#define STR2(x) #x
#define STR(x) STR2(x)
#define PACE() asm volatile("s_waitcnt vmcnt(" STR(PACE_N) ")" ::: "memory")
#define LPACE_N 7
#define LPACE() asm volatile("s_waitcnt vmcnt(" STR(LPACE_N) ")" ::: "memory")
#define MPACE_N 15
#define PACEC(flag) asm volatile("s_cmp_lg_u32 %0, 0\n\ts_cbranch_scc1 1f\n\ts_waitcnt vmcnt(" STR(PACE_N) ")\n1:\n\ts_waitcnt vmcnt(" STR(MPACE_N) ")" :: "s"(flag) : "scc", "memory")
#define PB 1
#define SVCPRIO 3

#define GAUX 16
#define DQ 32
#define NP 528
#define QR0 2
#define QR1 5
#define NL 1
#define QPRE_P 100
#define QPRE_X 60
#define XAUX 0
#define QVP 63
#define TOPN 4
#define EARLYP 1
#define GD 1
#define GK 1
#define GATE_N 6
#define PD 1
#define PK 1
#define FINW 0
#define GLATE 1
#define XEARLY 1
#define TOUCH 14
#define TOUCHQ 0
#define TOUCHP 2
#define ORD2 6
#define WLATE 1
#define WPIN 1
#define CBX 0
#define DYN 0
#define SP 15
#define DQT 1
#define DMT 2
#define PTOUCH 1
#define NP2 520
#define NP1 532
#define D3 16
#define XADJ 2, -1, 2, -1, 2, -2, 0, -2
constexpr u32 xadj_cum() {
    const int a[8] = {XADJ};
    u32 r = 0; int c = 0;
    for (int i = 0; i < 8; ++i) { r |= (u32)(c + 8) << (4 * i); c += a[i]; }
    return c == 0 ? r : 0xffffffffu;
}
constexpr u32 XCUM = xadj_cum();
static_assert(XCUM != 0xffffffffu, "adjustments sum to zero");
#define RADJ 1, 1, -1, 1, 1, -1, 0, -2
#define RADJ2 1, 0, -1, 0, 0, -1, 1, 0
#define RADJ1 1, 0, -1, 0, 0, -1, 1, 0
constexpr u32 radj_cum(int which) {
    const int a[8] = {RADJ}, a1[8] = {RADJ1}, a2[8] = {RADJ2};
    u32 r = 0; int c = 0;
    for (int i = 0; i < 8; ++i) { r |= (u32)(c + 8) << (4 * i); c += which == 2 ? a2[i] : which == 1 ? a1[i] : a[i]; }
    return c == 0 ? r : 0xffffffffu;
}
constexpr u32 RCUM = radj_cum(0), RCUM1 = radj_cum(1), RCUM2 = radj_cum(2);
static_assert(RCUM != 0xffffffffu && RCUM1 != 0xffffffffu && RCUM2 != 0xffffffffu, "adjustments sum to zero");
static_assert(NP % 4 == 0 && NP2 % 4 == 0 && NP1 % 4 == 0 && DQ % 4 == 0 && D3 % 4 == 0 && 4096 - 7 * NP + 2 * DQ > 160 && 4096 - 7 * NP1 + 2 * DQ + D3 > 160, "key split");
struct AttnState { f32x4 acc[8]; float m, l; };

__device__ __forceinline__ void qk_tile(const u32x4* k, const u32x4 (&qf)[4], f32x4& s0, f32x4& s1) {
    s0 = f32x4{0.f, 0.f, 0.f, 0.f}; s1 = f32x4{0.f, 0.f, 0.f, 0.f};
#pragma unroll
    for (int st = 0; st < 4; ++st) { s0 = mfma(k[st], qf[st], s0); s1 = mfma(k[4 + st], qf[st], s1); }
}
template <bool MASKED = false>
__device__ __forceinline__ u32x4 softmax_step(const f32x4 s0, const f32x4 s1, AttnState& S, int nj = 8) {
    constexpr float SCL = 0.08838834764831845f, LOG2E = 1.4426950408889634f;
    float z[8];
#pragma unroll
    for (int j = 0; j < 2; ++j) {
        const u32 a = pack2(s0[2 * j], s0[2 * j + 1]), c = pack2(s1[2 * j], s1[2 * j + 1]);
        const u32 a2 = pack2(bflo(a) * SCL, bfhi(a) * SCL), c2 = pack2(bflo(c) * SCL, bfhi(c) * SCL);
        z[2 * j] = bflo(a2); z[2 * j + 1] = bfhi(a2); z[4 + 2 * j] = bflo(c2); z[4 + 2 * j + 1] = bfhi(c2);
    }
    if (MASKED) {
#pragma unroll
        for (int j = 0; j < 8; ++j) z[j] = (j < nj) ? z[j] : -INFINITY;
    }
    float mx = fmaxf(fmaxf(fmaxf(z[0], z[1]), fmaxf(z[2], z[3])), fmaxf(fmaxf(z[4], z[5]), fmaxf(z[6], z[7])));
    mx = xrow_max(mx) * LOG2E;
    if (__builtin_amdgcn_ballot_w64(mx > S.m) != 0ull) {
        const float mn = fmaxf(S.m, mx);
        const float al = __builtin_amdgcn_exp2f(S.m - mn);
        S.m = mn; S.l *= al;
        f32x2 al2 = {al, al};
        asm volatile("" : "+v"(al2));
        const f32x4 al4 = {al2.x, al2.y, al2.x, al2.y};
#pragma unroll
        for (int d = 0; d < 8; ++d) S.acc[d] *= al4;
    }
    float p[8];
#pragma unroll
    for (int j = 0; j < 8; ++j) p[j] = __builtin_amdgcn_exp2f(__builtin_fmaf(z[j], LOG2E, -S.m));
    S.l += ((p[0] + p[1]) + (p[2] + p[3])) + ((p[4] + p[5]) + (p[6] + p[7]));
    u32x4 pf = {pack2(p[0], p[1]), pack2(p[2], p[3]), pack2(p[4], p[5]), pack2(p[6], p[7])};
    return pf;
}
__device__ __forceinline__ void proc_tile(Ring& R, AttnState& S, const u32x4 (&qf)[4], rsrc_t ks, rsrc_t vs, u32 koff, u32 voff, u32 toff) {
    f32x4 s0, s1;
    qk_tile(&R.r[0], qf, s0, s1);
    SB();
    issue_k(R, ks, koff, toff);
    SB();
    const u32x4 pf = softmax_step(s0, s1, S);
    pv_tile(&R.r[8], pf, S.acc);
    SB();
    issue_v(R, vs, voff, toff);
    SB();
}
__device__ __forceinline__ u32 proc_tile_c(Ring& R, AttnState& S, const u32x4 (&qf)[4], rsrc_t ks, rsrc_t vs, u32 koff, u32 voff, u32 toff, rsrc_t cs, bool dyn) {
    f32x4 s0, s1;
    qk_tile(&R.r[0], qf, s0, s1);
    SB();
    issue_k(R, ks, koff, toff);
    SB();
    const u32x4 pf = softmax_step(s0, s1, S);
    asm volatile("" : "+v"(S.l));
    pv_tile(&R.r[8], pf, S.acc);
    SB();
    u32 claim = 0;
    if (dyn) claim = (u32)__builtin_amdgcn_raw_ptr_buffer_atomic_add_i32(1, cs, (int)voff, 0, 16);
    SB();
    issue_v(R, vs, voff, toff);
    SB();
    return claim;
}
__device__ __forceinline__ void proc_last(Ring& R, AttnState& S, const u32x4 (&qf)[4]) {
    f32x4 s0, s1;
    qk_tile(&R.r[0], qf, s0, s1);
    const u32x4 pf = softmax_step(s0, s1, S);
    pv_tile(&R.r[8], pf, S.acc);
}
__device__ __forceinline__ void proc_part(Ring& R, AttnState& S, const u32x4 (&qf)[4], int nj) {
    f32x4 s0, s1;
    qk_tile(&R.r[0], qf, s0, s1);
    const u32x4 pf = softmax_step<true>(s0, s1, S, nj);
    pv_tile(&R.r[8], pf, S.acc);
}
__device__ __forceinline__ void proc_lds(const char* sl, AttnState& S, const u32x4 (&qf)[4]) {
    u32x4 c[8];
#pragma unroll
    for (int i = 0; i < 8; ++i) c[i] = *(const u32x4*)(sl + i * 1024);
    f32x4 s0, s1;
    qk_tile(c, qf, s0, s1);
    SB();
#pragma unroll
    for (int i = 0; i < 8; ++i) c[i] = *(const u32x4*)(sl + 8192 + i * 1024);
    const u32x4 pf = softmax_step(s0, s1, S);
    pv_tile(c, pf, S.acc);
}

__global__ void __launch_bounds__(320, 2) fused_decode_kernel(Params P) {
    __shared__ __attribute__((aligned(16))) char lds[LDS_BYTES];
    const int tid = threadIdx.x, lane = tid & 63;
    const int wave = __builtin_amdgcn_readfirstlane(tid >> 6);
    const int j = blockIdx.x;
    DBG(unsigned long long ts[NSTAMP]; for (int z = 0; z < NSTAMP; ++z) ts[z] = 0;)
    STAMP(0);
    const int grp = j >> 3, split = j & 7;
    const int b = grp >> 3, ahkv = grp & 7;
    const int rank = (split - ahkv - 1) & 7;
    const bool grp_mlp = grp < NMLP;
    int rk = rank;
    asm volatile("" : "+s"(rk));
    int f_q = ((1 << QR0 | 1 << QR1) >> rk) & 1;
    int f_m = (((grp - NMLP) >> 24 & 0x80) >> rk) & 1;
    int f_q1 = (rk >> 2) & f_q;
    static_assert(QR0 < 4 && QR1 >= 4, "rank bit 2 tells the two q slices apart");
    asm volatile("" : "+s"(f_q), "+s"(f_m), "+s"(f_q1));
    int f_pre = f_q | f_m, f_svc = wave >> 2;
    asm volatile("" : "+s"(f_pre), "+s"(f_svc));
    const bool is_q = f_q != 0, is_mlp = f_m != 0, pre = f_pre != 0;
    const int g = lane >> 4, n16 = lane & 15;
    const int n_plain = b == 1 ? NP1 : grp_mlp ? NP : NP2;
    const int dq1 = b == 1 ? DQ + D3 : DQ;
    const int xc0 = (int)(XCUM >> (4 * ((ahkv + 1) & 7))) & 15, xc1 = (int)(XCUM >> (4 * split)) & 15, xc2 = (int)(XCUM >> (4 * ((split + 1) & 7))) & 15;
    const u32 rcum = b == 1 ? RCUM1 : grp_mlp ? RCUM : RCUM2;
    const int rc1 = (int)(rcum >> (4 * rank)) & 15, rc2 = (int)(rcum >> (4 * ((rank + 1) & 7))) & 15;
    const int nkeys = (rank == 7 ? 4096 + DQ + dq1 - 7 * n_plain : n_plain - f_q * DQ - f_q1 * (dq1 - DQ)) + 4 * (xc2 - xc1 + rc2 - rc1);
    const int nfull = (nkeys - 1) >> 5, nj = (nkeys - 32 * nfull) >> 2;
    const u32 w_start = (u32)(rank * n_plain - (rank > QR0 ? DQ : 0) - (rank > QR1 ? dq1 : 0) + 4 * (xc1 - xc0 + rc1 - 8)) * 1024u;
    const size_t tb = (size_t)grp * (SK * DH * 2);
    constexpr u32 trange = SK * DH * 2;
    const u32 t0 = w_start + (u32)wave * 8192;
    const u32 koff = (u32)((4 * (n16 & 3) + (n16 >> 2)) * 256 + g * 16);
    const u32 voff = (u32)(lane * 16);
    Ring A;
    u32 epoch = 0;
    {
        const u32 mq = 0u - (u32)f_q, mp = 0u - (u32)f_pre, ms = 0u - (u32)f_svc;
        auto sel = [](u32 m, u32 x, u32 y) { return y ^ ((x ^ y) & m); };
        const bool svc = f_svc != 0;
        const u32 wstr = sel(mq, 8192u, (u32)(FF * 2));
        const u32 wcol = sel(mq, (u32)(ahkv * 1024 + ((2 * b + f_q1) ^ CBX) * 128), (u32)(grp * 128));
        const u32 wb = (u32)(128 * wave) * wstr + wcol;
        const u32 nb = sel(mp, 512u * wstr, trange), na = sel(ms, 4096u, nb);
        const u32 wv = (u32)(32 * (n16 >> 3) + 8 * g) * wstr + (u32)((n16 & 7) * 16);
        const u32 va = sel(ms, (u32)(g * 1024 + n16 * 16), sel(mp, wv, koff));
        const u32 vb = sel(mp, wv, voff);
        const u32 sa = sel(ms, sel(mp, 0u, OOB), sel(mp, wb, t0)), sb = sel(mp, wb + 64u * wstr, t0);
        const u32 a1 = sel(ms, 256u, sel(mp, wstr, 64u)), a2 = sel(mp, 4u * wstr, 4096u);
        const u32 b1 = sel(mp, wstr, 1024u);
        SB();
        const uint64_t Mq = (uint64_t)(int64_t)(int)mq, Mp = (uint64_t)(int64_t)(int)mp, Ms = (uint64_t)(int64_t)(int)ms;
        auto sel64 = [](uint64_t m, uint64_t x, uint64_t y) { return y ^ ((x ^ y) & m); };
        const uint64_t wp = sel64(Mq, (uint64_t)P.wq, (uint64_t)P.w1);
        const uint64_t pa = sel64(Ms, (uint64_t)P.x, sel64(Mp, wp, (uint64_t)P.k + tb));
        const uint64_t pb = sel64(Mp, wp, (uint64_t)P.v + tb);
        const rsrc_t ra = __builtin_amdgcn_make_buffer_rsrc((void*)pa, 0, na, 0x00020000);
        const rsrc_t rb = __builtin_amdgcn_make_buffer_rsrc((void*)pb, 0, nb, 0x00020000);
        if (lane < 3) lds_st(lds + L_QF + lane * 4, 0u);
#pragma unroll
        for (int i = 0; i < 4; ++i) A.r[i] = __builtin_amdgcn_raw_buffer_load_b128(ra, va, sa + (u32)i * a1, XAUX);
        epoch = __builtin_amdgcn_raw_buffer_load_b32(__builtin_amdgcn_make_buffer_rsrc(P.ws, 0, 16, 0x00020000), 0, ~ms & OOB, 0);
        SB(); wg_barrier(); SB();
        if (!svc) {
#pragma unroll
            for (int i = 4; i < 16; ++i)
                A.r[i] = i < 8 ? ldb_nt(ra, va, sa + (u32)(i & 3) * a1 + a2) : ldb_nt(rb, vb, sb + (u32)(i & 3) * b1 + (u32)((i - 8) >> 2) * a2);
        }
        SB();
    }
    STAMP(15);
    int wave2 = wave;
    asm volatile("" : "+s"(wave2));
    const int hkv = j & 7, pidx = j >> 3;
    const __amdgpu_buffer_rsrc_t rs = __builtin_amdgcn_make_buffer_rsrc(P.ws, 0, WS_END, 0x00020000);
    const int oh = hkv * 4 + (pidx & 3), okh = (pidx >> 2) & 1, ons = pidx >> 3;

    if (wave2 < 4) {
        auto tof = [&](u32 i) { return t0 + i * TSTR; };
        const u32 m_off = w_start + (u32)nfull * TSTR + (u32)(wave * nj) * 1024u, m_end = m_off + (u32)nj * 1024u;
        const rsrc_t ks = __builtin_amdgcn_make_buffer_rsrc((char*)P.k + tb, 0, trange, 0x00020000);
        const rsrc_t vs = __builtin_amdgcn_make_buffer_rsrc((char*)P.v + tb, 0, trange, 0x00020000);
        char* const pf_base = lds + L_PF + wave * 32768;
        const u32x4 ksd = mk_srd((const char*)P.k + tb, trange), vsd = mk_srd((const char*)P.v + tb, trange);
        const u32 lb = (u32)__builtin_amdgcn_readfirstlane((u32)(uintptr_t)(__attribute__((address_space(3))) char*)pf_base);
        Ring B;
        AttnState S;
        const u32 tB = is_q ? tof(0) : tof(1);
        if (is_mlp) {
            const rsrc_t w2s = __builtin_amdgcn_make_buffer_rsrc((void*)P.w2, 0, FF * DM * 2, 0x00020000);
            const u32 w2b = (u32)(grp * 64 * 1024 + 256 * wave);
            const u32 w2off = (u32)(8 * g * 1024 + n16 * 16);
#pragma unroll
            for (int i = 0; i < 16; ++i) B.r[i] = ldb_nt(w2s, w2off, w2b + ((i >> 3) * 32 + (i & 7)) * 1024);
        } else {
            if (is_q) asm volatile("s_waitcnt vmcnt(" STR(QVP) ")" ::: "memory");
#pragma unroll
            for (int i = 0; i < 8; ++i) { if (!is_q && i % PB == 0) PACE(); B.r[8 + i] = ldb_nt(vs, voff + (i & 3) * 1024, tB + (i >> 2) * 4096); }
        }
        if (pre) {
            const float b1v = is_mlp ? bflo((u32)((const GAS unsigned short*)P.b1)[grp * 64 + lane]) : 0.f;
            wait_flag(lds + L_XF);
#pragma unroll
            for (int d = 0; d < 8; ++d) S.acc[d] = f32x4{0.f, 0.f, 0.f, 0.f};
#pragma unroll
            for (int tau = 0; tau < 2; ++tau) {
                const u32x4 xf = *(const u32x4*)(lds + L_XN + (n16 & 3) * 1024 + (128 * wave + 64 * tau + 32 * ((n16 >> 2) & 1) + 8 * g) * 2);
                pv_tile(&A.r[8 * tau], xf, S.acc);
            }
            if (n16 < 8 && (g >> 1) == (n16 >> 2)) {
                float* hp = (float*)(lds + L_HP) + ((wave * 2 + (n16 >> 2)) * 4 + (n16 & 3)) * 64 + 32 * (g & 1);
#pragma unroll
                for (int r = 0; r < 4; ++r) {
                    *(f32x4*)(hp + 8 * r) = f32x4{S.acc[0][r], S.acc[1][r], S.acc[2][r], S.acc[3][r]};
                    *(f32x4*)(hp + 8 * r + 4) = f32x4{S.acc[4][r], S.acc[5][r], S.acc[6][r], S.acc[7][r]};
                }
            }
            DBG(STAMP(6);)
            quad_barrier(lds + L_BC, 4u, lane);
            float sum = 0.f;
            {
                const float* hp = (const float*)(lds + L_HP) + wave * 64 + lane;
#pragma unroll
                for (int i = 0; i < 8; ++i) sum += hp[i * 256];
            }
            const u32 T = *(const u32*)(lds + L_T);
            if (is_q) {
                const u32 w01 = pack2(sum, dppf<XOR1>(sum));
                const u32 w23 = dppu<XOR2>(w01);
                if ((lane & 3) == 0)
                    st_unit(rs, WS_Q + (u32)((wave * 8 + ahkv) * 2048 + (16 * ((2 * b + f_q1) ^ CBX) + (lane >> 2)) * 16), mk_unit(w01, w23, T));
                DBG(STAMP(7);)
            } else {
                float h = bfr(bfr(sum) + b1v);
                h = h > 0.f ? h : 0.f;
                ((unsigned short*)(lds + L_H))[wave * 64 + lane] = (unsigned short)pack2(h * h, 0.f);
            }
            SB();
            const u32 tA = is_q ? tof(1) : tof(0);
            issue_k(A, ks, koff, tA);
            issue_v(A, vs, voff, tA);
            SB();
            if (is_mlp) {
                quad_barrier(lds + L_BC, 8u, lane);
#pragma unroll
                for (int d = 0; d < 8; ++d) S.acc[d] = f32x4{0.f, 0.f, 0.f, 0.f};
#pragma unroll
                for (int tau = 0; tau < 2; ++tau) {
                    const u32x4 hf = *(const u32x4*)(lds + L_H + (n16 >> 2) * 128 + (32 * tau + 8 * g) * 2);
                    pv_tile(&B.r[8 * tau], hf, S.acc);
                }
                publish_cols<32, MPAUX>(rs, WS_MP, S.acc, grp, 16 * wave, lane, T);
                DBG(STAMP(7);)
                SB();
                issue_v(B, vs, voff, tB);
            }
        }
        SB();
#pragma unroll
        for (int i = 0; i < 8; ++i) { if (i % PB == 0) PACE(); B.r[i] = ldb_nt(ks, koff + (i & 3) * 64, tB + (i >> 2) * 4096); }
        STAMP(11);
#pragma unroll
        for (int t = 0; t < NL; ++t) {
#pragma unroll
            for (int i = 0; i < 8; ++i) { if (i % PB == 0) LPACE(); dma16(lb + t * 16384 + i * 1024, koff, ksd, tof(2 + t) + kimm(i)); }
#pragma unroll
            for (int i = 0; i < 8; ++i) { if (i % PB == 0) LPACE(); dma16(lb + t * 16384 + 8192 + i * 1024, voff, vsd, tof(2 + t) + i * 1024); }
        }
        STAMP(1);
        wait_flag(lds + L_QF);
        STAMP(2);
        u32x4 qf[4];
#pragma unroll
        for (int st = 0; st < 4; ++st) qf[st] = *(const u32x4*)(lds + L_Q + (n16 & 3) * 256 + st * 64 + g * 16);
        const u32 T = *(const u32*)(lds + L_T);
#pragma unroll
        for (int d = 0; d < 8; ++d) S.acc[d] = f32x4{0.f, 0.f, 0.f, 0.f};
        S.m = -INFINITY; S.l = 0.f;
        proc_tile(A, S, qf, ks, vs, koff, voff, tof(2 + NL));
        DBG(STAMP(12);)
        proc_last(B, S, qf);
#pragma unroll
        for (int t = 0; t < NL; ++t) {
            asm volatile("s_waitcnt vmcnt(16)" ::: "memory");
            proc_lds(pf_base + t * 16384 + lane * 16, S, qf);
        }
        DBG(STAMP(13);)
#pragma unroll 1
        for (int i = 2 + NL; i < nfull; ++i) {
            const bool last = i + 1 == nfull;
            const u32 nrec = last ? m_end : trange;
            const rsrc_t kc = __builtin_amdgcn_make_buffer_rsrc((char*)P.k + tb, 0, nrec, 0x00020000);
            const rsrc_t vc = __builtin_amdgcn_make_buffer_rsrc((char*)P.v + tb, 0, nrec, 0x00020000);
            proc_tile(A, S, qf, kc, vc, koff, voff, last ? m_off : tof((u32)(i + 1)));
            DBG(if (i == 5) STAMP(8); if (i == 9) STAMP(9); if (i == 13) STAMP(10); )
        }
        proc_part(A, S, qf, nj);
        STAMP(3);
        const int half = lane >> 5;
        const u32 blk0 = (u32)((wave * 32 + oh) * 8);
        const u32 ooff = WS_PO + (blk0 + half) * 1024 + (u32)((32 * okh + (lane & 31)) * 16);
        const u32 loff = lane < 8 ? WS_PL + (blk0 + lane) * 16 : OOB;
        struct Pass { u32x4 l, t[4]; };
        Pass p0, p1;
        u32 want = 0xfu, have = 0;
        u32 lseen = 0;
        u32 lsev = 0;
        f32x2 uo[4];
        auto g_issue = [&](Pass& p) {
            SB();
            p.l = ld_unit(rs, loff);
#pragma unroll
            for (int r = 0; r < 4; ++r) p.t[r] = ld_unit(rs, ((want >> r) & 1u) ? ooff + r * 2048 : OOB);
            SB();
        };
        auto g_eval = [&](const Pass& p) -> bool {
            SB();
            const bool lok = unit_ok(p.l, T);
            lsev = lok ? p.l.x : lsev;
            lseen |= (u32)__builtin_amdgcn_ballot_w64(lok) & 0xffu;
#pragma unroll
            for (int r = 0; r < 4; ++r) {
                const bool ok = unit_ok(p.t[r], T);
                uo[r].x = ok ? __uint_as_float(p.t[r].x) : uo[r].x; uo[r].y = ok ? __uint_as_float(p.t[r].z) : uo[r].y;
                have |= ok ? 1u << r : 0u;
            }
            const u32 gate = (__builtin_popcount(lseen) >= GATE_N) ? 0xffu : lseen;
            const u32 gl = gate >> half;
            want = ((gl & 1u) | ((gl >> 1) & 2u) | ((gl >> 2) & 4u) | ((gl >> 3) & 8u)) & ~have;
            asm volatile("" : "+v"(uo[0].x), "+v"(uo[0].y), "+v"(uo[1].x), "+v"(uo[1].y), "+v"(uo[2].x), "+v"(uo[2].y), "+v"(uo[3].x), "+v"(uo[3].y),
                              "+v"(lsev), "+v"(have), "+v"(want));
            return lseen == 0xffu && __builtin_amdgcn_ballot_w64(have != 0xfu) == 0ull;
        };
#pragma unroll
        for (int r = 0; r < 4; ++r) uo[r] = f32x2{0.f, 0.f};
        const float lw = xrow_sum(S.l);
        if (n16 < 4) {
            float* mo = (float*)pf_base + n16 * 144 + g * 36;
#pragma unroll
            for (int d = 0; d < 8; ++d) *(f32x4*)(mo + 4 * d) = S.acc[d];
            if (g == 0) *(f32x2*)(lds + L_ML + (wave * 4 + n16) * 8) = f32x2{S.m, lw};
        }
        SB();
        g_issue(p0);
        SB();
        wg_barrier();
        {
            f32x2 ml[4], ov[4];
#pragma unroll
            for (int w = 0; w < 4; ++w) {
                ml[w] = *(const f32x2*)(lds + L_ML + (w * 4 + wave) * 8);
                const float* q = (const float*)(lds + L_PF + w * 32768) + wave * 144 + (lane >> 4) * 36 + 8 * (lane & 3) + ((lane & 15) >> 2);
                ov[w] = f32x2{q[0], q[4]};
            }
            const float M = fmaxf(fmaxf(ml[0].x, ml[1].x), fmaxf(ml[2].x, ml[3].x));
            float L = 0.f, o0 = 0.f, o1 = 0.f;
#pragma unroll
            for (int w = 0; w < 4; ++w) {
                const float a = __builtin_amdgcn_exp2f(ml[w].x - M);
                L += a * ml[w].y; o0 += a * ov[w].x; o1 += a * ov[w].y;
            }
            const float inv = __builtin_amdgcn_rcpf(L);
            const u32 blk = (u32)((b * 32 + ahkv * 4 + wave) * 8 + split);
            st_unit(rs, WS_PO + blk * 1024 + lane * 16, mk_unit(__float_as_uint(o0 * inv), __float_as_uint(o1 * inv), T));
            if (lane == 0) {
                const float lse = M + __builtin_amdgcn_logf(L);
                st_unit(rs, WS_PL + blk * 16, mk_unit(__float_as_uint(lse), 0u, T));
            }
        }
        STAMP(4);
        {
            SB();
#pragma unroll 1
            for (u32 spin = 0; spin < SPIN_CAP; ++spin) {
                asm volatile("" ::: "memory");
                if (g_eval(p0)) { STAMPV(14, 1000000ull + spin); break; }
                __builtin_amdgcn_s_sleep(GK);
                g_issue(p0);
            }
            const float lse = __uint_as_float(lsev);
            float M = fmaxf(lse, dppf<XOR1>(lse)); M = fmaxf(M, dppf<XOR2>(M)); M = fmaxf(M, dppf<HMIR>(M));
            const float al = __builtin_amdgcn_exp2f(lse - M);
            float W = al + dppf<XOR1>(al); W += dppf<XOR2>(W); W += dppf<HMIR>(W);
            const float inv = __builtin_amdgcn_rcpf(W);
            const u32 aw = __float_as_uint(al * inv);
            float aa[4];
#pragma unroll
            for (int r = 0; r < 4; ++r) {
                const u32 e = __builtin_amdgcn_readlane(aw, 2 * r), o = __builtin_amdgcn_readlane(aw, 2 * r + 1);
                aa[r] = __uint_as_float(half ? o : e);
            }
            const float a0 = aa[0], a1 = aa[1], a2 = aa[2], a3 = aa[3];
            float o0 = (a0 * uo[0].x + a1 * uo[1].x) + (a2 * uo[2].x + a3 * uo[3].x);
            float o1 = (a0 * uo[0].y + a1 * uo[1].y) + (a2 * uo[2].y + a3 * uo[3].y);
            o0 = x32_sum(o0); o1 = x32_sum(o1);
            if (lane < 32) *(u32*)(lds + L_O + wave * 128 + lane * 4) = pack2(o0, o1);
        }
        STAMP(5);
        wg_barrier();
        if (wave == FINW) {
            const int fb = j >> 6, fng = j & 63;
            const int fu = lane & 3, fi = lane >> 2;
            const u32 off = WS_SP + (u32)(((fb * 64 + fng) * 64 + fi) * 64 + fu * 16);
            f32x2 u[4];
#pragma unroll
            for (int r = 0; r < 4; ++r) u[r] = f32x2{0.f, 0.f};
            u32 hv = 0;
            auto f_issue = [&](u32x4 (&t)[4]) {
                SB();
#pragma unroll
                for (int r = 0; r < 4; ++r) t[r] = ld_unit(rs, ((hv >> r) & 1u) ? OOB : off + r * 1024);
                SB();
            };
            auto f_eval = [&](const u32x4 (&t)[4]) -> bool {
                SB();
#pragma unroll
                for (int r = 0; r < 4; ++r) {
                    const bool ok = unit_ok(t[r], T);
                    u[r].x = ok ? __uint_as_float(t[r].x) : u[r].x; u[r].y = ok ? __uint_as_float(t[r].z) : u[r].y;
                    hv |= ok ? 1u << r : 0u;
                }
                asm volatile("" : "+v"(u[0].x), "+v"(u[0].y), "+v"(u[1].x), "+v"(u[1].y), "+v"(u[2].x), "+v"(u[2].y), "+v"(u[3].x), "+v"(u[3].y), "+v"(hv));
                return __builtin_amdgcn_ballot_w64(hv != 0xfu) == 0ull;
            };
            const u32x4 fin = *(const u32x4*)(lds + L_FIN + fu * 16);
            u32x4 t0[4], t1[4], t2[4];
            f_issue(t0);
#pragma unroll 1
            for (u32 spin = 0; spin < SPIN_CAP; ++spin) {
                asm volatile("" ::: "memory");
                if (f_eval(t0)) break;
                f_issue(t0);
                __builtin_amdgcn_s_sleep(PK);
            }
            float a0 = (u[0].x + u[1].x) + (u[2].x + u[3].x);
            float a1 = (u[0].y + u[1].y) + (u[2].y + u[3].y);
            a0 += dppf<ROR4>(a0); a0 += dppf<ROR8>(a0); a0 = xrow_sum(a0);
            a1 += dppf<ROR4>(a1); a1 += dppf<ROR8>(a1); a1 = xrow_sum(a1);
            const float r0 = bflo(fin.z) + bfr(bfr(a0) * __uint_as_float(fin.x)), r1 = bfhi(fin.z) + bfr(bfr(a1) * __uint_as_float(fin.y));
            if (lane < 4) *(GAS u32*)((GAS char*)P.out + fb * 1024 + fng * 16 + fu * 4) = pack2(r0, r1);
            if (j == 0 && lane == 0) *(GAS u32*)((GAS char*)P.ws + WS_EPOCH) = T;
            DBG(STAMP(14);)
        }
        FLUSH_STAMPS();
    } else {
        __builtin_amdgcn_s_setprio(SVCPRIO);
        u32 tch[4] = {0, 0, 0, 0};
        auto touch = [&]() {
            const int fb = j >> 6, fng = j & 63;
            SB();
            if (TOUCH & 1) tch[0] = *(const GAS u32*)((gcchar*)P.wo + (size_t)(oh * 128 + 64 * okh + lane) * 1024 + 256 * ons);
            if (TOUCH & 2) tch[1] = __builtin_amdgcn_raw_buffer_load_b32(rs, WS_SP + (u32)(((fb * 64 + fng) * 64 + lane) * 64), 0, 0);
            if (TOUCH & 4) tch[2] = __builtin_amdgcn_raw_buffer_load_b32(rs, WS_SP + (u32)((((lane >> 4) * 64 + 16 * ons + (lane & 15)) * 64 + oh * 2 + okh) * 64), 0, 0);
            if (TOUCH & 8) tch[3] = *(const GAS u32*)((gcchar*)P.out + fb * 1024 + fng * 16);
            SB();
        };
        u32 T = epoch + 1u; if (T == 0u) T = 1u;
        T = __builtin_amdgcn_readfirstlane(T);
        if (lane == 0) *(u32*)(lds + L_T) = T;
        if (pre) {
        DBG(STAMP(8);)

        {
            float s = 0.f;
#pragma unroll
            for (int i = 0; i < 4; ++i)
#pragma unroll
                for (int w = 0; w < 4; ++w) s = dot2(A.r[i][w], 0x3f803f80u, s);
            s += dppf<XOR1>(s); s += dppf<XOR2>(s); s += dppf<ROR4>(s); s += dppf<ROR8>(s);
            const float mean = s * (1.0f / 512.0f);
            f32x2 q2 = {0.f, 0.f};
#pragma unroll
            for (int i = 0; i < 4; ++i)
#pragma unroll
                for (int w = 0; w < 4; ++w) {
                    const f32x2 d = f32x2{bflo(A.r[i][w]), bfhi(A.r[i][w])} - mean;
                    q2 += d * d;
                }
            float q = q2.x + q2.y;
            q += dppf<XOR1>(q); q += dppf<XOR2>(q); q += dppf<ROR4>(q); q += dppf<ROR8>(q);
            const float var = bfr(q * (1.0f / 512.0f));
            const float mb = bfr(mean);
            const float rden = 1.0f / bfr(__builtin_sqrtf(bfr(var + 1e-5f)));
#pragma unroll
            for (int i = 0; i < 4; ++i) {
                u32x4 o;
#pragma unroll
                for (int w = 0; w < 4; ++w) {
                    const f32x2 d = f32x2{bflo(A.r[i][w]), bfhi(A.r[i][w])} - mb;
                    const u32 c = pack2(d.x, d.y);
                    const f32x2 e = f32x2{bflo(c), bfhi(c)} * rden;
                    o[w] = pack2(e.x, e.y);
                }
                *(u32x4*)(lds + L_XN + g * 1024 + i * 256 + n16 * 16) = o;
            }
        }
        asm volatile("" ::: "memory");
        if (lane == 0) lds_st(lds + L_XF, 1u);
        asm volatile("" ::: "memory");
        }
        u32x4 wa[2][8];
        u32x4 wo[16];
        auto issue_wo = [&]() {
            gcchar* wob = (gcchar*)P.wo + (size_t)(oh * 128 + 64 * okh) * 1024 + 256 * ons;
            const u32 wooff = (u32)(8 * g * 1024 + n16 * 16);
#pragma unroll
            for (int i = 0; i < 16; ++i) wo[i] = ldnt(wob + wooff + ((i >> 3) * 32 + (i & 7)) * 1024);
        };
        auto perm_wo = [&]() {
#pragma unroll
            for (int tau = 0; tau < 2; ++tau) {
                wa[tau][0] = tfrag<0>(&wo[8 * tau]); wa[tau][1] = tfrag<1>(&wo[8 * tau]); wa[tau][2] = tfrag<2>(&wo[8 * tau]); wa[tau][3] = tfrag<3>(&wo[8 * tau]);
                wa[tau][4] = tfrag<4>(&wo[8 * tau]); wa[tau][5] = tfrag<5>(&wo[8 * tau]); wa[tau][6] = tfrag<6>(&wo[8 * tau]); wa[tau][7] = tfrag<7>(&wo[8 * tau]);
            }
        };
        const int fb = j >> 6, fng = j & 63;
        const int fu = lane & 3, fi = lane >> 2;
        const u32 xw = *(const GAS u32*)((gcchar*)P.x + fb * 1024 + fng * 16 + fu * 4);
        const u32 b2w = *(const GAS u32*)((gcchar*)P.b2 + fng * 16 + fu * 4);
        STAMP(1);
        {
            u32x4 u0, u1;
            const u32 off = WS_Q + (u32)(grp * 2048 + lane * 16);
            if (pre) __builtin_amdgcn_s_sleep(QPRE_X); else __builtin_amdgcn_s_sleep(QPRE_P);
#pragma unroll 1
            for (u32 spin = 0; spin < SPIN_CAP; ++spin) {
                asm volatile("" ::: "memory");
                u0 = ld_unit(rs, off); u1 = ld_unit(rs, off + 1024);
                DBG(if (spin == 0) { asm volatile("s_waitcnt vmcnt(0)" ::: "memory"); STAMP(10); })
                if (__builtin_amdgcn_ballot_w64(!(unit_ok(u0, T) & unit_ok(u1, T))) == 0ull) { STAMPV(11, 1000000ull + spin); break; }
                __builtin_amdgcn_s_sleep(QINT);
            }
            *(u32x2*)(lds + L_Q + lane * 8) = u32x2{u0.x, u0.z};
            *(u32x2*)(lds + L_Q + 512 + lane * 8) = u32x2{u1.x, u1.z};
        }
        STAMP(3);
        asm volatile("" ::: "memory");
        if (lane == 0) lds_st(lds + L_QF, 1u);
        asm volatile("" ::: "memory");
        auto pin_wa = [&]() {
#pragma unroll
            for (int tau = 0; tau < 2; ++tau)
#pragma unroll
                for (int d = 0; d < 8; ++d) asm volatile("" : "+v"(wa[tau][d]));
        };
        auto gate = [&]() {
            const u32 off = WS_MP + (u32)(((fb * 64 + fng) * 32 + fi) * 64 + fu * 16);
            const bool has1 = fi + 16 < NMLP;
            u32x4 u0 = {0, 0, 0, 0}, u1 = {0, 0, 0, 0};
#pragma unroll 1
            for (u32 spin = 0; spin < SPIN_CAP; ++spin) {
                asm volatile("" ::: "memory");
                u0 = __builtin_amdgcn_raw_buffer_load_b128(rs, off, 0, GAUX); u1 = __builtin_amdgcn_raw_buffer_load_b128(rs, off + 1024, 0, GAUX);
                if (__builtin_amdgcn_ballot_w64(!(unit_ok(u0, T) & (unit_ok(u1, T) | !has1))) == 0ull) break;
                __builtin_amdgcn_s_sleep(2);
            }
            float a0 = __uint_as_float(u0.x) + (has1 ? __uint_as_float(u1.x) : 0.f);
            float a1 = __uint_as_float(u0.z) + (has1 ? __uint_as_float(u1.z) : 0.f);
            a0 += dppf<ROR4>(a0); a0 += dppf<ROR8>(a0); a0 = xrow_sum(a0);
            a1 += dppf<ROR4>(a1); a1 += dppf<ROR8>(a1); a1 = xrow_sum(a1);
            const float y0 = bfr(bfr(a0) + bflo(b2w)), y1 = bfr(bfr(a1) + bfhi(b2w));
            const float g0 = bfr(y0 / (1.0f + __expf(-y0)));
            const float g1 = bfr(y1 / (1.0f + __expf(-y1)));
            if (lane < 4) *(u32x4*)(lds + L_FIN + lane * 16) = u32x4{__float_as_uint(g0), __float_as_uint(g1), xw, 0u};
        };
        STAMP(4);
        wg_barrier();
        {
            __builtin_amdgcn_s_sleep(ORD2);
            asm volatile("" ::: "memory");
            const u32 goff = WS_MP + (u32)(((fb * 64 + fng) * 32 + fi) * 64 + fu * 16);
            const bool has1 = fi + 16 < NMLP;
            SB();
            u32x4 m0 = ld_unit(rs, goff), m1 = ld_unit(rs, goff + 1024);
            SB();
            issue_wo();
            SB();
            touch();
            SB();
            if (__builtin_amdgcn_ballot_w64(!(unit_ok(m0, T) & (unit_ok(m1, T) | !has1))) != 0ull) {
#pragma unroll 1
                for (u32 spin = 0; spin < SPIN_CAP; ++spin) {
                    __builtin_amdgcn_s_sleep(2);
                    asm volatile("" ::: "memory");
                    m0 = ld_unit(rs, goff); m1 = ld_unit(rs, goff + 1024);
                    if (__builtin_amdgcn_ballot_w64(!(unit_ok(m0, T) & (unit_ok(m1, T) | !has1))) == 0ull) break;
                }
            }
            float a0 = __uint_as_float(m0.x) + (has1 ? __uint_as_float(m1.x) : 0.f);
            float a1 = __uint_as_float(m0.z) + (has1 ? __uint_as_float(m1.z) : 0.f);
            a0 += dppf<ROR4>(a0); a0 += dppf<ROR8>(a0); a0 = xrow_sum(a0);
            a1 += dppf<ROR4>(a1); a1 += dppf<ROR8>(a1); a1 = xrow_sum(a1);
            const float y0 = bfr(bfr(a0) + bflo(b2w)), y1 = bfr(bfr(a1) + bfhi(b2w));
            const float g0 = bfr(y0 / (1.0f + __expf(-y0)));
            const float g1 = bfr(y1 / (1.0f + __expf(-y1)));
            if (lane < 4) *(u32x4*)(lds + L_FIN + lane * 16) = u32x4{__float_as_uint(g0), __float_as_uint(g1), xw, 0u};
            DBG(STAMP(7);)
            SB();
            perm_wo();
            pin_wa();
        }
        DBG(STAMP(9);)
        wg_barrier();
        STAMP(5);
        {
            f32x4 acc[8];
#pragma unroll
            for (int d = 0; d < 8; ++d) acc[d] = f32x4{0.f, 0.f, 0.f, 0.f};
#pragma unroll
            for (int tau = 0; tau < 2; ++tau) {
                const u32x4 of = *(const u32x4*)(lds + L_O + (n16 >> 2) * 128 + (32 * tau + 8 * g) * 2);
#pragma unroll
                for (int d = 0; d < 8; ++d) acc[d] = mfma(wa[tau][d], of, acc[d]);
            }
            publish_cols<64>(rs, WS_SP, acc, oh * 2 + okh, 16 * ons, lane, T);
        }
        asm volatile("" :: "v"(tch[0]), "v"(tch[1]), "v"(tch[2]), "v"(tch[3]));
        STAMP(6);
        FLUSH_STAMPS();
    }
}

extern "C" void kernel_launch(void* const* d_in, const int* in_sizes, int n_in,
                              void* d_out, int out_size, void* d_ws, size_t ws_size,
                              hipStream_t stream) {
    Params P;
    P.x = d_in[0]; P.k = d_in[1]; P.v = d_in[2]; P.wq = d_in[3]; P.wo = d_in[4];
    P.w1 = d_in[6]; P.b1 = d_in[7]; P.w2 = d_in[8]; P.b2 = d_in[9];
    P.out = d_out; P.ws = d_ws;
    fused_decode_kernel<<<256, 320, 0, stream>>>(P);
}
```

```cpp
#include <hip/hip_runtime.h>
#include <hip/hip_bf16.h>
#include <stdint.h>

typedef unsigned int u32;
typedef u32 u32x4 __attribute__((ext_vector_type(4)));
typedef u32 u32x2 __attribute__((ext_vector_type(2)));
typedef float f32x4 __attribute__((ext_vector_type(4)));
typedef float f32x2 __attribute__((ext_vector_type(2)));
typedef __bf16 bf16x8 __attribute__((ext_vector_type(8)));
typedef __bf16 bf16x2 __attribute__((ext_vector_type(2)));
#define GAS __attribute__((address_space(1)))
typedef GAS const u32x4 gcu32x4;
typedef GAS const char gcchar;
#define LAS __attribute__((address_space(3)))
__device__ __forceinline__ unsigned int lds_ld(const char* p) { return *(volatile LAS const unsigned int*)(LAS const char*)p; }
__device__ __forceinline__ void lds_st(char* p, unsigned int v) { *(volatile LAS unsigned int*)(LAS char*)p = v; }

constexpr int NB = 4, DM = 512, HKV = 8, SK = 16384, DH = 128, FF = 1792;
constexpr int NMLP = 28;
constexpr u32 WS_EPOCH = 0;
constexpr u32 WS_Q  = 0x1000;
constexpr u32 WS_PL = 0x20000;
constexpr u32 WS_MP = 0x30000;
constexpr u32 WS_PO = 0x100000;
constexpr u32 WS_SP = 0x200000;
constexpr u32 WS_END = 0x400000;
constexpr u32 SPIN_CAP = 1u << 17;
constexpr int L_Q   = 0;
constexpr int L_T   = 1024;
constexpr int L_QF  = 1040;
constexpr int L_XF  = 1044;
constexpr int L_BC  = 1048;
constexpr int L_FIN = 1088;
constexpr int L_XN  = 2048;
constexpr int L_HP  = 6144;
constexpr int L_H   = 14336;
constexpr int L_ML  = 14848;
constexpr int L_O   = 15104;
constexpr int L_PF  = 18432;
constexpr int LDS_BYTES = L_PF + 4 * 32768;

struct Params {
    const void* x; const void* k; const void* v; const void* wq; const void* wo;
    const void* w1; const void* b1; const void* w2; const void* b2;
    void* out; void* ws;
};

__device__ __forceinline__ u32x4 ldnt(gcchar* p) { return __builtin_nontemporal_load((gcu32x4*)p); }
__device__ __forceinline__ float bflo(u32 w) { return __uint_as_float(w << 16); }
__device__ __forceinline__ float bfhi(u32 w) { return __uint_as_float(w & 0xffff0000u); }
__device__ __forceinline__ u32 pack2(float lo, float hi) {
    f32x2 v = {lo, hi};
    return __builtin_bit_cast(u32, __builtin_convertvector(v, bf16x2));
}
__device__ __forceinline__ float bfr(float x) { return bflo(pack2(x, 0.f)); }
__device__ __forceinline__ f32x4 mfma(u32x4 a, u32x4 b, f32x4 c) {
    return __builtin_amdgcn_mfma_f32_16x16x32_bf16(__builtin_bit_cast(bf16x8, a), __builtin_bit_cast(bf16x8, b), c, 0, 0, 0);
}
__device__ __forceinline__ float dot2(u32 a, u32 b, float c) {
    return __builtin_amdgcn_fdot2_f32_bf16(__builtin_bit_cast(bf16x2, a), __builtin_bit_cast(bf16x2, b), c, false);
}
template <int CTRL> __device__ __forceinline__ u32 dppu(u32 x) { return (u32)__builtin_amdgcn_mov_dpp((int)x, CTRL, 0xf, 0xf, true); }
template <int CTRL> __device__ __forceinline__ float dppf(float x) { return __uint_as_float(dppu<CTRL>(__float_as_uint(x))); }
constexpr int XOR1 = 0xB1, XOR2 = 0x4E, HMIR = 0x141, ROR4 = 0x124, ROR8 = 0x128;
__device__ __forceinline__ float xrow_max(float x) {
    auto s = __builtin_amdgcn_permlane16_swap(__float_as_uint(x), __float_as_uint(x), false, false);
    x = fmaxf(__uint_as_float(s[0]), __uint_as_float(s[1]));
    auto t = __builtin_amdgcn_permlane32_swap(__float_as_uint(x), __float_as_uint(x), false, false);
    return fmaxf(__uint_as_float(t[0]), __uint_as_float(t[1]));
}
__device__ __forceinline__ float xrow_sum(float x) {
    auto s = __builtin_amdgcn_permlane16_swap(__float_as_uint(x), __float_as_uint(x), false, false);
    x = __uint_as_float(s[0]) + __uint_as_float(s[1]);
    auto t = __builtin_amdgcn_permlane32_swap(__float_as_uint(x), __float_as_uint(x), false, false);
    return __uint_as_float(t[0]) + __uint_as_float(t[1]);
}
__device__ __forceinline__ float x32_sum(float x) {
    auto t = __builtin_amdgcn_permlane32_swap(__float_as_uint(x), __float_as_uint(x), false, false);
    return __uint_as_float(t[0]) + __uint_as_float(t[1]);
}
__device__ __forceinline__ void wg_barrier() { asm volatile("s_waitcnt lgkmcnt(0)\n\ts_barrier" ::: "memory"); }
__device__ __forceinline__ void quad_barrier(char* cnt, u32 target, int lane) {
    asm volatile("" ::: "memory");
    if (lane == 0) __hip_atomic_fetch_add((LAS unsigned int*)(LAS char*)cnt, 1u, __ATOMIC_RELAXED, __HIP_MEMORY_SCOPE_WORKGROUP);
#pragma unroll 1
    for (u32 spin = 0; spin < SPIN_CAP; ++spin) {
        asm volatile("" ::: "memory");
        if (lds_ld(cnt) >= target) break;
    }
    asm volatile("" ::: "memory");
}
__device__ __forceinline__ void wait_flag(const char* f) {
#pragma unroll 1
    for (u32 spin = 0; spin < SPIN_CAP; ++spin) {
        asm volatile("" ::: "memory");
        if (lds_ld(f) != 0u) break;
        __builtin_amdgcn_s_sleep(1);
    }
    asm volatile("" ::: "memory");
}
constexpr u32 OOB = 0x40000000u;

__device__ __forceinline__ u32x4 mk_unit(u32 v0, u32 v1, u32 T) { u32x4 u = {v0, T ^ v1, v1, T ^ v0}; return u; }
__device__ __forceinline__ bool unit_ok(u32x4 u, u32 T) { return ((u.y ^ u.z) == T) & ((u.w ^ u.x) == T); }
__device__ __forceinline__ void st_unit(__amdgpu_buffer_rsrc_t rs, u32 off, u32x4 u) { __builtin_amdgcn_raw_buffer_store_b128(u, rs, off, 0, 16); }
__device__ __forceinline__ u32x4 ld_unit(__amdgpu_buffer_rsrc_t rs, u32 off) { return __builtin_amdgcn_raw_buffer_load_b128(rs, off, 0, 16); }

template <int DD> __device__ __forceinline__ u32x4 tfrag(const u32x4* v) {
    constexpr u32 sel = (DD & 1) ? 0x07060302u : 0x05040100u;
    u32x4 a;
    a.x = __builtin_amdgcn_perm(v[1][DD >> 1], v[0][DD >> 1], sel);
    a.y = __builtin_amdgcn_perm(v[3][DD >> 1], v[2][DD >> 1], sel);
    a.z = __builtin_amdgcn_perm(v[5][DD >> 1], v[4][DD >> 1], sel);
    a.w = __builtin_amdgcn_perm(v[7][DD >> 1], v[6][DD >> 1], sel);
    return a;
}
__device__ __forceinline__ void pv_tile(const u32x4* v, u32x4 p, f32x4 (&acc)[8]) {
    acc[0] = mfma(tfrag<0>(v), p, acc[0]); acc[1] = mfma(tfrag<1>(v), p, acc[1]);
    acc[2] = mfma(tfrag<2>(v), p, acc[2]); acc[3] = mfma(tfrag<3>(v), p, acc[3]);
    acc[4] = mfma(tfrag<4>(v), p, acc[4]); acc[5] = mfma(tfrag<5>(v), p, acc[5]);
    acc[6] = mfma(tfrag<6>(v), p, acc[6]); acc[7] = mfma(tfrag<7>(v), p, acc[7]);
}
#define MPAUX 16
template <int NSLOT, int AUX = 16>
__device__ __forceinline__ void publish_cols(__amdgpu_buffer_rsrc_t rs, u32 base, const f32x4 (&acc)[8], int slot, int ng0, int lane, u32 T) {
    const int g = lane >> 4, b = (lane >> 2) & 3, rho = lane & 3;
    const u32 off0 = base + (u32)(((b * 64 + ng0 + 4 * g) * NSLOT + slot) * 64 + rho * 16);
#pragma unroll
    for (int r = 0; r < 4; ++r) {
        float a0 = (rho & 2) ? acc[4][r] : acc[0][r], a1 = (rho & 2) ? acc[6][r] : acc[2][r];
        float c0 = (rho & 2) ? acc[5][r] : acc[1][r], c1 = (rho & 2) ? acc[7][r] : acc[3][r];
        float v0 = (rho & 1) ? a1 : a0, v1 = (rho & 1) ? c1 : c0;
        __builtin_amdgcn_raw_buffer_store_b128(mk_unit(__float_as_uint(v0), __float_as_uint(v1), T), rs, off0 + (u32)(r * NSLOT * 64), 0, AUX);
    }
}

struct Ring { u32x4 r[16]; };
typedef __amdgpu_buffer_rsrc_t rsrc_t;
#define DMAMOD "nt"
constexpr int NT = 2;
__device__ __forceinline__ u32x4 ldb_nt(rsrc_t rs, u32 voff, u32 soff) { return __builtin_amdgcn_raw_buffer_load_b128(rs, voff, soff, NT); }
constexpr u32 TSTR = 4 * 8192;
__device__ __forceinline__ constexpr u32 kimm(int i) { return (u32)((i >> 2) * 4096 + (i & 3) * 64); }
__device__ __forceinline__ void issue_k(Ring& R, rsrc_t ks, u32 koff, u32 toff, u32 toff2) {
#pragma unroll
    for (int i = 0; i < 8; ++i) R.r[i] = ldb_nt(ks, koff + (i & 3) * 64, i < 4 ? toff : toff2);
}
__device__ __forceinline__ void issue_v(Ring& R, rsrc_t vs, u32 voff, u32 toff, u32 toff2) {
#pragma unroll
    for (int i = 0; i < 8; ++i) R.r[8 + i] = ldb_nt(vs, voff + (i & 3) * 1024, i < 4 ? toff : toff2);
}
__device__ __forceinline__ void issue_k(Ring& R, rsrc_t ks, u32 koff, u32 toff) { issue_k(R, ks, koff, toff, toff + 4096); }
__device__ __forceinline__ void issue_v(Ring& R, rsrc_t vs, u32 voff, u32 toff) { issue_v(R, vs, voff, toff, toff + 4096); }
__device__ __forceinline__ void dma16(u32 ldst, u32 voff, u32x4 srd, u32 soff) {
    u32 keep;
    asm volatile("s_mov_b32 %0, m0\n\ts_mov_b32 m0, %1\n\ts_nop 0\n\tbuffer_load_dwordx4 %2, %3, %4 offen " DMAMOD " lds\n\ts_mov_b32 m0, %0"
                 : "=&s"(keep) : "s"(ldst), "v"(voff), "s"(srd), "s"(soff) : "memory");
}
__device__ __forceinline__ u32x4 mk_srd(const void* base, u32 bytes) {
    const uint64_t a = (uint64_t)base;
    u32x4 d = {(u32)__builtin_amdgcn_readfirstlane((u32)a), (u32)__builtin_amdgcn_readfirstlane((u32)(a >> 32) & 0xffffu), bytes, 0x00020000u};
    return d;
}
#define SB() __builtin_amdgcn_sched_barrier(0)
#define QINT 16
#define PACE_N 7
#define STR2(x) #x
#define STR(x) STR2(x)
#define PACE() asm volatile("s_waitcnt vmcnt(" STR(PACE_N) ")" ::: "memory")
#define LPACE_N 7
#define LPACE() asm volatile("s_waitcnt vmcnt(" STR(LPACE_N) ")" ::: "memory")
#define PB 1
#define SVCPRIO 3

#define DQ 32
#define NP 528
#define QR0 2
#define QR1 5
#define NL 1
#define QPRE_P 100
#define QPRE_X 60
#define XAUX 0
#define QVP 63
#define GK 1
#define GATE_N 6
#define PK 1
#define FINW 0
#define TOUCH 14
#define ORD2 6
#define CBX 0
#define NP2 520
#define NP1 532
#define D3 20
#define XADJ 1, -1, 2, 0, 1, -2, 0, -1
constexpr u32 xadj_cum() {
    const int a[8] = {XADJ};
    u32 r = 0; int c = 0;
    for (int i = 0; i < 8; ++i) { r |= (u32)(c + 8) << (4 * i); c += a[i]; }
    return c == 0 ? r : 0xffffffffu;
}
constexpr u32 XCUM = xadj_cum();
static_assert(XCUM != 0xffffffffu, "adjustments sum to zero");
#define RADJ 1, 1, -1, 1, 1, -1, 0, -2
#define RADJ2 1, 0, -1, 0, 0, -1, 1, 0
#define RADJ1 1, 0, -1, 0, 0, -1, 1, 0
constexpr u32 radj_cum(int which) {
    const int a[8] = {RADJ}, a1[8] = {RADJ1}, a2[8] = {RADJ2};
    u32 r = 0; int c = 0;
    for (int i = 0; i < 8; ++i) { r |= (u32)(c + 8) << (4 * i); c += which == 2 ? a2[i] : which == 1 ? a1[i] : a[i]; }
    return c == 0 ? r : 0xffffffffu;
}
constexpr u32 RCUM = radj_cum(0), RCUM1 = radj_cum(1), RCUM2 = radj_cum(2);
static_assert(RCUM != 0xffffffffu && RCUM1 != 0xffffffffu && RCUM2 != 0xffffffffu, "adjustments sum to zero");
static_assert(NP % 4 == 0 && NP2 % 4 == 0 && NP1 % 4 == 0 && DQ % 4 == 0 && D3 % 4 == 0 && 4096 - 7 * NP + 2 * DQ > 160 && 4096 - 7 * NP1 + 2 * DQ + D3 > 160, "key split");
struct AttnState { f32x4 acc[8]; float m, l; };

__device__ __forceinline__ void qk_tile(const u32x4* k, const u32x4 (&qf)[4], f32x4& s0, f32x4& s1) {
    s0 = f32x4{0.f, 0.f, 0.f, 0.f}; s1 = f32x4{0.f, 0.f, 0.f, 0.f};
#pragma unroll
    for (int st = 0; st < 4; ++st) { s0 = mfma(k[st], qf[st], s0); s1 = mfma(k[4 + st], qf[st], s1); }
}
template <bool MASKED = false>
__device__ __forceinline__ u32x4 softmax_step(const f32x4 s0, const f32x4 s1, AttnState& S, int nj = 8) {
    constexpr float SCL = 0.08838834764831845f, LOG2E = 1.4426950408889634f;
    float z[8];
#pragma unroll
    for (int j = 0; j < 2; ++j) {
        const u32 a = pack2(s0[2 * j], s0[2 * j + 1]), c = pack2(s1[2 * j], s1[2 * j + 1]);
        const u32 a2 = pack2(bflo(a) * SCL, bfhi(a) * SCL), c2 = pack2(bflo(c) * SCL, bfhi(c) * SCL);
        z[2 * j] = bflo(a2); z[2 * j + 1] = bfhi(a2); z[4 + 2 * j] = bflo(c2); z[4 + 2 * j + 1] = bfhi(c2);
    }
    if (MASKED) {
#pragma unroll
        for (int j = 0; j < 8; ++j) z[j] = (j < nj) ? z[j] : -INFINITY;
    }
    float mx = fmaxf(fmaxf(fmaxf(z[0], z[1]), fmaxf(z[2], z[3])), fmaxf(fmaxf(z[4], z[5]), fmaxf(z[6], z[7])));
    mx = xrow_max(mx) * LOG2E;
    if (__builtin_amdgcn_ballot_w64(mx > S.m) != 0ull) {
        const float mn = fmaxf(S.m, mx);
        const float al = __builtin_amdgcn_exp2f(S.m - mn);
        S.m = mn; S.l *= al;
        f32x2 al2 = {al, al};
        asm volatile("" : "+v"(al2));
        const f32x4 al4 = {al2.x, al2.y, al2.x, al2.y};
#pragma unroll
        for (int d = 0; d < 8; ++d) S.acc[d] *= al4;
    }
    float p[8];
#pragma unroll
    for (int j = 0; j < 8; ++j) p[j] = __builtin_amdgcn_exp2f(__builtin_fmaf(z[j], LOG2E, -S.m));
    S.l += ((p[0] + p[1]) + (p[2] + p[3])) + ((p[4] + p[5]) + (p[6] + p[7]));
    u32x4 pf = {pack2(p[0], p[1]), pack2(p[2], p[3]), pack2(p[4], p[5]), pack2(p[6], p[7])};
    return pf;
}
__device__ __forceinline__ void proc_tile(Ring& R, AttnState& S, const u32x4 (&qf)[4], rsrc_t ks, rsrc_t vs, u32 koff, u32 voff, u32 toff) {
    f32x4 s0, s1;
    qk_tile(&R.r[0], qf, s0, s1);
    SB();
    issue_k(R, ks, koff, toff);
    SB();
    const u32x4 pf = softmax_step(s0, s1, S);
    pv_tile(&R.r[8], pf, S.acc);
    SB();
    issue_v(R, vs, voff, toff);
    SB();
}
__device__ __forceinline__ void proc_last(Ring& R, AttnState& S, const u32x4 (&qf)[4]) {
    f32x4 s0, s1;
    qk_tile(&R.r[0], qf, s0, s1);
    const u32x4 pf = softmax_step(s0, s1, S);
    pv_tile(&R.r[8], pf, S.acc);
}
__device__ __forceinline__ void proc_part(Ring& R, AttnState& S, const u32x4 (&qf)[4], int nj) {
    f32x4 s0, s1;
    qk_tile(&R.r[0], qf, s0, s1);
    const u32x4 pf = softmax_step<true>(s0, s1, S, nj);
    pv_tile(&R.r[8], pf, S.acc);
}
__device__ __forceinline__ void proc_lds(const char* sl, AttnState& S, const u32x4 (&qf)[4]) {
    u32x4 c[8];
#pragma unroll
    for (int i = 0; i < 8; ++i) c[i] = *(const u32x4*)(sl + i * 1024);
    f32x4 s0, s1;
    qk_tile(c, qf, s0, s1);
    SB();
#pragma unroll
    for (int i = 0; i < 8; ++i) c[i] = *(const u32x4*)(sl + 8192 + i * 1024);
    const u32x4 pf = softmax_step(s0, s1, S);
    pv_tile(c, pf, S.acc);
}

__global__ void __launch_bounds__(320, 2) fused_decode_kernel(Params P) {
    __shared__ __attribute__((aligned(16))) char lds[LDS_BYTES];
    const int tid = threadIdx.x, lane = tid & 63;
    const int wave = __builtin_amdgcn_readfirstlane(tid >> 6);
    const int j = blockIdx.x;
    const int grp = j >> 3, split = j & 7;
    const int b = grp >> 3, ahkv = grp & 7;
    const int rank = (split - ahkv - 1) & 7;
    const bool grp_mlp = grp < NMLP;
    int rk = rank;
    asm volatile("" : "+s"(rk));
    int f_q = ((1 << QR0 | 1 << QR1) >> rk) & 1;
    int f_m = (((grp - NMLP) >> 24 & 0x80) >> rk) & 1;
    int f_q1 = (rk >> 2) & f_q;
    static_assert(QR0 < 4 && QR1 >= 4, "rank bit 2 tells the two q slices apart");
    asm volatile("" : "+s"(f_q), "+s"(f_m), "+s"(f_q1));
    int f_pre = f_q | f_m, f_svc = wave >> 2;
    asm volatile("" : "+s"(f_pre), "+s"(f_svc));
    const bool is_q = f_q != 0, is_mlp = f_m != 0, pre = f_pre != 0;
    const int g = lane >> 4, n16 = lane & 15;
    const int n_plain = b == 1 ? NP1 : grp_mlp ? NP : NP2;
    const int dq1 = b == 1 ? DQ + D3 : DQ;
    const int xc0 = (int)(XCUM >> (4 * ((ahkv + 1) & 7))) & 15, xc1 = (int)(XCUM >> (4 * split)) & 15, xc2 = (int)(XCUM >> (4 * ((split + 1) & 7))) & 15;
    const u32 rcum = b == 1 ? RCUM1 : grp_mlp ? RCUM : RCUM2;
    const int rc1 = (int)(rcum >> (4 * rank)) & 15, rc2 = (int)(rcum >> (4 * ((rank + 1) & 7))) & 15;
    const int nkeys = (rank == 7 ? 4096 + DQ + dq1 - 7 * n_plain : n_plain - f_q * DQ - f_q1 * (dq1 - DQ)) + 4 * (xc2 - xc1 + rc2 - rc1);
    const int nfull = (nkeys - 1) >> 5, nj = (nkeys - 32 * nfull) >> 2;
    const u32 w_start = (u32)(rank * n_plain - (rank > QR0 ? DQ : 0) - (rank > QR1 ? dq1 : 0) + 4 * (xc1 - xc0 + rc1 - 8)) * 1024u;
    const size_t tb = (size_t)grp * (SK * DH * 2);
    constexpr u32 trange = SK * DH * 2;
    const u32 t0 = w_start + (u32)wave * 8192;
    const u32 koff = (u32)((4 * (n16 & 3) + (n16 >> 2)) * 256 + g * 16);
    const u32 voff = (u32)(lane * 16);
    Ring A;
    u32 epoch = 0;
    {
        const u32 mq = 0u - (u32)f_q, mp = 0u - (u32)f_pre, ms = 0u - (u32)f_svc;
        auto sel = [](u32 m, u32 x, u32 y) { return y ^ ((x ^ y) & m); };
        const bool svc = f_svc != 0;
        const u32 wstr = sel(mq, 8192u, (u32)(FF * 2));
        const u32 wcol = sel(mq, (u32)(ahkv * 1024 + ((2 * b + f_q1) ^ CBX) * 128), (u32)(grp * 128));
        const u32 wb = (u32)(128 * wave) * wstr + wcol;
        const u32 nb = sel(mp, 512u * wstr, trange), na = sel(ms, 4096u, nb);
        const u32 wv = (u32)(32 * (n16 >> 3) + 8 * g) * wstr + (u32)((n16 & 7) * 16);
        const u32 va = sel(ms, (u32)(g * 1024 + n16 * 16), sel(mp, wv, koff));
        const u32 vb = sel(mp, wv, voff);
        const u32 sa = sel(ms, sel(mp, 0u, OOB), sel(mp, wb, t0)), sb = sel(mp, wb + 64u * wstr, t0);
        const u32 a1 = sel(ms, 256u, sel(mp, wstr, 64u)), a2 = sel(mp, 4u * wstr, 4096u);
        const u32 b1 = sel(mp, wstr, 1024u);
        SB();
        const uint64_t Mq = (uint64_t)(int64_t)(int)mq, Mp = (uint64_t)(int64_t)(int)mp, Ms = (uint64_t)(int64_t)(int)ms;
        auto sel64 = [](uint64_t m, uint64_t x, uint64_t y) { return y ^ ((x ^ y) & m); };
        const uint64_t wp = sel64(Mq, (uint64_t)P.wq, (uint64_t)P.w1);
        const uint64_t pa = sel64(Ms, (uint64_t)P.x, sel64(Mp, wp, (uint64_t)P.k + tb));
        const uint64_t pb = sel64(Mp, wp, (uint64_t)P.v + tb);
        const rsrc_t ra = __builtin_amdgcn_make_buffer_rsrc((void*)pa, 0, na, 0x00020000);
        const rsrc_t rb = __builtin_amdgcn_make_buffer_rsrc((void*)pb, 0, nb, 0x00020000);
        if (lane < 3) lds_st(lds + L_QF + lane * 4, 0u);
#pragma unroll
        for (int i = 0; i < 4; ++i) A.r[i] = __builtin_amdgcn_raw_buffer_load_b128(ra, va, sa + (u32)i * a1, XAUX);
        epoch = __builtin_amdgcn_raw_buffer_load_b32(__builtin_amdgcn_make_buffer_rsrc(P.ws, 0, 16, 0x00020000), 0, ~ms & OOB, 0);
        SB(); wg_barrier(); SB();
        if (!svc) {
#pragma unroll
            for (int i = 4; i < 16; ++i)
                A.r[i] = i < 8 ? ldb_nt(ra, va, sa + (u32)(i & 3) * a1 + a2) : ldb_nt(rb, vb, sb + (u32)(i & 3) * b1 + (u32)((i - 8) >> 2) * a2);
        }
        SB();
    }
    int wave2 = wave;
    asm volatile("" : "+s"(wave2));
    const int hkv = j & 7, pidx = j >> 3;
    const __amdgpu_buffer_rsrc_t rs = __builtin_amdgcn_make_buffer_rsrc(P.ws, 0, WS_END, 0x00020000);
    const int oh = hkv * 4 + (pidx & 3), okh = (pidx >> 2) & 1, ons = pidx >> 3;

    if (wave2 < 4) {
        auto tof = [&](u32 i) { return t0 + i * TSTR; };
        const u32 m_off = w_start + (u32)nfull * TSTR + (u32)(wave * nj) * 1024u, m_end = m_off + (u32)nj * 1024u;
        const rsrc_t ks = __builtin_amdgcn_make_buffer_rsrc((char*)P.k + tb, 0, trange, 0x00020000);
        const rsrc_t vs = __builtin_amdgcn_make_buffer_rsrc((char*)P.v + tb, 0, trange, 0x00020000);
        char* const pf_base = lds + L_PF + wave * 32768;
        const u32x4 ksd = mk_srd((const char*)P.k + tb, trange), vsd = mk_srd((const char*)P.v + tb, trange);
        const u32 lb = (u32)__builtin_amdgcn_readfirstlane((u32)(uintptr_t)(__attribute__((address_space(3))) char*)pf_base);
        Ring B;
        AttnState S;
        const u32 tB = is_q ? tof(0) : tof(1);
        if (is_mlp) {
            const rsrc_t w2s = __builtin_amdgcn_make_buffer_rsrc((void*)P.w2, 0, FF * DM * 2, 0x00020000);
            const u32 w2b = (u32)(grp * 64 * 1024 + 256 * wave);
            const u32 w2off = (u32)(8 * g * 1024 + n16 * 16);
#pragma unroll
            for (int i = 0; i < 16; ++i) B.r[i] = ldb_nt(w2s, w2off, w2b + ((i >> 3) * 32 + (i & 7)) * 1024);
        } else {
            if (is_q) asm volatile("s_waitcnt vmcnt(" STR(QVP) ")" ::: "memory");
#pragma unroll
            for (int i = 0; i < 8; ++i) { if (!is_q && i % PB == 0) PACE(); B.r[8 + i] = ldb_nt(vs, voff + (i & 3) * 1024, tB + (i >> 2) * 4096); }
        }
        if (pre) {
            const float b1v = is_mlp ? bflo((u32)((const GAS unsigned short*)P.b1)[grp * 64 + lane]) : 0.f;
            wait_flag(lds + L_XF);
#pragma unroll
            for (int d = 0; d < 8; ++d) S.acc[d] = f32x4{0.f, 0.f, 0.f, 0.f};
#pragma unroll
            for (int tau = 0; tau < 2; ++tau) {
                const u32x4 xf = *(const u32x4*)(lds + L_XN + (n16 & 3) * 1024 + (128 * wave + 64 * tau + 32 * ((n16 >> 2) & 1) + 8 * g) * 2);
                pv_tile(&A.r[8 * tau], xf, S.acc);
            }
            if (n16 < 8 && (g >> 1) == (n16 >> 2)) {
                float* hp = (float*)(lds + L_HP) + ((wave * 2 + (n16 >> 2)) * 4 + (n16 & 3)) * 64 + 32 * (g & 1);
#pragma unroll
                for (int r = 0; r < 4; ++r) {
                    *(f32x4*)(hp + 8 * r) = f32x4{S.acc[0][r], S.acc[1][r], S.acc[2][r], S.acc[3][r]};
                    *(f32x4*)(hp + 8 * r + 4) = f32x4{S.acc[4][r], S.acc[5][r], S.acc[6][r], S.acc[7][r]};
                }
            }
            quad_barrier(lds + L_BC, 4u, lane);
            float sum = 0.f;
            {
                const float* hp = (const float*)(lds + L_HP) + wave * 64 + lane;
#pragma unroll
                for (int i = 0; i < 8; ++i) sum += hp[i * 256];
            }
            const u32 T = *(const u32*)(lds + L_T);
            if (is_q) {
                const u32 w01 = pack2(sum, dppf<XOR1>(sum));
                const u32 w23 = dppu<XOR2>(w01);
                if ((lane & 3) == 0)
                    st_unit(rs, WS_Q + (u32)((wave * 8 + ahkv) * 2048 + (16 * ((2 * b + f_q1) ^ CBX) + (lane >> 2)) * 16), mk_unit(w01, w23, T));
            } else {
                float h = bfr(bfr(sum) + b1v);
                h = h > 0.f ? h : 0.f;
                ((unsigned short*)(lds + L_H))[wave * 64 + lane] = (unsigned short)pack2(h * h, 0.f);
            }
            SB();
            const u32 tA = is_q ? tof(1) : tof(0);
            issue_k(A, ks, koff, tA);
            issue_v(A, vs, voff, tA);
            SB();
            if (is_mlp) {
                quad_barrier(lds + L_BC, 8u, lane);
#pragma unroll
                for (int d = 0; d < 8; ++d) S.acc[d] = f32x4{0.f, 0.f, 0.f, 0.f};
#pragma unroll
                for (int tau = 0; tau < 2; ++tau) {
                    const u32x4 hf = *(const u32x4*)(lds + L_H + (n16 >> 2) * 128 + (32 * tau + 8 * g) * 2);
                    pv_tile(&B.r[8 * tau], hf, S.acc);
                }
                publish_cols<32, MPAUX>(rs, WS_MP, S.acc, grp, 16 * wave, lane, T);
                SB();
                issue_v(B, vs, voff, tB);
            }
        }
        SB();
#pragma unroll
        for (int i = 0; i < 8; ++i) { if (i % PB == 0) PACE(); B.r[i] = ldb_nt(ks, koff + (i & 3) * 64, tB + (i >> 2) * 4096); }
#pragma unroll
        for (int t = 0; t < NL; ++t) {
#pragma unroll
            for (int i = 0; i < 8; ++i) { if (i % PB == 0) LPACE(); dma16(lb + t * 16384 + i * 1024, koff, ksd, tof(2 + t) + kimm(i)); }
#pragma unroll
            for (int i = 0; i < 8; ++i) { if (i % PB == 0) LPACE(); dma16(lb + t * 16384 + 8192 + i * 1024, voff, vsd, tof(2 + t) + i * 1024); }
        }
        wait_flag(lds + L_QF);
        u32x4 qf[4];
#pragma unroll
        for (int st = 0; st < 4; ++st) qf[st] = *(const u32x4*)(lds + L_Q + (n16 & 3) * 256 + st * 64 + g * 16);
        const u32 T = *(const u32*)(lds + L_T);
#pragma unroll
        for (int d = 0; d < 8; ++d) S.acc[d] = f32x4{0.f, 0.f, 0.f, 0.f};
        S.m = -INFINITY; S.l = 0.f;
        proc_tile(A, S, qf, ks, vs, koff, voff, tof(2 + NL));
        proc_last(B, S, qf);
#pragma unroll
        for (int t = 0; t < NL; ++t) {
            asm volatile("s_waitcnt vmcnt(16)" ::: "memory");
            proc_lds(pf_base + t * 16384 + lane * 16, S, qf);
        }
#pragma unroll 1
        for (int i = 2 + NL; i < nfull; ++i) {
            const bool last = i + 1 == nfull;
            const u32 nrec = last ? m_end : trange;
            const rsrc_t kc = __builtin_amdgcn_make_buffer_rsrc((char*)P.k + tb, 0, nrec, 0x00020000);
            const rsrc_t vc = __builtin_amdgcn_make_buffer_rsrc((char*)P.v + tb, 0, nrec, 0x00020000);
            proc_tile(A, S, qf, kc, vc, koff, voff, last ? m_off : tof((u32)(i + 1)));
        }
        proc_part(A, S, qf, nj);
        const int half = lane >> 5;
        const u32 blk0 = (u32)((wave * 32 + oh) * 8);
        const u32 ooff = WS_PO + (blk0 + half) * 1024 + (u32)((32 * okh + (lane & 31)) * 16);
        const u32 loff = lane < 8 ? WS_PL + (blk0 + lane) * 16 : OOB;
        struct Pass { u32x4 l, t[4]; };
        Pass p0;
        u32 want = 0xfu, have = 0;
        u32 lseen = 0;
        u32 lsev = 0;
        f32x2 uo[4];
        auto g_issue = [&](Pass& p) {
            SB();
            p.l = ld_unit(rs, loff);
#pragma unroll
            for (int r = 0; r < 4; ++r) p.t[r] = ld_unit(rs, ((want >> r) & 1u) ? ooff + r * 2048 : OOB);
            SB();
        };
        auto g_eval = [&](const Pass& p) -> bool {
            SB();
            const bool lok = unit_ok(p.l, T);
            lsev = lok ? p.l.x : lsev;
            lseen |= (u32)__builtin_amdgcn_ballot_w64(lok) & 0xffu;
#pragma unroll
            for (int r = 0; r < 4; ++r) {
                const bool ok = unit_ok(p.t[r], T);
                uo[r].x = ok ? __uint_as_float(p.t[r].x) : uo[r].x; uo[r].y = ok ? __uint_as_float(p.t[r].z) : uo[r].y;
                have |= ok ? 1u << r : 0u;
            }
            const u32 gate = (__builtin_popcount(lseen) >= GATE_N) ? 0xffu : lseen;
            const u32 gl = gate >> half;
            want = ((gl & 1u) | ((gl >> 1) & 2u) | ((gl >> 2) & 4u) | ((gl >> 3) & 8u)) & ~have;
            asm volatile("" : "+v"(uo[0].x), "+v"(uo[0].y), "+v"(uo[1].x), "+v"(uo[1].y), "+v"(uo[2].x), "+v"(uo[2].y), "+v"(uo[3].x), "+v"(uo[3].y),
                              "+v"(lsev), "+v"(have), "+v"(want));
            return lseen == 0xffu && __builtin_amdgcn_ballot_w64(have != 0xfu) == 0ull;
        };
#pragma unroll
        for (int r = 0; r < 4; ++r) uo[r] = f32x2{0.f, 0.f};
        const float lw = xrow_sum(S.l);
        if (n16 < 4) {
            float* mo = (float*)pf_base + n16 * 144 + g * 36;
#pragma unroll
            for (int d = 0; d < 8; ++d) *(f32x4*)(mo + 4 * d) = S.acc[d];
            if (g == 0) *(f32x2*)(lds + L_ML + (wave * 4 + n16) * 8) = f32x2{S.m, lw};
        }
        SB();
        g_issue(p0);
        SB();
        wg_barrier();
        {
            f32x2 ml[4], ov[4];
#pragma unroll
            for (int w = 0; w < 4; ++w) {
                ml[w] = *(const f32x2*)(lds + L_ML + (w * 4 + wave) * 8);
                const float* q = (const float*)(lds + L_PF + w * 32768) + wave * 144 + (lane >> 4) * 36 + 8 * (lane & 3) + ((lane & 15) >> 2);
                ov[w] = f32x2{q[0], q[4]};
            }
            const float M = fmaxf(fmaxf(ml[0].x, ml[1].x), fmaxf(ml[2].x, ml[3].x));
            float L = 0.f, o0 = 0.f, o1 = 0.f;
#pragma unroll
            for (int w = 0; w < 4; ++w) {
                const float a = __builtin_amdgcn_exp2f(ml[w].x - M);
                L += a * ml[w].y; o0 += a * ov[w].x; o1 += a * ov[w].y;
            }
            const float inv = __builtin_amdgcn_rcpf(L);
            const u32 blk = (u32)((b * 32 + ahkv * 4 + wave) * 8 + split);
            st_unit(rs, WS_PO + blk * 1024 + lane * 16, mk_unit(__float_as_uint(o0 * inv), __float_as_uint(o1 * inv), T));
            if (lane == 0) {
                const float lse = M + __builtin_amdgcn_logf(L);
                st_unit(rs, WS_PL + blk * 16, mk_unit(__float_as_uint(lse), 0u, T));
            }
        }
        {
            SB();
#pragma unroll 1
            for (u32 spin = 0; spin < SPIN_CAP; ++spin) {
                asm volatile("" ::: "memory");
                if (g_eval(p0)) break;
                __builtin_amdgcn_s_sleep(GK);
                g_issue(p0);
            }
            const float lse = __uint_as_float(lsev);
            float M = fmaxf(lse, dppf<XOR1>(lse)); M = fmaxf(M, dppf<XOR2>(M)); M = fmaxf(M, dppf<HMIR>(M));
            const float al = __builtin_amdgcn_exp2f(lse - M);
            float W = al + dppf<XOR1>(al); W += dppf<XOR2>(W); W += dppf<HMIR>(W);
            const float inv = __builtin_amdgcn_rcpf(W);
            const u32 aw = __float_as_uint(al * inv);
            float aa[4];
#pragma unroll
            for (int r = 0; r < 4; ++r) {
                const u32 e = __builtin_amdgcn_readlane(aw, 2 * r), o = __builtin_amdgcn_readlane(aw, 2 * r + 1);
                aa[r] = __uint_as_float(half ? o : e);
            }
            const float a0 = aa[0], a1 = aa[1], a2 = aa[2], a3 = aa[3];
            float o0 = (a0 * uo[0].x + a1 * uo[1].x) + (a2 * uo[2].x + a3 * uo[3].x);
            float o1 = (a0 * uo[0].y + a1 * uo[1].y) + (a2 * uo[2].y + a3 * uo[3].y);
            o0 = x32_sum(o0); o1 = x32_sum(o1);
            if (lane < 32) *(u32*)(lds + L_O + wave * 128 + lane * 4) = pack2(o0, o1);
        }
        wg_barrier();
        if (wave == FINW) {
            const int fb = j >> 6, fng = j & 63;
            const int fu = lane & 3, fi = lane >> 2;
            const u32 off = WS_SP + (u32)(((fb * 64 + fng) * 64 + fi) * 64 + fu * 16);
            f32x2 u[4];
#pragma unroll
            for (int r = 0; r < 4; ++r) u[r] = f32x2{0.f, 0.f};
            u32 hv = 0;
            auto f_issue = [&](u32x4 (&t)[4]) {
                SB();
#pragma unroll
                for (int r = 0; r < 4; ++r) t[r] = ld_unit(rs, ((hv >> r) & 1u) ? OOB : off + r * 1024);
                SB();
            };
            auto f_eval = [&](const u32x4 (&t)[4]) -> bool {
                SB();
#pragma unroll
                for (int r = 0; r < 4; ++r) {
                    const bool ok = unit_ok(t[r], T);
                    u[r].x = ok ? __uint_as_float(t[r].x) : u[r].x; u[r].y = ok ? __uint_as_float(t[r].z) : u[r].y;
                    hv |= ok ? 1u << r : 0u;
                }
                asm volatile("" : "+v"(u[0].x), "+v"(u[0].y), "+v"(u[1].x), "+v"(u[1].y), "+v"(u[2].x), "+v"(u[2].y), "+v"(u[3].x), "+v"(u[3].y), "+v"(hv));
                return __builtin_amdgcn_ballot_w64(hv != 0xfu) == 0ull;
            };
            const u32x4 fin = *(const u32x4*)(lds + L_FIN + fu * 16);
            u32x4 t0[4];
            f_issue(t0);
#pragma unroll 1
            for (u32 spin = 0; spin < SPIN_CAP; ++spin) {
                asm volatile("" ::: "memory");
                if (f_eval(t0)) break;
                f_issue(t0);
                __builtin_amdgcn_s_sleep(PK);
            }
            float a0 = (u[0].x + u[1].x) + (u[2].x + u[3].x);
            float a1 = (u[0].y + u[1].y) + (u[2].y + u[3].y);
            a0 += dppf<ROR4>(a0); a0 += dppf<ROR8>(a0); a0 = xrow_sum(a0);
            a1 += dppf<ROR4>(a1); a1 += dppf<ROR8>(a1); a1 = xrow_sum(a1);
            const float r0 = bflo(fin.z) + bfr(bfr(a0) * __uint_as_float(fin.x)), r1 = bfhi(fin.z) + bfr(bfr(a1) * __uint_as_float(fin.y));
            if (lane < 4) *(GAS u32*)((GAS char*)P.out + fb * 1024 + fng * 16 + fu * 4) = pack2(r0, r1);
            if (j == 0 && lane == 0) *(GAS u32*)((GAS char*)P.ws + WS_EPOCH) = T;
        }
    } else {
        __builtin_amdgcn_s_setprio(SVCPRIO);
        u32 tch[4] = {0, 0, 0, 0};
        auto touch = [&]() {
            const int fb = j >> 6, fng = j & 63;
            SB();
            if (TOUCH & 1) tch[0] = *(const GAS u32*)((gcchar*)P.wo + (size_t)(oh * 128 + 64 * okh + lane) * 1024 + 256 * ons);
            if (TOUCH & 2) tch[1] = __builtin_amdgcn_raw_buffer_load_b32(rs, WS_SP + (u32)(((fb * 64 + fng) * 64 + lane) * 64), 0, 0);
            if (TOUCH & 4) tch[2] = __builtin_amdgcn_raw_buffer_load_b32(rs, WS_SP + (u32)((((lane >> 4) * 64 + 16 * ons + (lane & 15)) * 64 + oh * 2 + okh) * 64), 0, 0);
            if (TOUCH & 8) tch[3] = *(const GAS u32*)((gcchar*)P.out + fb * 1024 + fng * 16);
            SB();
        };
        u32 T = epoch + 1u; if (T == 0u) T = 1u;
        T = __builtin_amdgcn_readfirstlane(T);
        if (lane == 0) *(u32*)(lds + L_T) = T;
        if (pre) {

        {
            float s = 0.f;
#pragma unroll
            for (int i = 0; i < 4; ++i)
#pragma unroll
                for (int w = 0; w < 4; ++w) s = dot2(A.r[i][w], 0x3f803f80u, s);
            s += dppf<XOR1>(s); s += dppf<XOR2>(s); s += dppf<ROR4>(s); s += dppf<ROR8>(s);
            const float mean = s * (1.0f / 512.0f);
            f32x2 q2 = {0.f, 0.f};
#pragma unroll
            for (int i = 0; i < 4; ++i)
#pragma unroll
                for (int w = 0; w < 4; ++w) {
                    const f32x2 d = f32x2{bflo(A.r[i][w]), bfhi(A.r[i][w])} - mean;
                    q2 += d * d;
                }
            float q = q2.x + q2.y;
            q += dppf<XOR1>(q); q += dppf<XOR2>(q); q += dppf<ROR4>(q); q += dppf<ROR8>(q);
            const float var = bfr(q * (1.0f / 512.0f));
            const float mb = bfr(mean);
            const float rden = 1.0f / bfr(__builtin_sqrtf(bfr(var + 1e-5f)));
#pragma unroll
            for (int i = 0; i < 4; ++i) {
                u32x4 o;
#pragma unroll
                for (int w = 0; w < 4; ++w) {
                    const f32x2 d = f32x2{bflo(A.r[i][w]), bfhi(A.r[i][w])} - mb;
                    const u32 c = pack2(d.x, d.y);
                    const f32x2 e = f32x2{bflo(c), bfhi(c)} * rden;
                    o[w] = pack2(e.x, e.y);
                }
                *(u32x4*)(lds + L_XN + g * 1024 + i * 256 + n16 * 16) = o;
            }
        }
        asm volatile("" ::: "memory");
        if (lane == 0) lds_st(lds + L_XF, 1u);
        asm volatile("" ::: "memory");
        }
        u32x4 wa[2][8];
        u32x4 wo[16];
        auto issue_wo = [&]() {
            gcchar* wob = (gcchar*)P.wo + (size_t)(oh * 128 + 64 * okh) * 1024 + 256 * ons;
            const u32 wooff = (u32)(8 * g * 1024 + n16 * 16);
#pragma unroll
            for (int i = 0; i < 16; ++i) wo[i] = ldnt(wob + wooff + ((i >> 3) * 32 + (i & 7)) * 1024);
        };
        auto perm_wo = [&]() {
#pragma unroll
            for (int tau = 0; tau < 2; ++tau) {
                wa[tau][0] = tfrag<0>(&wo[8 * tau]); wa[tau][1] = tfrag<1>(&wo[8 * tau]); wa[tau][2] = tfrag<2>(&wo[8 * tau]); wa[tau][3] = tfrag<3>(&wo[8 * tau]);
                wa[tau][4] = tfrag<4>(&wo[8 * tau]); wa[tau][5] = tfrag<5>(&wo[8 * tau]); wa[tau][6] = tfrag<6>(&wo[8 * tau]); wa[tau][7] = tfrag<7>(&wo[8 * tau]);
            }
        };
        const int fb = j >> 6, fng = j & 63;
        const int fu = lane & 3, fi = lane >> 2;
        const u32 xw = *(const GAS u32*)((gcchar*)P.x + fb * 1024 + fng * 16 + fu * 4);
        const u32 b2w = *(const GAS u32*)((gcchar*)P.b2 + fng * 16 + fu * 4);
        {
            u32x4 u0, u1;
            const u32 off = WS_Q + (u32)(grp * 2048 + lane * 16);
            if (pre) __builtin_amdgcn_s_sleep(QPRE_X); else __builtin_amdgcn_s_sleep(QPRE_P);
#pragma unroll 1
            for (u32 spin = 0; spin < SPIN_CAP; ++spin) {
                asm volatile("" ::: "memory");
                u0 = ld_unit(rs, off); u1 = ld_unit(rs, off + 1024);
                if (__builtin_amdgcn_ballot_w64(!(unit_ok(u0, T) & unit_ok(u1, T))) == 0ull) break;
                __builtin_amdgcn_s_sleep(QINT);
            }
            *(u32x2*)(lds + L_Q + lane * 8) = u32x2{u0.x, u0.z};
            *(u32x2*)(lds + L_Q + 512 + lane * 8) = u32x2{u1.x, u1.z};
        }
        asm volatile("" ::: "memory");
        if (lane == 0) lds_st(lds + L_QF, 1u);
        asm volatile("" ::: "memory");
        auto pin_wa = [&]() {
#pragma unroll
            for (int tau = 0; tau < 2; ++tau)
#pragma unroll
                for (int d = 0; d < 8; ++d) asm volatile("" : "+v"(wa[tau][d]));
        };
        wg_barrier();
        {
            __builtin_amdgcn_s_sleep(ORD2);
            asm volatile("" ::: "memory");
            const u32 goff = WS_MP + (u32)(((fb * 64 + fng) * 32 + fi) * 64 + fu * 16);
            const bool has1 = fi + 16 < NMLP;
            SB();
            u32x4 m0 = ld_unit(rs, goff), m1 = ld_unit(rs, goff + 1024);
            SB();
            issue_wo();
            SB();
            touch();
            SB();
            if (__builtin_amdgcn_ballot_w64(!(unit_ok(m0, T) & (unit_ok(m1, T) | !has1))) != 0ull) {
#pragma unroll 1
                for (u32 spin = 0; spin < SPIN_CAP; ++spin) {
                    __builtin_amdgcn_s_sleep(2);
                    asm volatile("" ::: "memory");
                    m0 = ld_unit(rs, goff); m1 = ld_unit(rs, goff + 1024);
                    if (__builtin_amdgcn_ballot_w64(!(unit_ok(m0, T) & (unit_ok(m1, T) | !has1))) == 0ull) break;
                }
            }
            float a0 = __uint_as_float(m0.x) + (has1 ? __uint_as_float(m1.x) : 0.f);
            float a1 = __uint_as_float(m0.z) + (has1 ? __uint_as_float(m1.z) : 0.f);
            a0 += dppf<ROR4>(a0); a0 += dppf<ROR8>(a0); a0 = xrow_sum(a0);
            a1 += dppf<ROR4>(a1); a1 += dppf<ROR8>(a1); a1 = xrow_sum(a1);
            const float y0 = bfr(bfr(a0) + bflo(b2w)), y1 = bfr(bfr(a1) + bfhi(b2w));
            const float g0 = bfr(y0 / (1.0f + __expf(-y0)));
            const float g1 = bfr(y1 / (1.0f + __expf(-y1)));
            if (lane < 4) *(u32x4*)(lds + L_FIN + lane * 16) = u32x4{__float_as_uint(g0), __float_as_uint(g1), xw, 0u};
            SB();
            perm_wo();
            pin_wa();
        }
        wg_barrier();
        {
            f32x4 acc[8];
#pragma unroll
            for (int d = 0; d < 8; ++d) acc[d] = f32x4{0.f, 0.f, 0.f, 0.f};
#pragma unroll
            for (int tau = 0; tau < 2; ++tau) {
                const u32x4 of = *(const u32x4*)(lds + L_O + (n16 >> 2) * 128 + (32 * tau + 8 * g) * 2);
#pragma unroll
                for (int d = 0; d < 8; ++d) acc[d] = mfma(wa[tau][d], of, acc[d]);
            }
            publish_cols<64>(rs, WS_SP, acc, oh * 2 + okh, 16 * ons, lane, T);
        }
        asm volatile("" :: "v"(tch[0]), "v"(tch[1]), "v"(tch[2]), "v"(tch[3]));
    }
}

extern "C" void kernel_launch(void* const* d_in, const int* in_sizes, int n_in,
                              void* d_out, int out_size, void* d_ws, size_t ws_size,
                              hipStream_t stream) {
    Params P;
    P.x = d_in[0]; P.k = d_in[1]; P.v = d_in[2]; P.wq = d_in[3]; P.wo = d_in[4];
    P.w1 = d_in[6]; P.b1 = d_in[7]; P.w2 = d_in[8]; P.b2 = d_in[9];
    P.out = d_out; P.ws = d_ws;
    fused_decode_kernel<<<256, 320, 0, stream>>>(P);
}
```
